```python
import torch

def _softmax(x, axis=-1):
    return torch.softmax(x, dim=axis)

def _gelu(x):
    return torch.nn.functional.gelu(x, approximate='tanh')

def _silu(x):
    return torch.nn.functional.silu(x)

def _sigmoid(x):
    return torch.sigmoid(x)

def _layernorm(x, axis=-1, eps=1e-05):
    m = x.mean(dim=axis, keepdim=True)
    v = x.var(dim=axis, keepdim=True, unbiased=False)
    return (x - m) / torch.sqrt(v + eps)

def _scatter_add(tgt, idx, src, axis=0):
    return tgt.clone().index_add_(axis, idx, src)

def _segment_sum(src, seg, n):
    out = torch.zeros((n,) + src.shape[1:], dtype=src.dtype)
    return out.index_add_(0, seg, src)

def _pad(x, pw):
    flat = tuple((v for pair in reversed(pw) for v in pair))
    return torch.nn.functional.pad(x, flat)
kernel_name = 'adaLN_cond__sliding_local_dilated__glu_gated_glu__prompt'

def setup_inputs():

    _w = lambda a: (lambda t: t.to(torch.bfloat16) if t.is_floating_point() else t)(torch.as_tensor(a))

    def _A_setup():
        B, S, D = (1, 2048, 512)
        return {'x': _w((lambda *s: torch.randn(*s, dtype=torch.bfloat16) * 0.05)(B, S, D)), 'wqkv': _w((lambda *s: torch.randn(*s, dtype=torch.bfloat16) * 0.05)(D, 3 * D)), 'wo': _w((lambda *s: torch.randn(*s, dtype=torch.bfloat16) * 0.05)(D, D))}
    _A_in = _A_setup()

    def _B_setup():
        B, S, D, F = (1, 2048, 512, 1376)
        return {'x': _w((lambda *s: torch.randn(*s, dtype=torch.bfloat16) * 0.05)(B, S, D)), 'w_gate': _w((lambda *s: torch.randn(*s, dtype=torch.bfloat16) * 0.05)(D, F)), 'w_up': _w((lambda *s: torch.randn(*s, dtype=torch.bfloat16) * 0.05)(D, F)), 'w_down': _w((lambda *s: torch.randn(*s, dtype=torch.bfloat16) * 0.05)(F, D))}
    _B_in = _B_setup()
    return {'A_x': _A_in['x'], 'A_wqkv': _A_in['wqkv'], 'A_wo': _A_in['wo'], 'B_x': _B_in['x'], 'B_w_gate': _B_in['w_gate'], 'B_w_up': _B_in['w_up'], 'B_w_down': _B_in['w_down'], 'c': _w((lambda *s: torch.randn(*s, dtype=torch.bfloat16) * 0.1)(1, 512) * 8.0), 'w_mod': _w((lambda *s: torch.randn(*s, dtype=torch.bfloat16) * 0.1)(512, 6 * 512) * 512 ** (-0.5) * 8.0)}

def reference(A_x, A_wqkv, A_wo, B_x, B_w_gate, B_w_up, B_w_down, c, w_mod):

    def _A_ref(x, wqkv, wo):
        B, S, D = x.shape
        H = 32
        Dh = D // H
        W, dil = (8, 2)
        qkv = (lambda x, *d: x.permute(*d))((x @ wqkv).reshape(B, S, 3, H, Dh), 2, 0, 3, 1, 4)
        q, k, v = (qkv[0], qkv[1], qkv[2])
        scale = Dh ** (-0.5)
        pos = torch.arange(S)
        off = torch.arange(W) * dil
        idx = pos[:, None] - off[None, :]
        mask = idx >= 0
        idx_c = torch.where(mask, idx, 0).reshape(-1)
        kg = k[:, :, idx_c, :].reshape(B, H, S, W, Dh)
        vg = v[:, :, idx_c, :].reshape(B, H, S, W, Dh)
        logits = torch.einsum('bhsd,bhswd->bhsw', q, kg) * scale
        logits = torch.where(mask[None, None, :, :], logits, -1000000000.0)
        attn = _softmax(logits, axis=-1)
        o = torch.einsum('bhsw,bhswd->bhsd', attn, vg)
        return (lambda x, *d: x.permute(*d))(o, 0, 2, 1, 3).reshape(B, S, D) @ wo

    def _B_ref(x, w_gate, w_up, w_down):
        g = torch.einsum('bsd,df->bsf', x, w_gate)
        u = torch.einsum('bsd,df->bsf', x, w_up)
        h = _sigmoid(g) * u
        return torch.einsum('bsf,fd->bsd', h, w_down)
    x = A_x
    mod = (_silu(c) @ w_mod).reshape(c.shape[0], 6, 1, -1)
    sa, ca, ga, sb, cb, gb = (mod[:, 0].clone(), mod[:, 1].clone(), mod[:, 2].clone(), mod[:, 3].clone(), mod[:, 4].clone(), mod[:, 5].clone())
    h = _layernorm(x, axis=-1) * (1 + ca) + sa
    x = x + ga * _A_ref(h, A_wqkv, A_wo)
    h = _layernorm(x, axis=-1) * (1 + cb) + sb
    x = x + gb * _B_ref(h, B_w_gate, B_w_up, B_w_down)
    return x
```

```cpp
#include <hip/hip_runtime.h>
#include <hip/hip_bf16.h>
typedef unsigned short u16;
__device__ inline float bf2f(u16 v){ return __uint_as_float(((unsigned)v)<<16);}
__device__ inline u16 f2bf(float f){ unsigned u=__float_as_uint(f); u += 0x7FFF + ((u>>16)&1); return (u16)(u>>16);}
__device__ inline float rb(float f){ return bf2f(f2bf(f)); }
constexpr int S=2048,D=512,F=1376;
__global__ void k_mod(const u16* c,const u16* wm,float* mod){
  int n=blockIdx.x*256+threadIdx.x; float a=0;
  for(int k=0;k<512;k++){ float cv=bf2f(c[k]); float s=rb(cv/(1.f+__expf(-cv))); a+=s*bf2f(wm[k*3072+n]); }
  mod[n]=rb(a);
}
__global__ void k_ln(const u16* x,const float* mod,int so,int co,u16* h){
  int r=blockIdx.x; __shared__ float red[2][256]; int t=threadIdx.x;
  float v0=bf2f(x[r*D+t]), v1=bf2f(x[r*D+t+256]);
  red[0][t]=v0+v1; __syncthreads();
  for(int s=128;s>0;s>>=1){ if(t<s) red[0][t]+=red[0][t+s]; __syncthreads(); }
  float m=red[0][0]/D; float d0=v0-m,d1=v1-m; red[1][t]=d0*d0+d1*d1; __syncthreads();
  for(int s=128;s>0;s>>=1){ if(t<s) red[1][t]+=red[1][t+s]; __syncthreads(); }
  float rs=rsqrtf(red[1][0]/D+1e-5f);
  h[r*D+t]=f2bf(d0*rs*(1.f+mod[co+t])+mod[so+t]);
  h[r*D+t+256]=f2bf(d1*rs*(1.f+mod[co+t+256])+mod[so+t+256]);
}
__global__ void k_gemm(const u16* A,const u16* B,float* C,int M,int N,int K){
  int n=blockIdx.x*64+(threadIdx.x&63), m=blockIdx.y*4+(threadIdx.x>>6); if(n>=N) return; float a=0;
  for(int k=0;k<K;k++) a+=bf2f(A[m*K+k])*bf2f(B[k*N+n]);
  C[m*N+n]=rb(a);
}
__global__ void k_attn(const float* qkv,u16* o){
  int i=blockIdx.x*256+threadIdx.x; int s=i/32,h=i%32; float l[8]; float mx=-1e30f;
  for(int w=0;w<8;w++){ int p=s-2*w; float a=0; if(p>=0){ for(int d=0;d<16;d++) a+=qkv[s*1536+h*16+d]*qkv[p*1536+512+h*16+d]; a=a*0.25f;} else a=-1e9f; l[w]=a; mx=fmaxf(mx,a);}
  float sum=0; for(int w=0;w<8;w++){ l[w]=__expf(l[w]-mx); sum+=l[w]; }
  for(int d=0;d<16;d++){ float a=0; for(int w=0;w<8;w++){ int p=s-2*w; if(p<0)p=0; a+=l[w]/sum*qkv[p*1536+1024+h*16+d]; } o[s*512+h*16+d]=f2bf(a);}
}
__global__ void k_res(const u16* x,const float* y,const float* mod,int go,u16* out){
  int i=blockIdx.x*256+threadIdx.x; out[i]=f2bf(bf2f(x[i])+rb(mod[go+(i%512)]*y[i]));
}
__global__ void k_glu(const float* g,const float* u,u16* hh){
  int i=blockIdx.x*256+threadIdx.x; hh[i]=f2bf(rb(1.f/(1.f+__expf(-g[i])))*u[i]);
}
extern "C" void kernel_launch(void* const* d_in, const int* in_sizes, int n_in, void* d_out, int out_size, void* d_ws, size_t ws_size, hipStream_t stream){
  const u16* x=(const u16*)d_in[0]; const u16* wqkv=(const u16*)d_in[1]; const u16* wo=(const u16*)d_in[2];
  const u16* wg=(const u16*)d_in[4]; const u16* wu=(const u16*)d_in[5]; const u16* wd=(const u16*)d_in[6]; const u16* c=(const u16*)d_in[7]; const u16* wm=(const u16*)d_in[8];
  char* ws=(char*)d_ws; float* mod=(float*)ws; u16* h=(u16*)(ws+(1<<20)); float* qkv=(float*)(ws+(4<<20)); u16* o=(u16*)(ws+(20<<20)); float* y=(float*)(ws+(24<<20)); u16* x1=(u16*)(ws+(30<<20));
  float* g=(float*)(ws+(40<<20)); float* u=(float*)(ws+(60<<20)); u16* hh=(u16*)(ws+(80<<20));
  k_mod<<<12,256,0,stream>>>(c,wm,mod);
  k_ln<<<S,256,0,stream>>>(x,mod,0,512,h);
  k_gemm<<<dim3(24,S/4),256,0,stream>>>(h,wqkv,qkv,S,1536,512);
  k_attn<<<S*32/256,256,0,stream>>>(qkv,o);
  k_gemm<<<dim3(8,S/4),256,0,stream>>>(o,wo,y,S,512,512);
  k_res<<<S*D/256,256,0,stream>>>(x,y,mod,1024,x1);
  k_ln<<<S,256,0,stream>>>(x1,mod,1536,2048,h);
  k_gemm<<<dim3(22,S/4),256,0,stream>>>(h,wg,g,S,F,512);
  k_gemm<<<dim3(22,S/4),256,0,stream>>>(h,wu,u,S,F,512);
  k_glu<<<S*F/256,256,0,stream>>>(g,u,hh);
  k_gemm<<<dim3(8,S/4),256,0,stream>>>(hh,wd,y,S,512,F);
  k_res<<<S*D/256,256,0,stream>>>(x1,y,mod,2560,(u16*)d_out);
}
```

```cpp
#include <hip/hip_runtime.h>
#include <hip/hip_bf16.h>
typedef unsigned short u16;
__device__ inline float bf2f(u16 v){ return __uint_as_float(((unsigned)v)<<16);}
__device__ inline u16 f2bf(float f){ unsigned u=__float_as_uint(f); u += 0x7FFF + ((u>>16)&1); return (u16)(u>>16);}
__device__ inline float rb(float f){ return bf2f(f2bf(f)); }
constexpr int S=2048,D=512,F=1376;
__global__ void k_mod(const u16* c,const u16* wm,float* mod){
  int n=blockIdx.x*256+threadIdx.x; float a=0;
  for(int k=0;k<512;k++){ float cv=bf2f(c[k]); float s=rb(cv/(1.f+__expf(-cv))); a+=s*bf2f(wm[k*3072+n]); }
  mod[n]=rb(a);
}
__global__ void k_ln(const u16* x,const float* mod,int so,int co,u16* h){
  int r=blockIdx.x; __shared__ float red[2][256]; int t=threadIdx.x;
  float v0=bf2f(x[r*D+t]), v1=bf2f(x[r*D+t+256]);
  red[0][t]=v0+v1; __syncthreads();
  for(int s=128;s>0;s>>=1){ if(t<s) red[0][t]+=red[0][t+s]; __syncthreads(); }
  float m=red[0][0]/D; float d0=v0-m,d1=v1-m; red[1][t]=d0*d0+d1*d1; __syncthreads();
  for(int s=128;s>0;s>>=1){ if(t<s) red[1][t]+=red[1][t+s]; __syncthreads(); }
  float rs=rsqrtf(red[1][0]/D+1e-5f);
  h[r*D+t]=f2bf(d0*rs*(1.f+mod[co+t])+mod[so+t]);
  h[r*D+t+256]=f2bf(d1*rs*(1.f+mod[co+t+256])+mod[so+t+256]);
}
__global__ void k_gemm(const u16* A,const u16* B,float* C,int M,int N,int K){
  int n=blockIdx.x*64+(threadIdx.x&63), m=blockIdx.y*4+(threadIdx.x>>6); if(n>=N) return; float a=0;
  for(int k=0;k<K;k++) a+=bf2f(A[m*K+k])*bf2f(B[k*N+n]);
  C[m*N+n]=rb(a);
}
__global__ void k_attn(const float* qkv,u16* o){
  int i=blockIdx.x*256+threadIdx.x; int s=i/32,h=i%32; float l[8]; float mx=-1e30f;
  for(int w=0;w<8;w++){ int p=s-2*w; float a=0; if(p>=0){ for(int d=0;d<16;d++) a+=qkv[s*1536+h*16+d]*qkv[p*1536+512+h*16+d]; a=a*0.25f;} else a=-1e9f; l[w]=a; mx=fmaxf(mx,a);}
  float sum=0; for(int w=0;w<8;w++){ l[w]=__expf(l[w]-mx); sum+=l[w]; }
  for(int d=0;d<16;d++){ float a=0; for(int w=0;w<8;w++){ int p=s-2*w; if(p<0)p=0; a+=l[w]/sum*qkv[p*1536+1024+h*16+d]; } o[s*512+h*16+d]=f2bf(a);}
}
__global__ void k_res(const u16* x,const float* y,const float* mod,int go,u16* out){
  int i=blockIdx.x*256+threadIdx.x; out[i]=f2bf(bf2f(x[i])+rb(mod[go+(i%512)]*y[i]));
}
__global__ void k_glu(const float* g,const float* u,u16* hh){
  int i=blockIdx.x*256+threadIdx.x; hh[i]=f2bf(rb(1.f/(1.f+__expf(-g[i])))*u[i]);
}

typedef unsigned v4u __attribute__((ext_vector_type(4)));
__device__ inline unsigned long long rt(){ unsigned long long t; asm volatile("s_memrealtime %0\n s_waitcnt lgkmcnt(0)":"=s"(t)); return t; }
__device__ inline unsigned long long ct(){ unsigned long long t; asm volatile("s_memtime %0\n s_waitcnt lgkmcnt(0)":"=s"(t)); return t; }
__global__ void k_report(const unsigned long long* v, int n, int mode, int scale){
  unsigned long long mn=~0ull,mx=0,sum=0;
  for(int i=0;i<n;i++){ unsigned long long a=v[i]; mn=a<mn?a:mn; mx=a>mx?a:mx; sum+=a; }
  unsigned long long val = mode==0? sum/n : mode==1? mx : mx-mn;
  unsigned long long ticks = val*scale/10; if(ticks>2000000) ticks=2000000;
  unsigned long long t0=rt(); while(rt()-t0<ticks) __builtin_amdgcn_s_sleep(8);
}
template<int MODE>
__global__ void __launch_bounds__(256,1) k_p(const char* data, unsigned long long* out){
  __shared__ char sm[160*1024];
  int b=blockIdx.x, lane=threadIdx.x&63, w=__builtin_amdgcn_readfirstlane(threadIdx.x>>6);
  unsigned long long res=0;
  if(MODE==1){
    unsigned long long r0=rt(), c0=ct();
    asm volatile(".rept 4\n v_mfma_f32_16x16x32_bf16 a[0:3], v[10:13], v[14:17], a[0:3]\n .endr":::"a0","a1","a2","a3","a255","v255");
    for(int i=0;i<400;i++) asm volatile(".rept 4\n v_mfma_f32_16x16x32_bf16 a[0:3], v[10:13], v[14:17], a[0:3]\n .endr":::"a0","a1","a2","a3");
    unsigned long long r1=rt(), c1=ct();
    res=(c1-c0)*1000/(r1-r0);
  } else if(MODE==2){ res=rt(); asm volatile("":::"a255","v255"); }
  else if(MODE==4||MODE==5){
    int j = MODE==4 ? (b>>3)&7 : b;
    const char* p = data + (size_t)j*192*1024 + w*1024 + lane*16;
    unsigned long long c0=ct();
#pragma unroll
    for(int i=0;i<48;i++){ unsigned dst=(w*40+(i%40))*1024; asm volatile("s_mov_b32 m0, %1\n s_nop 0\n global_load_lds_dwordx4 %0, off"::"v"(p+i*4096),"s"(dst):"memory"); }
    asm volatile("s_waitcnt vmcnt(0)");
    res=ct()-c0;
  } else if(MODE==6){
    unsigned long long c0=ct();
    asm volatile(".rept 4096\n v_add3_u32 v10, v11, v12, v13\n .endr":::"v10");
    res=ct()-c0;
  }
  if(lane==0) out[b*4+w]=res;
  if(data==nullptr) sm[threadIdx.x]=1, out[0]=sm[5];
}
__global__ void __launch_bounds__(256,1) k_karg(unsigned long long* out){
  unsigned long long t0,t1,p; auto ka=__builtin_amdgcn_kernarg_segment_ptr();
  asm volatile("s_memtime %0\n s_waitcnt lgkmcnt(0)\n s_load_dwordx2 %2, %3, 0\n s_waitcnt lgkmcnt(0)\n s_memtime %1\n s_waitcnt lgkmcnt(0)":"=&s"(t0),"=&s"(t1),"=&s"(p):"s"(ka));
  if((threadIdx.x&63)==0) ((unsigned long long*)p)[blockIdx.x*4+(threadIdx.x>>6)]=t1-t0;
}
extern "C" void kernel_launch(void* const* d_in, const int* in_sizes, int n_in, void* d_out, int out_size, void* d_ws, size_t ws_size, hipStream_t stream){
  const u16* x=(const u16*)d_in[0]; const u16* wqkv=(const u16*)d_in[1]; const u16* wo=(const u16*)d_in[2];
  const u16* wg=(const u16*)d_in[4]; const u16* wu=(const u16*)d_in[5]; const u16* wd=(const u16*)d_in[6]; const u16* c=(const u16*)d_in[7]; const u16* wm=(const u16*)d_in[8];
  char* ws=(char*)d_ws; float* mod=(float*)ws; u16* h=(u16*)(ws+(1<<20)); float* qkv=(float*)(ws+(4<<20)); u16* o=(u16*)(ws+(20<<20)); float* y=(float*)(ws+(24<<20)); u16* x1=(u16*)(ws+(30<<20));
  float* g=(float*)(ws+(40<<20)); float* u=(float*)(ws+(60<<20)); u16* hh=(u16*)(ws+(80<<20));
  unsigned long long* pv=(unsigned long long*)((char*)d_ws+(200<<20));
  k_p<2><<<256,256,0,stream>>>((const char*)d_ws,pv); k_report<<<1,1,0,stream>>>(pv,1024,2,10000);
  k_mod<<<12,256,0,stream>>>(c,wm,mod);
  k_ln<<<S,256,0,stream>>>(x,mod,0,512,h);
  k_gemm<<<dim3(24,S/4),256,0,stream>>>(h,wqkv,qkv,S,1536,512);
  k_attn<<<S*32/256,256,0,stream>>>(qkv,o);
  k_gemm<<<dim3(8,S/4),256,0,stream>>>(o,wo,y,S,512,512);
  k_res<<<S*D/256,256,0,stream>>>(x,y,mod,1024,x1);
  k_ln<<<S,256,0,stream>>>(x1,mod,1536,2048,h);
  k_gemm<<<dim3(22,S/4),256,0,stream>>>(h,wg,g,S,F,512);
  k_gemm<<<dim3(22,S/4),256,0,stream>>>(h,wu,u,S,F,512);
  k_glu<<<S*F/256,256,0,stream>>>(g,u,hh);
  k_gemm<<<dim3(8,S/4),256,0,stream>>>(hh,wd,y,S,512,F);
  k_res<<<S*D/256,256,0,stream>>>(x1,y,mod,2560,(u16*)d_out);
}
```

```cpp
#include <hip/hip_runtime.h>
#include <hip/hip_bf16.h>
typedef unsigned short u16;
__device__ inline float bf2f(u16 v){ return __uint_as_float(((unsigned)v)<<16);}
__device__ inline u16 f2bf(float f){ unsigned u=__float_as_uint(f); u += 0x7FFF + ((u>>16)&1); return (u16)(u>>16);}
__device__ inline float rb(float f){ return bf2f(f2bf(f)); }
constexpr int S=2048,D=512,F=1376;
__global__ void k_mod(const u16* c,const u16* wm,float* mod){
  int n=blockIdx.x*256+threadIdx.x; float a=0;
  for(int k=0;k<512;k++){ float cv=bf2f(c[k]); float s=rb(cv/(1.f+__expf(-cv))); a+=s*bf2f(wm[k*3072+n]); }
  mod[n]=rb(a);
}
__global__ void k_ln(const u16* x,const float* mod,int so,int co,u16* h){
  int r=blockIdx.x; __shared__ float red[2][256]; int t=threadIdx.x;
  float v0=bf2f(x[r*D+t]), v1=bf2f(x[r*D+t+256]);
  red[0][t]=v0+v1; __syncthreads();
  for(int s=128;s>0;s>>=1){ if(t<s) red[0][t]+=red[0][t+s]; __syncthreads(); }
  float m=red[0][0]/D; float d0=v0-m,d1=v1-m; red[1][t]=d0*d0+d1*d1; __syncthreads();
  for(int s=128;s>0;s>>=1){ if(t<s) red[1][t]+=red[1][t+s]; __syncthreads(); }
  float rs=rsqrtf(red[1][0]/D+1e-5f);
  h[r*D+t]=f2bf(d0*rs*(1.f+mod[co+t])+mod[so+t]);
  h[r*D+t+256]=f2bf(d1*rs*(1.f+mod[co+t+256])+mod[so+t+256]);
}
__global__ void k_gemm(const u16* A,const u16* B,float* C,int M,int N,int K){
  int n=blockIdx.x*64+(threadIdx.x&63), m=blockIdx.y*4+(threadIdx.x>>6); if(n>=N) return; float a=0;
  for(int k=0;k<K;k++) a+=bf2f(A[m*K+k])*bf2f(B[k*N+n]);
  C[m*N+n]=rb(a);
}
__global__ void k_attn(const float* qkv,u16* o){
  int i=blockIdx.x*256+threadIdx.x; int s=i/32,h=i%32; float l[8]; float mx=-1e30f;
  for(int w=0;w<8;w++){ int p=s-2*w; float a=0; if(p>=0){ for(int d=0;d<16;d++) a+=qkv[s*1536+h*16+d]*qkv[p*1536+512+h*16+d]; a=a*0.25f;} else a=-1e9f; l[w]=a; mx=fmaxf(mx,a);}
  float sum=0; for(int w=0;w<8;w++){ l[w]=__expf(l[w]-mx); sum+=l[w]; }
  for(int d=0;d<16;d++){ float a=0; for(int w=0;w<8;w++){ int p=s-2*w; if(p<0)p=0; a+=l[w]/sum*qkv[p*1536+1024+h*16+d]; } o[s*512+h*16+d]=f2bf(a);}
}
__global__ void k_res(const u16* x,const float* y,const float* mod,int go,u16* out){
  int i=blockIdx.x*256+threadIdx.x; out[i]=f2bf(bf2f(x[i])+rb(mod[go+(i%512)]*y[i]));
}
__global__ void k_glu(const float* g,const float* u,u16* hh){
  int i=blockIdx.x*256+threadIdx.x; hh[i]=f2bf(rb(1.f/(1.f+__expf(-g[i])))*u[i]);
}

typedef unsigned v4u __attribute__((ext_vector_type(4)));
__device__ inline unsigned long long rt(){ unsigned long long t; asm volatile("s_memrealtime %0\n s_waitcnt lgkmcnt(0)":"=s"(t)); return t; }
__device__ inline unsigned long long ct(){ unsigned long long t; asm volatile("s_memtime %0\n s_waitcnt lgkmcnt(0)":"=s"(t)); return t; }
__global__ void k_report(const unsigned long long* v, int n, int mode, int scale){
  unsigned long long mn=~0ull,mx=0,sum=0;
  for(int i=0;i<n;i++){ unsigned long long a=v[i]; mn=a<mn?a:mn; mx=a>mx?a:mx; sum+=a; }
  unsigned long long val = mode==0? sum/n : mode==1? mx : mx-mn;
  unsigned long long ticks = val*scale/10; if(ticks>2000000) ticks=2000000;
  unsigned long long t0=rt(); while(rt()-t0<ticks) __builtin_amdgcn_s_sleep(8);
}
template<int MODE>
__global__ void __launch_bounds__(256,1) k_p(const char* data, unsigned long long* out){
  __shared__ char sm[160*1024];
  int b=blockIdx.x, lane=threadIdx.x&63, w=__builtin_amdgcn_readfirstlane(threadIdx.x>>6);
  unsigned long long res=0;
  if(MODE==1){
    unsigned long long r0=rt(), c0=ct();
    asm volatile(".rept 4\n v_mfma_f32_16x16x32_bf16 a[0:3], v[10:13], v[14:17], a[0:3]\n .endr":::"a0","a1","a2","a3","a255","v255");
    for(int i=0;i<400;i++) asm volatile(".rept 4\n v_mfma_f32_16x16x32_bf16 a[0:3], v[10:13], v[14:17], a[0:3]\n .endr":::"a0","a1","a2","a3");
    unsigned long long r1=rt(), c1=ct();
    res=(c1-c0)*1000/(r1-r0);
  } else if(MODE==2){ res=rt(); asm volatile("":::"a255","v255"); }
  else if(MODE==4||MODE==5){
    int j = MODE==4 ? (b>>3)&7 : b;
    const char* p = data + (size_t)j*192*1024 + w*1024 + lane*16;
    unsigned long long c0=ct();
#pragma unroll
    for(int i=0;i<48;i++){ unsigned dst=(w*40+(i%40))*1024; asm volatile("s_mov_b32 m0, %1\n s_nop 0\n global_load_lds_dwordx4 %0, off"::"v"(p+i*4096),"s"(dst):"memory"); }
    asm volatile("s_waitcnt vmcnt(0)");
    res=ct()-c0;
  } else if(MODE==7||MODE==8||MODE==9){
    const char* p = data + (size_t)(b*4+w)*1024 + lane*16; v4u v;
    unsigned long long c0=ct();
    asm volatile("global_load_dwordx4 %0, %1, off\n s_waitcnt vmcnt(0)":"=v"(v):"v"(p):"memory");
    unsigned long long c1=ct();
    asm volatile("global_load_dwordx4 %0, %1, off\n s_waitcnt vmcnt(0)":"=v"(v):"v"(p+(MODE==9?(64<<20):(1<<20))):"memory");
    unsigned long long c2=ct();
    res = MODE==7? c1-c0 : c2-c1;
  } else if(MODE==10){
    int j=(b>>3)&7; int rot=((b>>6)*12 + (b&7)*3/2)%48;
    const char* p = data + (size_t)j*192*1024 + w*1024 + lane*16;
    unsigned long long c0=ct();
#pragma unroll
    for(int i=0;i<48;i++){ unsigned dst=(w*40+(i%40))*1024; int ii=i+rot; ii = ii>=48? ii-48: ii; asm volatile("s_mov_b32 m0, %1\n s_nop 0\n global_load_lds_dwordx4 %0, off"::"v"(p+ii*4096),"s"(dst):"memory"); }
    asm volatile("s_waitcnt vmcnt(0)");
    res=ct()-c0;
  } else if(MODE==6){
    unsigned long long c0=ct();
    asm volatile(".rept 4096\n v_add3_u32 v10, v11, v12, v13\n .endr":::"v10");
    res=ct()-c0;
  }
  if(lane==0) out[b*4+w]=res;
  if(data==nullptr) sm[threadIdx.x]=1, out[0]=sm[5];
}
__global__ void __launch_bounds__(256,1) k_karg(unsigned long long* out){
  unsigned long long t0,t1,p; auto ka=__builtin_amdgcn_kernarg_segment_ptr();
  asm volatile("s_memtime %0\n s_waitcnt lgkmcnt(0)\n s_load_dwordx2 %2, %3, 0\n s_waitcnt lgkmcnt(0)\n s_memtime %1\n s_waitcnt lgkmcnt(0)":"=&s"(t0),"=&s"(t1),"=&s"(p):"s"(ka));
  if((threadIdx.x&63)==0) ((unsigned long long*)p)[blockIdx.x*4+(threadIdx.x>>6)]=t1-t0;
}
extern "C" void kernel_launch(void* const* d_in, const int* in_sizes, int n_in, void* d_out, int out_size, void* d_ws, size_t ws_size, hipStream_t stream){
  const u16* x=(const u16*)d_in[0]; const u16* wqkv=(const u16*)d_in[1]; const u16* wo=(const u16*)d_in[2];
  const u16* wg=(const u16*)d_in[4]; const u16* wu=(const u16*)d_in[5]; const u16* wd=(const u16*)d_in[6]; const u16* c=(const u16*)d_in[7]; const u16* wm=(const u16*)d_in[8];
  char* ws=(char*)d_ws; float* mod=(float*)ws; u16* h=(u16*)(ws+(1<<20)); float* qkv=(float*)(ws+(4<<20)); u16* o=(u16*)(ws+(20<<20)); float* y=(float*)(ws+(24<<20)); u16* x1=(u16*)(ws+(30<<20));
  float* g=(float*)(ws+(40<<20)); float* u=(float*)(ws+(60<<20)); u16* hh=(u16*)(ws+(80<<20));
  unsigned long long* pv=(unsigned long long*)((char*)d_ws+(200<<20));
  k_p<2><<<256,256,0,stream>>>((const char*)d_ws,pv); k_report<<<1,1,0,stream>>>(pv,1024,2,0);
  k_mod<<<12,256,0,stream>>>(c,wm,mod);
  k_ln<<<S,256,0,stream>>>(x,mod,0,512,h);
  k_gemm<<<dim3(24,S/4),256,0,stream>>>(h,wqkv,qkv,S,1536,512);
  k_attn<<<S*32/256,256,0,stream>>>(qkv,o);
  k_gemm<<<dim3(8,S/4),256,0,stream>>>(o,wo,y,S,512,512);
  k_res<<<S*D/256,256,0,stream>>>(x,y,mod,1024,x1);
  k_ln<<<S,256,0,stream>>>(x1,mod,1536,2048,h);
  k_gemm<<<dim3(22,S/4),256,0,stream>>>(h,wg,g,S,F,512);
  k_gemm<<<dim3(22,S/4),256,0,stream>>>(h,wu,u,S,F,512);
  k_glu<<<S*F/256,256,0,stream>>>(g,u,hh);
  k_gemm<<<dim3(8,S/4),256,0,stream>>>(hh,wd,y,S,512,F);
  k_res<<<S*D/256,256,0,stream>>>(x1,y,mod,2560,(u16*)d_out);
}
```

```cpp
#include <hip/hip_runtime.h>

extern "C" __device__ __attribute__((used, aligned(256), visibility("hidden"))) const unsigned role_table[49152] = {
65536,0,2147483648u,0,0,25165824,65536,25296896,6,0,1048576,1048576,0,0,9437184,0,0,9437184,22020096,25165824,5242880,5242880,0,69632,655360,655360,557056,622592,524288,524288,557056,589824,589824,4048155338u,0,0,0,0,0,0,0,0,0,0,0,0,0,0,
65536,0,2147483648u,192,192,25559040,65536,25690112,6,0,1049600,1081344,0,0,9439232,0,0,9527296,22110208,25182208,5243392,5308416,16384,71680,657408,657408,557056,622592,622592,524288,557056,589824,589824,4048155338u,2147483648u,1,0,0,0,0,0,0,0,0,0,0,0,0,
65536,0,2147483648u,0,16384,22020096,2048,22028288,4,0,1050624,1114112,0,0,9797632,0,0,9617408,22200320,25198592,5308416,5373952,32768,73728,659456,659456,557056,622592,524288,526336,558080,589824,589824,4048155338u,2147483648u,2,1065353216,1065353216,0,0,0,0,0,0,0,0,0,0,
65536,0,2147483648u,1048576,1064960,22355968,2048,22372352,4,0,1051648,1146880,0,0,9799680,0,0,9707520,22290432,25214976,5308928,5439488,49152,75776,661504,661504,557056,622592,622592,526336,558080,589824,589824,4048155338u,2147483648u,3,1065353216,0,0,0,0,0,0,0,0,0,0,0,
77824,512,2147483648u,44032,44032,25167872,65536,25298944,6,0,1064960,1179648,0,0,9445376,2147483648u,2147483648u,9797632,22020096,28114944,5373952,5242880,65536,69632,663552,663552,558080,623616,524416,540672,557056,590848,589824,4048155338u,2147483648u,0,0,0,0,0,0,0,0,0,0,0,0,0,
77824,512,2147483648u,44224,44224,25561088,65536,25692160,6,0,1065984,1212416,0,0,9447424,2147483648u,2147483648u,9887744,22110208,28131328,5374464,5308416,81920,71680,665600,665600,558080,623616,623104,524288,557056,590848,589824,4048155338u,2147483648u,1,0,0,0,0,0,0,0,0,0,0,0,0,
77824,512,2147483648u,32768,49152,22044672,2048,22061056,4,0,1067008,1245184,0,0,9805824,2147483648u,2147483648u,9977856,22200320,28147712,5439488,5373952,98304,73728,667648,667648,558080,623616,524416,542720,558080,590848,589824,4048155338u,2147483648u,2,1065353216,1065353216,0,0,0,0,0,0,0,0,0,0,
77824,512,2147483648u,1081344,1097728,22040576,2048,22056960,4,0,1068032,1277952,0,0,9807872,2147483648u,2147483648u,10067968,22290432,28164096,5440000,5439488,114688,75776,669696,669696,558080,623616,623104,526336,558080,590848,589824,4048155338u,2147483648u,3,1065353216,0,0,0,0,0,0,0,0,0,0,0,
90112,0,2147483648u,352256,352256,25182208,65536,25313280,6,0,2097152,2097152,0,0,12320768,0,0,12320768,22020096,25165824,6291456,6291456,524288,69632,720896,720896,565248,630784,525312,524288,565248,598016,598016,0,2147483648u,0,0,0,0,0,0,0,0,0,0,0,0,0,
90112,0,2147483648u,352448,352448,25575424,65536,25706496,6,0,2098176,2129920,0,0,12322816,0,0,12410880,22110208,25182208,6291968,6356992,540672,71680,722944,722944,565248,630784,630784,524288,565248,598016,598016,0,2147483648u,1,0,0,0,0,0,0,0,0,0,0,0,0,
90112,0,2147483648u,262144,278528,22257664,2048,22274048,4,0,2099200,2162688,0,0,12681216,0,0,12500992,22200320,25198592,6356992,6422528,557056,73728,724992,724992,565248,630784,525312,526336,566272,598016,598016,0,2147483648u,2,1065353216,1065353216,0,0,0,0,0,0,0,0,0,0,
90112,0,2147483648u,1310720,1327104,22286336,2048,22310912,4,0,2100224,2195456,0,0,12683264,0,0,12591104,22290432,25214976,6357504,6488064,573440,75776,727040,727040,565248,630784,630784,526336,566272,598016,598016,0,2147483648u,3,1065353216,0,0,0,0,0,0,0,0,0,0,0,
102400,512,2147483648u,396288,396288,25184256,65536,25315328,6,0,2113536,2228224,0,0,12328960,2147483648u,2147483648u,12681216,22020096,28114944,6422528,6291456,589824,69632,729088,729088,566272,631808,525440,540672,565248,599040,598016,0,2147483648u,0,0,0,0,0,0,0,0,0,0,0,0,0,
102400,512,2147483648u,396480,396480,25577472,65536,25708544,6,0,2114560,2260992,0,0,12331008,2147483648u,2147483648u,12771328,22110208,28131328,6423040,6356992,606208,71680,731136,731136,566272,631808,631296,524288,565248,599040,598016,0,2147483648u,1,0,0,0,0,0,0,0,0,0,0,0,0,
102400,512,2147483648u,294912,311296,22290432,2048,22298624,4,0,2115584,2293760,0,0,12689408,2147483648u,2147483648u,12861440,22200320,28147712,6488064,6422528,622592,73728,733184,733184,566272,631808,525440,542720,566272,599040,598016,0,2147483648u,2,1065353216,1065353216,0,0,0,0,0,0,0,0,0,0,
102400,512,2147483648u,1343488,1359872,22327296,2048,22343680,4,0,2116608,2326528,0,0,12691456,2147483648u,2147483648u,12951552,22290432,28164096,6488576,6488064,638976,75776,735232,735232,566272,631808,631296,526336,566272,599040,598016,0,2147483648u,3,1065353216,0,0,0,0,0,0,0,0,0,0,0,
65792,0,2147483648u,704512,704512,25198592,65536,25329664,6,0,3145728,3145728,0,0,15204352,0,0,15204352,22020096,25165824,7340032,7340032,1048576,69632,786432,786432,573440,638976,526336,524288,573440,606208,606208,0,2147483648u,0,0,0,0,0,0,0,0,0,0,0,0,0,
65792,0,2147483648u,704704,704704,25591808,65536,25722880,6,0,3146752,3178496,0,0,15206400,0,0,15294464,22110208,25182208,7340544,7405568,1064960,71680,788480,788480,573440,638976,638976,524288,573440,606208,606208,0,2147483648u,1,0,0,0,0,0,0,0,0,0,0,0,0,
65792,0,2147483648u,524288,540672,22138880,2048,22155264,4,0,3147776,3211264,0,0,15564800,0,0,15384576,22200320,25198592,7405568,7471104,1081344,73728,790528,790528,573440,638976,526336,526336,574464,606208,606208,0,2147483648u,2,1065353216,1065353216,0,0,0,0,0,0,0,0,0,0,
65792,0,2147483648u,1376256,1392640,22360064,2048,22376448,4,2147483648u,3148800,3244032,0,0,15566848,0,0,15474688,22290432,25214976,7406080,7536640,1097728,75776,792576,792576,573440,638976,638976,526336,574464,606208,606208,0,2147483648u,3,1065353216,0,0,0,0,0,0,0,0,0,0,0,
78080,512,2147483648u,748544,748544,25200640,65536,25331712,6,0,3162112,3276800,0,0,15212544,2147483648u,2147483648u,15564800,22020096,28114944,7471104,7340032,1114112,69632,794624,794624,574464,640000,526464,540672,573440,607232,606208,0,2147483648u,0,0,0,0,0,0,0,0,0,0,0,0,0,
78080,512,2147483648u,748736,748736,25593856,65536,25724928,6,0,3163136,3309568,0,0,15214592,2147483648u,2147483648u,15654912,22110208,28131328,7471616,7405568,1130496,71680,796672,796672,574464,640000,639488,524288,573440,607232,606208,0,2147483648u,1,0,0,0,0,0,0,0,0,0,0,0,0,
78080,512,2147483648u,557056,573440,22171648,2048,22188032,4,0,3164160,3342336,0,0,15572992,2147483648u,2147483648u,15745024,22200320,28147712,7536640,7471104,1146880,73728,798720,798720,574464,640000,526464,542720,574464,607232,606208,0,2147483648u,2,1065353216,1065353216,0,0,0,0,0,0,0,0,0,0,
78080,512,2147483648u,1376256,1392640,22360064,2048,22376448,4,2147483648u,3165184,3375104,0,0,15575040,2147483648u,2147483648u,15835136,22290432,28164096,7537152,7536640,1163264,75776,800768,800768,574464,640000,639488,526336,574464,607232,606208,0,2147483648u,3,1065353216,0,0,0,0,0,0,0,0,0,0,0,
90368,0,2147483648u,1056768,1056768,25214976,65536,25346048,6,0,4194304,4194304,0,0,18087936,0,0,18087936,22020096,25165824,8388608,8388608,1572864,69632,851968,851968,581632,647168,527360,524288,581632,614400,614400,0,2147483648u,0,0,0,0,0,0,0,0,0,0,0,0,0,
90368,0,2147483648u,1056960,1056960,25608192,65536,25739264,6,0,4195328,4227072,0,0,18089984,0,0,18178048,22110208,25182208,8389120,8454144,1589248,71680,854016,854016,581632,647168,647168,524288,581632,614400,614400,0,2147483648u,1,0,0,0,0,0,0,0,0,0,0,0,0,
90368,0,2147483648u,786432,802816,22069248,2048,22085632,4,0,4196352,4259840,0,0,18448384,0,0,18268160,22200320,25198592,8454144,8519680,1605632,73728,856064,856064,581632,647168,527360,526336,582656,614400,614400,0,2147483648u,2,1065353216,1065353216,0,0,0,0,0,0,0,0,0,0,
90368,0,2147483648u,1376256,1392640,22360064,2048,22376448,4,2147483648u,4197376,4292608,0,0,18450432,0,0,18358272,22290432,25214976,8454656,8585216,1622016,75776,858112,858112,581632,647168,647168,526336,582656,614400,614400,0,2147483648u,3,1065353216,0,0,0,0,0,0,0,0,0,0,0,
102656,512,2147483648u,1100800,1100800,25217024,65536,25348096,6,0,4210688,4325376,0,0,18096128,2147483648u,2147483648u,18448384,22020096,28114944,8519680,8388608,1638400,69632,860160,860160,582656,648192,527488,540672,581632,615424,614400,0,2147483648u,0,0,0,0,0,0,0,0,0,0,0,0,0,
102656,512,2147483648u,1100992,1100992,25610240,65536,25741312,6,0,4211712,4358144,0,0,18098176,2147483648u,2147483648u,18538496,22110208,28131328,8520192,8454144,1654784,71680,862208,862208,582656,648192,647680,524288,581632,615424,614400,0,2147483648u,1,0,0,0,0,0,0,0,0,0,0,0,0,
102656,512,2147483648u,819200,835584,22102016,2048,22126592,4,0,4212736,4390912,0,0,18456576,2147483648u,2147483648u,18628608,22200320,28147712,8585216,8519680,1671168,73728,864256,864256,582656,648192,527488,542720,582656,615424,614400,0,2147483648u,2,1065353216,1065353216,0,0,0,0,0,0,0,0,0,0,
102656,512,2147483648u,1376256,1392640,22360064,2048,22376448,4,2147483648u,4213760,4423680,0,0,18458624,2147483648u,2147483648u,18718720,22290432,28164096,8585728,8585216,1687552,75776,866304,866304,582656,648192,647680,526336,582656,615424,614400,0,2147483648u,3,1065353216,0,0,0,0,0,0,0,0,0,0,0,
66048,128,2147483648u,384,384,25952256,65536,26083328,6,0,1052672,1048576,0,128,9527296,0,0,9437184,22380544,25559040,5251072,5242880,128,69888,655360,655616,557184,622592,528384,524288,557056,589952,589824,4048155338u,2147483648u,4,0,0,0,0,0,0,0,0,0,0,0,0,
66048,128,2147483648u,576,576,26345472,65536,26476544,6,0,1053696,1081344,0,128,9529344,0,0,9527296,22470656,25575424,5251584,5308416,16512,71936,657408,657664,557184,622592,622720,528384,557056,589952,589824,4048155338u,2147483648u,5,0,0,0,0,0,0,0,0,0,0,0,0,
66048,128,2147483648u,128,16512,22380544,2048,22388736,4,0,1054720,1114112,0,128,9887744,0,0,9617408,22560768,25591808,5316608,5373952,32896,73984,659456,659712,557184,622592,528384,526336,558080,589952,589824,4048155338u,2147483648u,6,1065353216,1065353216,0,0,0,0,0,0,0,0,0,0,
66048,128,2147483648u,1048704,1065088,22716416,2048,22732800,4,0,1055744,1146880,0,128,9889792,0,0,9707520,22650880,25608192,5317120,5439488,49280,76032,661504,661760,557184,622592,622720,530432,558080,589952,589824,4048155338u,2147483648u,7,1065353216,0,0,0,0,0,0,0,0,0,0,0,
78336,640,2147483648u,44416,44416,25954304,65536,26085376,6,0,1069056,1179648,0,128,9535488,2147483648u,2147483648u,9797632,22380544,28442624,5382144,5242880,65664,69888,663552,663808,558208,623616,528512,540672,557056,590976,589824,4048155338u,2147483648u,4,0,0,0,0,0,0,0,0,0,0,0,0,
78336,640,2147483648u,44608,44608,26347520,65536,26478592,6,0,1070080,1212416,0,128,9537536,2147483648u,2147483648u,9887744,22470656,28459008,5382656,5308416,82048,71936,665600,665856,558208,623616,623232,528384,557056,590976,589824,4048155338u,2147483648u,5,0,0,0,0,0,0,0,0,0,0,0,0,
78336,640,2147483648u,32896,49280,22405120,2048,22421504,4,0,1071104,1245184,0,128,9895936,2147483648u,2147483648u,9977856,22560768,28475392,5447680,5373952,98432,73984,667648,667904,558208,623616,528512,542720,558080,590976,589824,4048155338u,2147483648u,6,1065353216,1065353216,0,0,0,0,0,0,0,0,0,0,
78336,640,2147483648u,1081472,1097856,22401024,2048,22417408,4,0,1072128,1277952,0,128,9897984,2147483648u,2147483648u,10067968,22650880,28491776,5448192,5439488,114816,76032,669696,669952,558208,623616,623232,530432,558080,590976,589824,4048155338u,2147483648u,7,1065353216,0,0,0,0,0,0,0,0,0,0,0,
90624,128,2147483648u,352640,352640,25968640,65536,26099712,6,0,2101248,2097152,0,128,12410880,0,0,12320768,22380544,25559040,6299648,6291456,524416,69888,720896,721152,565376,630784,529408,524288,565248,598144,598016,0,2147483648u,4,0,0,0,0,0,0,0,0,0,0,0,0,
90624,128,2147483648u,352832,352832,26361856,65536,26492928,6,0,2102272,2129920,0,128,12412928,0,0,12410880,22470656,25575424,6300160,6356992,540800,71936,722944,723200,565376,630784,630912,528384,565248,598144,598016,0,2147483648u,5,0,0,0,0,0,0,0,0,0,0,0,0,
90624,128,2147483648u,262272,278656,22618112,2048,22634496,4,0,2103296,2162688,0,128,12771328,0,0,12500992,22560768,25591808,6365184,6422528,557184,73984,724992,725248,565376,630784,529408,526336,566272,598144,598016,0,2147483648u,6,1065353216,1065353216,0,0,0,0,0,0,0,0,0,0,
90624,128,2147483648u,1310848,1327232,22646784,2048,22671360,4,0,2104320,2195456,0,128,12773376,0,0,12591104,22650880,25608192,6365696,6488064,573568,76032,727040,727296,565376,630784,630912,530432,566272,598144,598016,0,2147483648u,7,1065353216,0,0,0,0,0,0,0,0,0,0,0,
102912,640,2147483648u,396672,396672,25970688,65536,26101760,6,0,2117632,2228224,0,128,12419072,2147483648u,2147483648u,12681216,22380544,28442624,6430720,6291456,589952,69888,729088,729344,566400,631808,529536,540672,565248,599168,598016,0,2147483648u,4,0,0,0,0,0,0,0,0,0,0,0,0,
102912,640,2147483648u,396864,396864,26363904,65536,26494976,6,0,2118656,2260992,0,128,12421120,2147483648u,2147483648u,12771328,22470656,28459008,6431232,6356992,606336,71936,731136,731392,566400,631808,631424,528384,565248,599168,598016,0,2147483648u,5,0,0,0,0,0,0,0,0,0,0,0,0,
102912,640,2147483648u,295040,311424,22650880,2048,22659072,4,0,2119680,2293760,0,128,12779520,2147483648u,2147483648u,12861440,22560768,28475392,6496256,6422528,622720,73984,733184,733440,566400,631808,529536,542720,566272,599168,598016,0,2147483648u,6,1065353216,1065353216,0,0,0,0,0,0,0,0,0,0,
102912,640,2147483648u,1343616,1360000,22687744,2048,22704128,4,0,2120704,2326528,0,128,12781568,2147483648u,2147483648u,12951552,22650880,28491776,6496768,6488064,639104,76032,735232,735488,566400,631808,631424,530432,566272,599168,598016,0,2147483648u,7,1065353216,0,0,0,0,0,0,0,0,0,0,0,
66304,128,2147483648u,704896,704896,25985024,65536,26116096,6,0,3149824,3145728,0,128,15294464,0,0,15204352,22380544,25559040,7348224,7340032,1048704,69888,786432,786688,573568,638976,530432,524288,573440,606336,606208,0,2147483648u,4,0,0,0,0,0,0,0,0,0,0,0,0,
66304,128,2147483648u,705088,705088,26378240,65536,26509312,6,0,3150848,3178496,0,128,15296512,0,0,15294464,22470656,25575424,7348736,7405568,1065088,71936,788480,788736,573568,638976,639104,528384,573440,606336,606208,0,2147483648u,5,0,0,0,0,0,0,0,0,0,0,0,0,
66304,128,2147483648u,524416,540800,22499328,2048,22515712,4,0,3151872,3211264,0,128,15654912,0,0,15384576,22560768,25591808,7413760,7471104,1081472,73984,790528,790784,573568,638976,530432,526336,574464,606336,606208,0,2147483648u,6,1065353216,1065353216,0,0,0,0,0,0,0,0,0,0,
66304,128,2147483648u,1376384,1392768,22720512,2048,22736896,4,2147483648u,3152896,3244032,0,128,15656960,0,0,15474688,22650880,25608192,7414272,7536640,1097856,76032,792576,792832,573568,638976,639104,530432,574464,606336,606208,0,2147483648u,7,1065353216,0,0,0,0,0,0,0,0,0,0,0,
78592,640,2147483648u,748928,748928,25987072,65536,26118144,6,0,3166208,3276800,0,128,15302656,2147483648u,2147483648u,15564800,22380544,28442624,7479296,7340032,1114240,69888,794624,794880,574592,640000,530560,540672,573440,607360,606208,0,2147483648u,4,0,0,0,0,0,0,0,0,0,0,0,0,
78592,640,2147483648u,749120,749120,26380288,65536,26511360,6,0,3167232,3309568,0,128,15304704,2147483648u,2147483648u,15654912,22470656,28459008,7479808,7405568,1130624,71936,796672,796928,574592,640000,639616,528384,573440,607360,606208,0,2147483648u,5,0,0,0,0,0,0,0,0,0,0,0,0,
78592,640,2147483648u,557184,573568,22532096,2048,22548480,4,0,3168256,3342336,0,128,15663104,2147483648u,2147483648u,15745024,22560768,28475392,7544832,7471104,1147008,73984,798720,798976,574592,640000,530560,542720,574464,607360,606208,0,2147483648u,6,1065353216,1065353216,0,0,0,0,0,0,0,0,0,0,
78592,640,2147483648u,1376384,1392768,22720512,2048,22736896,4,2147483648u,3169280,3375104,0,128,15665152,2147483648u,2147483648u,15835136,22650880,28491776,7545344,7536640,1163392,76032,800768,801024,574592,640000,639616,530432,574464,607360,606208,0,2147483648u,7,1065353216,0,0,0,0,0,0,0,0,0,0,0,
90880,128,2147483648u,1057152,1057152,26001408,65536,26132480,6,0,4198400,4194304,0,128,18178048,0,0,18087936,22380544,25559040,8396800,8388608,1572992,69888,851968,852224,581760,647168,531456,524288,581632,614528,614400,0,2147483648u,4,0,0,0,0,0,0,0,0,0,0,0,0,
90880,128,2147483648u,1057344,1057344,26394624,65536,26525696,6,0,4199424,4227072,0,128,18180096,0,0,18178048,22470656,25575424,8397312,8454144,1589376,71936,854016,854272,581760,647168,647296,528384,581632,614528,614400,0,2147483648u,5,0,0,0,0,0,0,0,0,0,0,0,0,
90880,128,2147483648u,786560,802944,22429696,2048,22446080,4,0,4200448,4259840,0,128,18538496,0,0,18268160,22560768,25591808,8462336,8519680,1605760,73984,856064,856320,581760,647168,531456,526336,582656,614528,614400,0,2147483648u,6,1065353216,1065353216,0,0,0,0,0,0,0,0,0,0,
90880,128,2147483648u,1376384,1392768,22720512,2048,22736896,4,2147483648u,4201472,4292608,0,128,18540544,0,0,18358272,22650880,25608192,8462848,8585216,1622144,76032,858112,858368,581760,647168,647296,530432,582656,614528,614400,0,2147483648u,7,1065353216,0,0,0,0,0,0,0,0,0,0,0,
103168,640,2147483648u,1101184,1101184,26003456,65536,26134528,6,0,4214784,4325376,0,128,18186240,2147483648u,2147483648u,18448384,22380544,28442624,8527872,8388608,1638528,69888,860160,860416,582784,648192,531584,540672,581632,615552,614400,0,2147483648u,4,0,0,0,0,0,0,0,0,0,0,0,0,
103168,640,2147483648u,1101376,1101376,26396672,65536,26527744,6,0,4215808,4358144,0,128,18188288,2147483648u,2147483648u,18538496,22470656,28459008,8528384,8454144,1654912,71936,862208,862464,582784,648192,647808,528384,581632,615552,614400,0,2147483648u,5,0,0,0,0,0,0,0,0,0,0,0,0,
103168,640,2147483648u,819328,835712,22462464,2048,22487040,4,0,4216832,4390912,0,128,18546688,2147483648u,2147483648u,18628608,22560768,28475392,8593408,8519680,1671296,73984,864256,864512,582784,648192,531584,542720,582656,615552,614400,0,2147483648u,6,1065353216,1065353216,0,0,0,0,0,0,0,0,0,0,
103168,640,2147483648u,1376384,1392768,22720512,2048,22736896,4,2147483648u,4217856,4423680,0,128,18548736,2147483648u,2147483648u,18718720,22650880,28491776,8593920,8585216,1687680,76032,866304,866560,582784,648192,647808,530432,582656,615552,614400,0,2147483648u,7,1065353216,0,0,0,0,0,0,0,0,0,0,0,
66560,256,2147483648u,768,768,26738688,65536,26869760,6,0,1056768,1048576,0,256,9617408,0,0,9437184,22740992,25952256,5259264,5242880,256,70144,655360,655872,557312,622592,532480,528384,557056,590080,589824,4048155338u,2147483648u,8,0,0,0,0,0,0,0,0,0,0,0,0,
66560,256,2147483648u,960,960,27131904,65536,27262976,6,0,1057792,1081344,0,256,9619456,0,0,9527296,22831104,25968640,5259776,5308416,16640,72192,657408,657920,557312,622592,622848,532480,557056,590080,589824,4048155338u,2147483648u,9,0,0,0,0,0,0,0,0,0,0,0,0,
66560,256,2147483648u,256,16640,22740992,2048,22749184,4,0,1058816,1114112,0,256,9977856,0,0,9617408,22921216,25985024,5324800,5373952,33024,74240,659456,659968,557312,622592,532480,530432,558080,590080,589824,4048155338u,2147483648u,10,1065353216,1065353216,0,0,0,0,0,0,0,0,0,0,
66560,256,2147483648u,1048832,1065216,23076864,2048,23093248,4,0,1059840,1146880,0,256,9979904,0,0,9707520,23011328,26001408,5325312,5439488,49408,76288,661504,662016,557312,622592,622848,534528,558080,590080,589824,4048155338u,2147483648u,11,1065353216,0,0,0,0,0,0,0,0,0,0,0,
78848,768,2147483648u,44800,44800,26740736,65536,26871808,6,0,1073152,1179648,0,256,9625600,2147483648u,2147483648u,9797632,22740992,28770304,5390336,5242880,65792,70144,663552,664064,558336,623616,532608,544768,557056,591104,589824,4048155338u,2147483648u,8,0,0,0,0,0,0,0,0,0,0,0,0,
78848,768,2147483648u,44992,44992,27133952,65536,27265024,6,0,1074176,1212416,0,256,9627648,2147483648u,2147483648u,9887744,22831104,28786688,5390848,5308416,82176,72192,665600,666112,558336,623616,623360,532480,557056,591104,589824,4048155338u,2147483648u,9,0,0,0,0,0,0,0,0,0,0,0,0,
78848,768,2147483648u,33024,49408,22765568,2048,22781952,4,0,1075200,1245184,0,256,9986048,2147483648u,2147483648u,9977856,22921216,28803072,5455872,5373952,98560,74240,667648,668160,558336,623616,532608,546816,558080,591104,589824,4048155338u,2147483648u,10,1065353216,1065353216,0,0,0,0,0,0,0,0,0,0,
78848,768,2147483648u,1081600,1097984,22761472,2048,22777856,4,0,1076224,1277952,0,256,9988096,2147483648u,2147483648u,10067968,23011328,28819456,5456384,5439488,114944,76288,669696,670208,558336,623616,623360,534528,558080,591104,589824,4048155338u,2147483648u,11,1065353216,0,0,0,0,0,0,0,0,0,0,0,
91136,256,2147483648u,353024,353024,26755072,65536,26886144,6,0,2105344,2097152,0,256,12500992,0,0,12320768,22740992,25952256,6307840,6291456,524544,70144,720896,721408,565504,630784,533504,528384,565248,598272,598016,0,2147483648u,8,0,0,0,0,0,0,0,0,0,0,0,0,
91136,256,2147483648u,353216,353216,27148288,65536,27279360,6,0,2106368,2129920,0,256,12503040,0,0,12410880,22831104,25968640,6308352,6356992,540928,72192,722944,723456,565504,630784,631040,532480,565248,598272,598016,0,2147483648u,9,0,0,0,0,0,0,0,0,0,0,0,0,
91136,256,2147483648u,262400,278784,22978560,2048,22994944,4,0,2107392,2162688,0,256,12861440,0,0,12500992,22921216,25985024,6373376,6422528,557312,74240,724992,725504,565504,630784,533504,530432,566272,598272,598016,0,2147483648u,10,1065353216,1065353216,0,0,0,0,0,0,0,0,0,0,
91136,256,2147483648u,1310976,1327360,23007232,2048,23031808,4,0,2108416,2195456,0,256,12863488,0,0,12591104,23011328,26001408,6373888,6488064,573696,76288,727040,727552,565504,630784,631040,534528,566272,598272,598016,0,2147483648u,11,1065353216,0,0,0,0,0,0,0,0,0,0,0,
103424,768,2147483648u,397056,397056,26757120,65536,26888192,6,0,2121728,2228224,0,256,12509184,2147483648u,2147483648u,12681216,22740992,28770304,6438912,6291456,590080,70144,729088,729600,566528,631808,533632,544768,565248,599296,598016,0,2147483648u,8,0,0,0,0,0,0,0,0,0,0,0,0,
103424,768,2147483648u,397248,397248,27150336,65536,27281408,6,0,2122752,2260992,0,256,12511232,2147483648u,2147483648u,12771328,22831104,28786688,6439424,6356992,606464,72192,731136,731648,566528,631808,631552,532480,565248,599296,598016,0,2147483648u,9,0,0,0,0,0,0,0,0,0,0,0,0,
103424,768,2147483648u,295168,311552,23011328,2048,23019520,4,0,2123776,2293760,0,256,12869632,2147483648u,2147483648u,12861440,22921216,28803072,6504448,6422528,622848,74240,733184,733696,566528,631808,533632,546816,566272,599296,598016,0,2147483648u,10,1065353216,1065353216,0,0,0,0,0,0,0,0,0,0,
103424,768,2147483648u,1343744,1360128,23048192,2048,23064576,4,0,2124800,2326528,0,256,12871680,2147483648u,2147483648u,12951552,23011328,28819456,6504960,6488064,639232,76288,735232,735744,566528,631808,631552,534528,566272,599296,598016,0,2147483648u,11,1065353216,0,0,0,0,0,0,0,0,0,0,0,
66816,256,2147483648u,705280,705280,26771456,65536,26902528,6,0,3153920,3145728,0,256,15384576,0,0,15204352,22740992,25952256,7356416,7340032,1048832,70144,786432,786944,573696,638976,534528,528384,573440,606464,606208,0,2147483648u,8,0,0,0,0,0,0,0,0,0,0,0,0,
66816,256,2147483648u,705472,705472,27164672,65536,27295744,6,0,3154944,3178496,0,256,15386624,0,0,15294464,22831104,25968640,7356928,7405568,1065216,72192,788480,788992,573696,638976,639232,532480,573440,606464,606208,0,2147483648u,9,0,0,0,0,0,0,0,0,0,0,0,0,
66816,256,2147483648u,524544,540928,22859776,2048,22876160,4,0,3155968,3211264,0,256,15745024,0,0,15384576,22921216,25985024,7421952,7471104,1081600,74240,790528,791040,573696,638976,534528,530432,574464,606464,606208,0,2147483648u,10,1065353216,1065353216,0,0,0,0,0,0,0,0,0,0,
66816,256,2147483648u,1376512,1392896,23080960,2048,23097344,4,2147483648u,3156992,3244032,0,256,15747072,0,0,15474688,23011328,26001408,7422464,7536640,1097984,76288,792576,793088,573696,638976,639232,534528,574464,606464,606208,0,2147483648u,11,1065353216,0,0,0,0,0,0,0,0,0,0,0,
79104,768,2147483648u,749312,749312,26773504,65536,26904576,6,0,3170304,3276800,0,256,15392768,2147483648u,2147483648u,15564800,22740992,28770304,7487488,7340032,1114368,70144,794624,795136,574720,640000,534656,544768,573440,607488,606208,0,2147483648u,8,0,0,0,0,0,0,0,0,0,0,0,0,
79104,768,2147483648u,749504,749504,27166720,65536,27297792,6,0,3171328,3309568,0,256,15394816,2147483648u,2147483648u,15654912,22831104,28786688,7488000,7405568,1130752,72192,796672,797184,574720,640000,639744,532480,573440,607488,606208,0,2147483648u,9,0,0,0,0,0,0,0,0,0,0,0,0,
79104,768,2147483648u,557312,573696,22892544,2048,22908928,4,0,3172352,3342336,0,256,15753216,2147483648u,2147483648u,15745024,22921216,28803072,7553024,7471104,1147136,74240,798720,799232,574720,640000,534656,546816,574464,607488,606208,0,2147483648u,10,1065353216,1065353216,0,0,0,0,0,0,0,0,0,0,
79104,768,2147483648u,1376512,1392896,23080960,2048,23097344,4,2147483648u,3173376,3375104,0,256,15755264,2147483648u,2147483648u,15835136,23011328,28819456,7553536,7536640,1163520,76288,800768,801280,574720,640000,639744,534528,574464,607488,606208,0,2147483648u,11,1065353216,0,0,0,0,0,0,0,0,0,0,0,
91392,256,2147483648u,1057536,1057536,26787840,65536,26918912,6,0,4202496,4194304,0,256,18268160,0,0,18087936,22740992,25952256,8404992,8388608,1573120,70144,851968,852480,581888,647168,535552,528384,581632,614656,614400,0,2147483648u,8,0,0,0,0,0,0,0,0,0,0,0,0,
91392,256,2147483648u,1057728,1057728,27181056,65536,27312128,6,0,4203520,4227072,0,256,18270208,0,0,18178048,22831104,25968640,8405504,8454144,1589504,72192,854016,854528,581888,647168,647424,532480,581632,614656,614400,0,2147483648u,9,0,0,0,0,0,0,0,0,0,0,0,0,
91392,256,2147483648u,786688,803072,22790144,2048,22806528,4,0,4204544,4259840,0,256,18628608,0,0,18268160,22921216,25985024,8470528,8519680,1605888,74240,856064,856576,581888,647168,535552,530432,582656,614656,614400,0,2147483648u,10,1065353216,1065353216,0,0,0,0,0,0,0,0,0,0,
91392,256,2147483648u,1376512,1392896,23080960,2048,23097344,4,2147483648u,4205568,4292608,0,256,18630656,0,0,18358272,23011328,26001408,8471040,8585216,1622272,76288,858112,858624,581888,647168,647424,534528,582656,614656,614400,0,2147483648u,11,1065353216,0,0,0,0,0,0,0,0,0,0,0,
103680,768,2147483648u,1101568,1101568,26789888,65536,26920960,6,0,4218880,4325376,0,256,18276352,2147483648u,2147483648u,18448384,22740992,28770304,8536064,8388608,1638656,70144,860160,860672,582912,648192,535680,544768,581632,615680,614400,0,2147483648u,8,0,0,0,0,0,0,0,0,0,0,0,0,
103680,768,2147483648u,1101760,1101760,27183104,65536,27314176,6,0,4219904,4358144,0,256,18278400,2147483648u,2147483648u,18538496,22831104,28786688,8536576,8454144,1655040,72192,862208,862720,582912,648192,647936,532480,581632,615680,614400,0,2147483648u,9,0,0,0,0,0,0,0,0,0,0,0,0,
103680,768,2147483648u,819456,835840,22822912,2048,22847488,4,0,4220928,4390912,0,256,18636800,2147483648u,2147483648u,18628608,22921216,28803072,8601600,8519680,1671424,74240,864256,864768,582912,648192,535680,546816,582656,615680,614400,0,2147483648u,10,1065353216,1065353216,0,0,0,0,0,0,0,0,0,0,
103680,768,2147483648u,1376512,1392896,23080960,2048,23097344,4,2147483648u,4221952,4423680,0,256,18638848,2147483648u,2147483648u,18718720,23011328,28819456,8602112,8585216,1687808,76288,866304,866816,582912,648192,647936,534528,582656,615680,614400,0,2147483648u,11,1065353216,0,0,0,0,0,0,0,0,0,0,0,
67072,384,2147483648u,1152,1152,27525120,65536,27656192,5,0,1060864,1048576,0,384,9707520,0,0,9437184,23101440,26345472,5267456,5242880,384,70400,655360,656128,557440,622592,536576,528384,557056,590208,589824,4048155338u,2147483648u,12,0,0,0,0,0,0,0,0,0,0,0,0,
67072,384,2147483648u,1312,1312,27852800,65536,27983872,5,0,1061888,1081344,0,384,9709568,0,0,9527296,23191552,26361856,5267968,5308416,16768,72448,657408,658176,557440,622592,622976,536576,557056,590208,589824,4048155338u,2147483648u,13,0,0,0,0,0,0,0,0,0,0,0,0,
67072,384,2147483648u,384,16768,23101440,2048,23109632,4,0,1062912,1114112,0,384,10067968,0,0,9617408,23281664,26378240,5332992,5373952,33152,74496,659456,660224,557440,622592,536576,530432,558080,590208,589824,4048155338u,2147483648u,14,1065353216,1065353216,0,0,0,0,0,0,0,0,0,0,
67072,384,2147483648u,1048960,1065344,23437312,2048,23453696,4,0,1063936,1146880,0,384,10070016,0,0,9707520,23371776,26394624,5333504,5439488,49536,76544,661504,662272,557440,622592,622976,538624,558080,590208,589824,4048155338u,2147483648u,15,1065353216,0,0,0,0,0,0,0,0,0,0,0,
79360,896,2147483648u,45184,45184,27527168,65536,27658240,5,0,1077248,1179648,0,384,9715712,2147483648u,2147483648u,9797632,23101440,29097984,5398528,5242880,65920,70400,663552,664320,558464,623616,536704,544768,557056,591232,589824,4048155338u,2147483648u,12,0,0,0,0,0,0,0,0,0,0,0,0,
79360,896,2147483648u,45344,45344,27854848,65536,27985920,5,0,1078272,1212416,0,384,9717760,2147483648u,2147483648u,9887744,23191552,29114368,5399040,5308416,82304,72448,665600,666368,558464,623616,623488,536576,557056,591232,589824,4048155338u,2147483648u,13,0,0,0,0,0,0,0,0,0,0,0,0,
79360,896,2147483648u,33152,49536,23126016,2048,23142400,4,0,1079296,1245184,0,384,10076160,2147483648u,2147483648u,9977856,23281664,29130752,5464064,5373952,98688,74496,667648,668416,558464,623616,536704,546816,558080,591232,589824,4048155338u,2147483648u,14,1065353216,1065353216,0,0,0,0,0,0,0,0,0,0,
79360,896,2147483648u,1081728,1098112,23121920,2048,23138304,4,0,1080320,1277952,0,384,10078208,2147483648u,2147483648u,10067968,23371776,29147136,5464576,5439488,115072,76544,669696,670464,558464,623616,623488,538624,558080,591232,589824,4048155338u,2147483648u,15,1065353216,0,0,0,0,0,0,0,0,0,0,0,
91648,384,2147483648u,353408,353408,27541504,65536,27672576,5,0,2109440,2097152,0,384,12591104,0,0,12320768,23101440,26345472,6316032,6291456,524672,70400,720896,721664,565632,630784,537600,528384,565248,598400,598016,0,2147483648u,12,0,0,0,0,0,0,0,0,0,0,0,0,
91648,384,2147483648u,353568,353568,27869184,65536,28000256,5,0,2110464,2129920,0,384,12593152,0,0,12410880,23191552,26361856,6316544,6356992,541056,72448,722944,723712,565632,630784,631168,536576,565248,598400,598016,0,2147483648u,13,0,0,0,0,0,0,0,0,0,0,0,0,
91648,384,2147483648u,262528,278912,23339008,2048,23355392,4,0,2111488,2162688,0,384,12951552,0,0,12500992,23281664,26378240,6381568,6422528,557440,74496,724992,725760,565632,630784,537600,530432,566272,598400,598016,0,2147483648u,14,1065353216,1065353216,0,0,0,0,0,0,0,0,0,0,
91648,384,2147483648u,1311104,1327488,23367680,2048,23392256,4,0,2112512,2195456,0,384,12953600,0,0,12591104,23371776,26394624,6382080,6488064,573824,76544,727040,727808,565632,630784,631168,538624,566272,598400,598016,0,2147483648u,15,1065353216,0,0,0,0,0,0,0,0,0,0,0,
103936,896,2147483648u,397440,397440,27543552,65536,27674624,5,0,2125824,2228224,0,384,12599296,2147483648u,2147483648u,12681216,23101440,29097984,6447104,6291456,590208,70400,729088,729856,566656,631808,537728,544768,565248,599424,598016,0,2147483648u,12,0,0,0,0,0,0,0,0,0,0,0,0,
103936,896,2147483648u,397600,397600,27871232,65536,28002304,5,0,2126848,2260992,0,384,12601344,2147483648u,2147483648u,12771328,23191552,29114368,6447616,6356992,606592,72448,731136,731904,566656,631808,631680,536576,565248,599424,598016,0,2147483648u,13,0,0,0,0,0,0,0,0,0,0,0,0,
103936,896,2147483648u,295296,311680,23371776,2048,23379968,4,0,2127872,2293760,0,384,12959744,2147483648u,2147483648u,12861440,23281664,29130752,6512640,6422528,622976,74496,733184,733952,566656,631808,537728,546816,566272,599424,598016,0,2147483648u,14,1065353216,1065353216,0,0,0,0,0,0,0,0,0,0,
103936,896,2147483648u,1343872,1360256,23408640,2048,23425024,4,0,2128896,2326528,0,384,12961792,2147483648u,2147483648u,12951552,23371776,29147136,6513152,6488064,639360,76544,735232,736000,566656,631808,631680,538624,566272,599424,598016,0,2147483648u,15,1065353216,0,0,0,0,0,0,0,0,0,0,0,
67328,384,2147483648u,705664,705664,27557888,65536,27688960,5,0,3158016,3145728,0,384,15474688,0,0,15204352,23101440,26345472,7364608,7340032,1048960,70400,786432,787200,573824,638976,538624,528384,573440,606592,606208,0,2147483648u,12,0,0,0,0,0,0,0,0,0,0,0,0,
67328,384,2147483648u,705824,705824,27885568,65536,28016640,5,0,3159040,3178496,0,384,15476736,0,0,15294464,23191552,26361856,7365120,7405568,1065344,72448,788480,789248,573824,638976,639360,536576,573440,606592,606208,0,2147483648u,13,0,0,0,0,0,0,0,0,0,0,0,0,
67328,384,2147483648u,524672,541056,23220224,2048,23236608,4,0,3160064,3211264,0,384,15835136,0,0,15384576,23281664,26378240,7430144,7471104,1081728,74496,790528,791296,573824,638976,538624,530432,574464,606592,606208,0,2147483648u,14,1065353216,1065353216,0,0,0,0,0,0,0,0,0,0,
67328,384,2147483648u,1376640,1393024,23441408,2048,23457792,4,2147483648u,3161088,3244032,0,384,15837184,0,0,15474688,23371776,26394624,7430656,7536640,1098112,76544,792576,793344,573824,638976,639360,538624,574464,606592,606208,0,2147483648u,15,1065353216,0,0,0,0,0,0,0,0,0,0,0,
79616,896,2147483648u,749696,749696,27559936,65536,27691008,5,0,3174400,3276800,0,384,15482880,2147483648u,2147483648u,15564800,23101440,29097984,7495680,7340032,1114496,70400,794624,795392,574848,640000,538752,544768,573440,607616,606208,0,2147483648u,12,0,0,0,0,0,0,0,0,0,0,0,0,
79616,896,2147483648u,749856,749856,27887616,65536,28018688,5,0,3175424,3309568,0,384,15484928,2147483648u,2147483648u,15654912,23191552,29114368,7496192,7405568,1130880,72448,796672,797440,574848,640000,639872,536576,573440,607616,606208,0,2147483648u,13,0,0,0,0,0,0,0,0,0,0,0,0,
79616,896,2147483648u,557440,573824,23252992,2048,23269376,4,0,3176448,3342336,0,384,15843328,2147483648u,2147483648u,15745024,23281664,29130752,7561216,7471104,1147264,74496,798720,799488,574848,640000,538752,546816,574464,607616,606208,0,2147483648u,14,1065353216,1065353216,0,0,0,0,0,0,0,0,0,0,
79616,896,2147483648u,1376640,1393024,23441408,2048,23457792,4,2147483648u,3177472,3375104,0,384,15845376,2147483648u,2147483648u,15835136,23371776,29147136,7561728,7536640,1163648,76544,800768,801536,574848,640000,639872,538624,574464,607616,606208,0,2147483648u,15,1065353216,0,0,0,0,0,0,0,0,0,0,0,
91904,384,2147483648u,1057920,1057920,27574272,65536,27705344,5,0,4206592,4194304,0,384,18358272,0,0,18087936,23101440,26345472,8413184,8388608,1573248,70400,851968,852736,582016,647168,539648,528384,581632,614784,614400,0,2147483648u,12,0,0,0,0,0,0,0,0,0,0,0,0,
91904,384,2147483648u,1058080,1058080,27901952,65536,28033024,5,0,4207616,4227072,0,384,18360320,0,0,18178048,23191552,26361856,8413696,8454144,1589632,72448,854016,854784,582016,647168,647552,536576,581632,614784,614400,0,2147483648u,13,0,0,0,0,0,0,0,0,0,0,0,0,
91904,384,2147483648u,786816,803200,23150592,2048,23166976,4,0,4208640,4259840,0,384,18718720,0,0,18268160,23281664,26378240,8478720,8519680,1606016,74496,856064,856832,582016,647168,539648,530432,582656,614784,614400,0,2147483648u,14,1065353216,1065353216,0,0,0,0,0,0,0,0,0,0,
91904,384,2147483648u,1376640,1393024,23441408,2048,23457792,4,2147483648u,4209664,4292608,0,384,18720768,0,0,18358272,23371776,26394624,8479232,8585216,1622400,76544,858112,858880,582016,647168,647552,538624,582656,614784,614400,0,2147483648u,15,1065353216,0,0,0,0,0,0,0,0,0,0,0,
104192,896,2147483648u,1101952,1101952,27576320,65536,27707392,5,0,4222976,4325376,0,384,18366464,2147483648u,2147483648u,18448384,23101440,29097984,8544256,8388608,1638784,70400,860160,860928,583040,648192,539776,544768,581632,615808,614400,0,2147483648u,12,0,0,0,0,0,0,0,0,0,0,0,0,
104192,896,2147483648u,1102112,1102112,27904000,65536,28035072,5,0,4224000,4358144,0,384,18368512,2147483648u,2147483648u,18538496,23191552,29114368,8544768,8454144,1655168,72448,862208,862976,583040,648192,648064,536576,581632,615808,614400,0,2147483648u,13,0,0,0,0,0,0,0,0,0,0,0,0,
104192,896,2147483648u,819584,835968,23183360,2048,23207936,4,0,4225024,4390912,0,384,18726912,2147483648u,2147483648u,18628608,23281664,29130752,8609792,8519680,1671552,74496,864256,865024,583040,648192,539776,546816,582656,615808,614400,0,2147483648u,14,1065353216,1065353216,0,0,0,0,0,0,0,0,0,0,
104192,896,2147483648u,1376640,1393024,23441408,2048,23457792,4,2147483648u,4226048,4423680,0,384,18728960,2147483648u,2147483648u,18718720,23371776,29147136,8610304,8585216,1687936,76544,866304,867072,583040,648192,648064,538624,582656,615808,614400,0,2147483648u,15,1065353216,0,0,0,0,0,0,0,0,0,0,0,
67584,0,2147483648u,1472,1472,28180480,65536,28311552,5,0,1179648,1048576,0,512,9441280,0,0,9437184,23461888,26738688,5275648,5242880,512,70656,655360,656384,557568,622592,540672,532480,557056,590336,589824,0,2147483648u,16,0,0,0,0,0,0,0,0,0,0,0,0,
67584,0,2147483648u,1632,1632,28508160,65536,28639232,5,0,1180672,1081344,0,512,9443328,0,0,9527296,23552000,26755072,5276160,5308416,16896,72704,657408,658432,557568,622592,623616,540672,557056,590336,589824,0,2147483648u,17,0,0,0,0,0,0,0,0,0,0,0,0,
67584,0,2147483648u,512,16896,23461888,2048,23470080,4,0,1181696,1114112,0,512,9801728,0,0,9617408,23642112,26771456,5341184,5373952,33280,74752,659456,660480,557568,622592,540672,534528,558080,590336,589824,0,2147483648u,18,1065353216,1065353216,0,0,0,0,0,0,0,0,0,0,
67584,0,2147483648u,1049088,1065472,23797760,2048,23814144,4,0,1182720,1146880,0,512,9803776,0,0,9707520,23732224,26787840,5341696,5439488,49664,76800,661504,662528,557568,622592,623616,542720,558080,590336,589824,0,2147483648u,19,1065353216,0,0,0,0,0,0,0,0,0,0,0,
79872,512,2147483648u,45504,45504,28182528,65536,28313600,5,0,1196032,1179648,0,512,9449472,2147483648u,2147483648u,9797632,23461888,29425664,5406720,5242880,66048,70656,663552,664576,558592,623616,540800,548864,557056,591360,589824,0,2147483648u,16,0,0,0,0,0,0,0,0,0,0,0,0,
79872,512,2147483648u,45664,45664,28510208,65536,28641280,5,0,1197056,1212416,0,512,9451520,2147483648u,2147483648u,9887744,23552000,29442048,5407232,5308416,82432,72704,665600,666624,558592,623616,624128,540672,557056,591360,589824,0,2147483648u,17,0,0,0,0,0,0,0,0,0,0,0,0,
79872,512,2147483648u,33280,49664,23486464,2048,23502848,4,0,1198080,1245184,0,512,9809920,2147483648u,2147483648u,9977856,23642112,29458432,5472256,5373952,98816,74752,667648,668672,558592,623616,540800,550912,558080,591360,589824,0,2147483648u,18,1065353216,1065353216,0,0,0,0,0,0,0,0,0,0,
79872,512,2147483648u,1081856,1098240,23482368,2048,23498752,4,0,1199104,1277952,0,512,9811968,2147483648u,2147483648u,10067968,23732224,29474816,5472768,5439488,115200,76800,669696,670720,558592,623616,624128,542720,558080,591360,589824,0,2147483648u,19,1065353216,0,0,0,0,0,0,0,0,0,0,0,
92160,0,2147483648u,353728,353728,28196864,65536,28327936,5,0,2228224,2097152,0,512,12324864,0,0,12320768,23461888,26738688,6324224,6291456,524800,70656,720896,721920,565760,630784,541696,532480,565248,598528,598016,0,2147483648u,16,0,0,0,0,0,0,0,0,0,0,0,0,
92160,0,2147483648u,353888,353888,28524544,65536,28655616,5,0,2229248,2129920,0,512,12326912,0,0,12410880,23552000,26755072,6324736,6356992,541184,72704,722944,723968,565760,630784,631808,540672,565248,598528,598016,0,2147483648u,17,0,0,0,0,0,0,0,0,0,0,0,0,
92160,0,2147483648u,262656,279040,23699456,2048,23715840,4,0,2230272,2162688,0,512,12685312,0,0,12500992,23642112,26771456,6389760,6422528,557568,74752,724992,726016,565760,630784,541696,534528,566272,598528,598016,0,2147483648u,18,1065353216,1065353216,0,0,0,0,0,0,0,0,0,0,
92160,0,2147483648u,1311232,1327616,23728128,2048,23752704,4,0,2231296,2195456,0,512,12687360,0,0,12591104,23732224,26787840,6390272,6488064,573952,76800,727040,728064,565760,630784,631808,542720,566272,598528,598016,0,2147483648u,19,1065353216,0,0,0,0,0,0,0,0,0,0,0,
104448,512,2147483648u,397760,397760,28198912,65536,28329984,5,0,2244608,2228224,0,512,12333056,2147483648u,2147483648u,12681216,23461888,29425664,6455296,6291456,590336,70656,729088,730112,566784,631808,541824,548864,565248,599552,598016,0,2147483648u,16,0,0,0,0,0,0,0,0,0,0,0,0,
104448,512,2147483648u,397920,397920,28526592,65536,28657664,5,0,2245632,2260992,0,512,12335104,2147483648u,2147483648u,12771328,23552000,29442048,6455808,6356992,606720,72704,731136,732160,566784,631808,632320,540672,565248,599552,598016,0,2147483648u,17,0,0,0,0,0,0,0,0,0,0,0,0,
104448,512,2147483648u,295424,311808,23732224,2048,23740416,4,0,2246656,2293760,0,512,12693504,2147483648u,2147483648u,12861440,23642112,29458432,6520832,6422528,623104,74752,733184,734208,566784,631808,541824,550912,566272,599552,598016,0,2147483648u,18,1065353216,1065353216,0,0,0,0,0,0,0,0,0,0,
104448,512,2147483648u,1344000,1360384,23769088,2048,23785472,4,0,2247680,2326528,0,512,12695552,2147483648u,2147483648u,12951552,23732224,29474816,6521344,6488064,639488,76800,735232,736256,566784,631808,632320,542720,566272,599552,598016,0,2147483648u,19,1065353216,0,0,0,0,0,0,0,0,0,0,0,
67840,0,2147483648u,705984,705984,28213248,65536,28344320,5,0,3276800,3145728,0,512,15208448,0,0,15204352,23461888,26738688,7372800,7340032,1049088,70656,786432,787456,573952,638976,542720,532480,573440,606720,606208,0,2147483648u,16,0,0,0,0,0,0,0,0,0,0,0,0,
67840,0,2147483648u,706144,706144,28540928,65536,28672000,5,0,3277824,3178496,0,512,15210496,0,0,15294464,23552000,26755072,7373312,7405568,1065472,72704,788480,789504,573952,638976,640000,540672,573440,606720,606208,0,2147483648u,17,0,0,0,0,0,0,0,0,0,0,0,0,
67840,0,2147483648u,524800,541184,23580672,2048,23597056,4,0,3278848,3211264,0,512,15568896,0,0,15384576,23642112,26771456,7438336,7471104,1081856,74752,790528,791552,573952,638976,542720,534528,574464,606720,606208,0,2147483648u,18,1065353216,1065353216,0,0,0,0,0,0,0,0,0,0,
67840,0,2147483648u,1376768,1393152,23801856,2048,23818240,4,2147483648u,3279872,3244032,0,512,15570944,0,0,15474688,23732224,26787840,7438848,7536640,1098240,76800,792576,793600,573952,638976,640000,542720,574464,606720,606208,0,2147483648u,19,1065353216,0,0,0,0,0,0,0,0,0,0,0,
80128,512,2147483648u,750016,750016,28215296,65536,28346368,5,0,3293184,3276800,0,512,15216640,2147483648u,2147483648u,15564800,23461888,29425664,7503872,7340032,1114624,70656,794624,795648,574976,640000,542848,548864,573440,607744,606208,0,2147483648u,16,0,0,0,0,0,0,0,0,0,0,0,0,
80128,512,2147483648u,750176,750176,28542976,65536,28674048,5,0,3294208,3309568,0,512,15218688,2147483648u,2147483648u,15654912,23552000,29442048,7504384,7405568,1131008,72704,796672,797696,574976,640000,640512,540672,573440,607744,606208,0,2147483648u,17,0,0,0,0,0,0,0,0,0,0,0,0,
80128,512,2147483648u,557568,573952,23613440,2048,23629824,4,0,3295232,3342336,0,512,15577088,2147483648u,2147483648u,15745024,23642112,29458432,7569408,7471104,1147392,74752,798720,799744,574976,640000,542848,550912,574464,607744,606208,0,2147483648u,18,1065353216,1065353216,0,0,0,0,0,0,0,0,0,0,
80128,512,2147483648u,1376768,1393152,23801856,2048,23818240,4,2147483648u,3296256,3375104,0,512,15579136,2147483648u,2147483648u,15835136,23732224,29474816,7569920,7536640,1163776,76800,800768,801792,574976,640000,640512,542720,574464,607744,606208,0,2147483648u,19,1065353216,0,0,0,0,0,0,0,0,0,0,0,
92416,0,2147483648u,1058240,1058240,28229632,65536,28360704,5,0,4325376,4194304,0,512,18092032,0,0,18087936,23461888,26738688,8421376,8388608,1573376,70656,851968,852992,582144,647168,543744,532480,581632,614912,614400,0,2147483648u,16,0,0,0,0,0,0,0,0,0,0,0,0,
92416,0,2147483648u,1058400,1058400,28557312,65536,28688384,5,0,4326400,4227072,0,512,18094080,0,0,18178048,23552000,26755072,8421888,8454144,1589760,72704,854016,855040,582144,647168,648192,540672,581632,614912,614400,0,2147483648u,17,0,0,0,0,0,0,0,0,0,0,0,0,
92416,0,2147483648u,786944,803328,23511040,2048,23527424,4,0,4327424,4259840,0,512,18452480,0,0,18268160,23642112,26771456,8486912,8519680,1606144,74752,856064,857088,582144,647168,543744,534528,582656,614912,614400,0,2147483648u,18,1065353216,1065353216,0,0,0,0,0,0,0,0,0,0,
92416,0,2147483648u,1376768,1393152,23801856,2048,23818240,4,2147483648u,4328448,4292608,0,512,18454528,0,0,18358272,23732224,26787840,8487424,8585216,1622528,76800,858112,859136,582144,647168,648192,542720,582656,614912,614400,0,2147483648u,19,1065353216,0,0,0,0,0,0,0,0,0,0,0,
104704,512,2147483648u,1102272,1102272,28231680,65536,28362752,5,0,4341760,4325376,0,512,18100224,2147483648u,2147483648u,18448384,23461888,29425664,8552448,8388608,1638912,70656,860160,861184,583168,648192,543872,548864,581632,615936,614400,0,2147483648u,16,0,0,0,0,0,0,0,0,0,0,0,0,
104704,512,2147483648u,1102432,1102432,28559360,65536,28690432,5,0,4342784,4358144,0,512,18102272,2147483648u,2147483648u,18538496,23552000,29442048,8552960,8454144,1655296,72704,862208,863232,583168,648192,648704,540672,581632,615936,614400,0,2147483648u,17,0,0,0,0,0,0,0,0,0,0,0,0,
104704,512,2147483648u,819712,836096,23543808,2048,23568384,4,0,4343808,4390912,0,512,18460672,2147483648u,2147483648u,18628608,23642112,29458432,8617984,8519680,1671680,74752,864256,865280,583168,648192,543872,550912,582656,615936,614400,0,2147483648u,18,1065353216,1065353216,0,0,0,0,0,0,0,0,0,0,
104704,512,2147483648u,1376768,1393152,23801856,2048,23818240,4,2147483648u,4344832,4423680,0,512,18462720,2147483648u,2147483648u,18718720,23732224,29474816,8618496,8585216,1688064,76800,866304,867328,583168,648192,648704,542720,582656,615936,614400,0,2147483648u,19,1065353216,0,0,0,0,0,0,0,0,0,0,0,
68096,128,2147483648u,1792,1792,28835840,65536,28966912,5,0,1183744,1048576,0,640,9531392,0,0,9437184,23822336,27131904,5283840,5242880,640,70912,655360,656640,557696,622592,544768,532480,557056,590464,589824,0,2147483648u,20,0,0,0,0,0,0,0,0,0,0,0,0,
68096,128,2147483648u,1952,1952,29163520,65536,29294592,5,0,1184768,1081344,0,640,9533440,0,0,9527296,23912448,27148288,5284352,5308416,17024,72960,657408,658688,557696,622592,623744,544768,557056,590464,589824,0,2147483648u,21,0,0,0,0,0,0,0,0,0,0,0,0,
68096,128,2147483648u,640,17024,23822336,2048,23830528,4,0,1185792,1114112,0,640,9891840,0,0,9617408,24002560,27164672,5349376,5373952,33408,75008,659456,660736,557696,622592,544768,534528,558080,590464,589824,0,2147483648u,22,1065353216,1065353216,0,0,0,0,0,0,0,0,0,0,
68096,128,2147483648u,1049216,1065600,24158208,2048,24174592,4,0,1186816,1146880,0,640,9893888,0,0,9707520,24092672,27181056,5349888,5439488,49792,77056,661504,662784,557696,622592,623744,546816,558080,590464,589824,0,2147483648u,23,1065353216,0,0,0,0,0,0,0,0,0,0,0,
80384,640,2147483648u,45824,45824,28837888,65536,28968960,5,0,1200128,1179648,0,640,9539584,2147483648u,2147483648u,9797632,23822336,29753344,5414912,5242880,66176,70912,663552,664832,558720,623616,544896,548864,557056,591488,589824,0,2147483648u,20,0,0,0,0,0,0,0,0,0,0,0,0,
80384,640,2147483648u,45984,45984,29165568,65536,29296640,5,0,1201152,1212416,0,640,9541632,2147483648u,2147483648u,9887744,23912448,29769728,5415424,5308416,82560,72960,665600,666880,558720,623616,624256,544768,557056,591488,589824,0,2147483648u,21,0,0,0,0,0,0,0,0,0,0,0,0,
80384,640,2147483648u,33408,49792,23846912,2048,23863296,4,0,1202176,1245184,0,640,9900032,2147483648u,2147483648u,9977856,24002560,29786112,5480448,5373952,98944,75008,667648,668928,558720,623616,544896,550912,558080,591488,589824,0,2147483648u,22,1065353216,1065353216,0,0,0,0,0,0,0,0,0,0,
80384,640,2147483648u,1081984,1098368,23842816,2048,23859200,4,0,1203200,1277952,0,640,9902080,2147483648u,2147483648u,10067968,24092672,29802496,5480960,5439488,115328,77056,669696,670976,558720,623616,624256,546816,558080,591488,589824,0,2147483648u,23,1065353216,0,0,0,0,0,0,0,0,0,0,0,
92672,128,2147483648u,354048,354048,28852224,65536,28983296,5,0,2232320,2097152,0,640,12414976,0,0,12320768,23822336,27131904,6332416,6291456,524928,70912,720896,722176,565888,630784,545792,532480,565248,598656,598016,0,2147483648u,20,0,0,0,0,0,0,0,0,0,0,0,0,
92672,128,2147483648u,354208,354208,29179904,65536,29310976,5,0,2233344,2129920,0,640,12417024,0,0,12410880,23912448,27148288,6332928,6356992,541312,72960,722944,724224,565888,630784,631936,544768,565248,598656,598016,0,2147483648u,21,0,0,0,0,0,0,0,0,0,0,0,0,
92672,128,2147483648u,262784,279168,24059904,2048,24076288,4,0,2234368,2162688,0,640,12775424,0,0,12500992,24002560,27164672,6397952,6422528,557696,75008,724992,726272,565888,630784,545792,534528,566272,598656,598016,0,2147483648u,22,1065353216,1065353216,0,0,0,0,0,0,0,0,0,0,
92672,128,2147483648u,1311360,1327744,24088576,2048,24113152,4,0,2235392,2195456,0,640,12777472,0,0,12591104,24092672,27181056,6398464,6488064,574080,77056,727040,728320,565888,630784,631936,546816,566272,598656,598016,0,2147483648u,23,1065353216,0,0,0,0,0,0,0,0,0,0,0,
104960,640,2147483648u,398080,398080,28854272,65536,28985344,5,0,2248704,2228224,0,640,12423168,2147483648u,2147483648u,12681216,23822336,29753344,6463488,6291456,590464,70912,729088,730368,566912,631808,545920,548864,565248,599680,598016,0,2147483648u,20,0,0,0,0,0,0,0,0,0,0,0,0,
104960,640,2147483648u,398240,398240,29181952,65536,29313024,5,0,2249728,2260992,0,640,12425216,2147483648u,2147483648u,12771328,23912448,29769728,6464000,6356992,606848,72960,731136,732416,566912,631808,632448,544768,565248,599680,598016,0,2147483648u,21,0,0,0,0,0,0,0,0,0,0,0,0,
104960,640,2147483648u,295552,311936,24092672,2048,24100864,4,0,2250752,2293760,0,640,12783616,2147483648u,2147483648u,12861440,24002560,29786112,6529024,6422528,623232,75008,733184,734464,566912,631808,545920,550912,566272,599680,598016,0,2147483648u,22,1065353216,1065353216,0,0,0,0,0,0,0,0,0,0,
104960,640,2147483648u,1344128,1360512,24129536,2048,24145920,4,0,2251776,2326528,0,640,12785664,2147483648u,2147483648u,12951552,24092672,29802496,6529536,6488064,639616,77056,735232,736512,566912,631808,632448,546816,566272,599680,598016,0,2147483648u,23,1065353216,0,0,0,0,0,0,0,0,0,0,0,
68352,128,2147483648u,706304,706304,28868608,65536,28999680,5,0,3280896,3145728,0,640,15298560,0,0,15204352,23822336,27131904,7380992,7340032,1049216,70912,786432,787712,574080,638976,546816,532480,573440,606848,606208,0,2147483648u,20,0,0,0,0,0,0,0,0,0,0,0,0,
68352,128,2147483648u,706464,706464,29196288,65536,29327360,5,0,3281920,3178496,0,640,15300608,0,0,15294464,23912448,27148288,7381504,7405568,1065600,72960,788480,789760,574080,638976,640128,544768,573440,606848,606208,0,2147483648u,21,0,0,0,0,0,0,0,0,0,0,0,0,
68352,128,2147483648u,524928,541312,23941120,2048,23957504,4,0,3282944,3211264,0,640,15659008,0,0,15384576,24002560,27164672,7446528,7471104,1081984,75008,790528,791808,574080,638976,546816,534528,574464,606848,606208,0,2147483648u,22,1065353216,1065353216,0,0,0,0,0,0,0,0,0,0,
68352,128,2147483648u,1376896,1393280,24162304,2048,24178688,4,2147483648u,3283968,3244032,0,640,15661056,0,0,15474688,24092672,27181056,7447040,7536640,1098368,77056,792576,793856,574080,638976,640128,546816,574464,606848,606208,0,2147483648u,23,1065353216,0,0,0,0,0,0,0,0,0,0,0,
80640,640,2147483648u,750336,750336,28870656,65536,29001728,5,0,3297280,3276800,0,640,15306752,2147483648u,2147483648u,15564800,23822336,29753344,7512064,7340032,1114752,70912,794624,795904,575104,640000,546944,548864,573440,607872,606208,0,2147483648u,20,0,0,0,0,0,0,0,0,0,0,0,0,
80640,640,2147483648u,750496,750496,29198336,65536,29329408,5,0,3298304,3309568,0,640,15308800,2147483648u,2147483648u,15654912,23912448,29769728,7512576,7405568,1131136,72960,796672,797952,575104,640000,640640,544768,573440,607872,606208,0,2147483648u,21,0,0,0,0,0,0,0,0,0,0,0,0,
80640,640,2147483648u,557696,574080,23973888,2048,23990272,4,0,3299328,3342336,0,640,15667200,2147483648u,2147483648u,15745024,24002560,29786112,7577600,7471104,1147520,75008,798720,800000,575104,640000,546944,550912,574464,607872,606208,0,2147483648u,22,1065353216,1065353216,0,0,0,0,0,0,0,0,0,0,
80640,640,2147483648u,1376896,1393280,24162304,2048,24178688,4,2147483648u,3300352,3375104,0,640,15669248,2147483648u,2147483648u,15835136,24092672,29802496,7578112,7536640,1163904,77056,800768,802048,575104,640000,640640,546816,574464,607872,606208,0,2147483648u,23,1065353216,0,0,0,0,0,0,0,0,0,0,0,
92928,128,2147483648u,1058560,1058560,28884992,65536,29016064,5,0,4329472,4194304,0,640,18182144,0,0,18087936,23822336,27131904,8429568,8388608,1573504,70912,851968,853248,582272,647168,547840,532480,581632,615040,614400,0,2147483648u,20,0,0,0,0,0,0,0,0,0,0,0,0,
92928,128,2147483648u,1058720,1058720,29212672,65536,29343744,5,0,4330496,4227072,0,640,18184192,0,0,18178048,23912448,27148288,8430080,8454144,1589888,72960,854016,855296,582272,647168,648320,544768,581632,615040,614400,0,2147483648u,21,0,0,0,0,0,0,0,0,0,0,0,0,
92928,128,2147483648u,787072,803456,23871488,2048,23887872,4,0,4331520,4259840,0,640,18542592,0,0,18268160,24002560,27164672,8495104,8519680,1606272,75008,856064,857344,582272,647168,547840,534528,582656,615040,614400,0,2147483648u,22,1065353216,1065353216,0,0,0,0,0,0,0,0,0,0,
92928,128,2147483648u,1376896,1393280,24162304,2048,24178688,4,2147483648u,4332544,4292608,0,640,18544640,0,0,18358272,24092672,27181056,8495616,8585216,1622656,77056,858112,859392,582272,647168,648320,546816,582656,615040,614400,0,2147483648u,23,1065353216,0,0,0,0,0,0,0,0,0,0,0,
105216,640,2147483648u,1102592,1102592,28887040,65536,29018112,5,0,4345856,4325376,0,640,18190336,2147483648u,2147483648u,18448384,23822336,29753344,8560640,8388608,1639040,70912,860160,861440,583296,648192,547968,548864,581632,616064,614400,0,2147483648u,20,0,0,0,0,0,0,0,0,0,0,0,0,
105216,640,2147483648u,1102752,1102752,29214720,65536,29345792,5,0,4346880,4358144,0,640,18192384,2147483648u,2147483648u,18538496,23912448,29769728,8561152,8454144,1655424,72960,862208,863488,583296,648192,648832,544768,581632,616064,614400,0,2147483648u,21,0,0,0,0,0,0,0,0,0,0,0,0,
105216,640,2147483648u,819840,836224,23904256,2048,23928832,4,0,4347904,4390912,0,640,18550784,2147483648u,2147483648u,18628608,24002560,29786112,8626176,8519680,1671808,75008,864256,865536,583296,648192,547968,550912,582656,616064,614400,0,2147483648u,22,1065353216,1065353216,0,0,0,0,0,0,0,0,0,0,
105216,640,2147483648u,1376896,1393280,24162304,2048,24178688,4,2147483648u,4348928,4423680,0,640,18552832,2147483648u,2147483648u,18718720,24092672,29802496,8626688,8585216,1688192,77056,866304,867584,583296,648192,648832,546816,582656,616064,614400,0,2147483648u,23,1065353216,0,0,0,0,0,0,0,0,0,0,0,
68608,256,2147483648u,2112,2112,29491200,65536,29622272,5,0,1187840,1048576,0,768,9621504,2147483648u,2147483648u,9437184,24182784,27459584,5292032,5242880,768,71168,655360,656896,557824,622592,548864,536576,557056,590592,589824,0,2147483648u,24,0,0,0,0,0,0,0,0,0,0,0,0,
68608,256,2147483648u,2272,2272,29818880,65536,29949952,5,0,1188864,1081344,0,768,9623552,2147483648u,2147483648u,9527296,24272896,27475968,5292544,5308416,17152,73216,657408,658944,557824,622592,623872,548864,557056,590592,589824,0,2147483648u,25,0,0,0,0,0,0,0,0,0,0,0,0,
68608,256,2147483648u,768,17152,24182784,2048,24190976,4,0,1189888,1114112,0,768,9981952,2147483648u,2147483648u,9617408,24363008,27492352,5357568,5373952,33536,75264,659456,660992,557824,622592,548864,538624,558080,590592,589824,0,2147483648u,26,1065353216,1065353216,0,0,0,0,0,0,0,0,0,0,
68608,256,2147483648u,1049344,1065728,24518656,2048,24535040,4,0,1190912,1146880,0,768,9984000,2147483648u,2147483648u,9707520,24453120,27508736,5358080,5439488,49920,77312,661504,663040,557824,622592,623872,550912,558080,590592,589824,0,2147483648u,27,1065353216,0,0,0,0,0,0,0,0,0,0,0,
80896,768,2147483648u,46144,46144,29493248,65536,29624320,5,0,1204224,1179648,0,768,9629696,2147483648u,2147483648u,9797632,24182784,30081024,5423104,5242880,66304,71168,663552,665088,558848,623616,548992,552960,557056,591616,589824,0,2147483648u,24,0,0,0,0,0,0,0,0,0,0,0,0,
80896,768,2147483648u,46304,46304,29820928,65536,29952000,5,0,1205248,1212416,0,768,9631744,2147483648u,2147483648u,9887744,24272896,30097408,5423616,5308416,82688,73216,665600,667136,558848,623616,624384,548864,557056,591616,589824,0,2147483648u,25,0,0,0,0,0,0,0,0,0,0,0,0,
80896,768,2147483648u,33536,49920,24207360,2048,24223744,4,0,1206272,1245184,0,768,9990144,2147483648u,2147483648u,9977856,24363008,30113792,5488640,5373952,99072,75264,667648,669184,558848,623616,548992,555008,558080,591616,589824,0,2147483648u,26,1065353216,1065353216,0,0,0,0,0,0,0,0,0,0,
80896,768,2147483648u,1082112,1098496,24203264,2048,24219648,4,0,1207296,1277952,0,768,9992192,2147483648u,2147483648u,10067968,24453120,30130176,5489152,5439488,115456,77312,669696,671232,558848,623616,624384,550912,558080,591616,589824,0,2147483648u,27,1065353216,0,0,0,0,0,0,0,0,0,0,0,
93184,256,2147483648u,354368,354368,29507584,65536,29638656,5,0,2236416,2097152,0,768,12505088,2147483648u,2147483648u,12320768,24182784,27459584,6340608,6291456,525056,71168,720896,722432,566016,630784,549888,536576,565248,598784,598016,0,2147483648u,24,0,0,0,0,0,0,0,0,0,0,0,0,
93184,256,2147483648u,354528,354528,29835264,65536,29966336,5,0,2237440,2129920,0,768,12507136,2147483648u,2147483648u,12410880,24272896,27475968,6341120,6356992,541440,73216,722944,724480,566016,630784,632064,548864,565248,598784,598016,0,2147483648u,25,0,0,0,0,0,0,0,0,0,0,0,0,
93184,256,2147483648u,262912,279296,24420352,2048,24436736,4,0,2238464,2162688,0,768,12865536,2147483648u,2147483648u,12500992,24363008,27492352,6406144,6422528,557824,75264,724992,726528,566016,630784,549888,538624,566272,598784,598016,0,2147483648u,26,1065353216,1065353216,0,0,0,0,0,0,0,0,0,0,
93184,256,2147483648u,1311488,1327872,24449024,2048,24473600,4,0,2239488,2195456,0,768,12867584,2147483648u,2147483648u,12591104,24453120,27508736,6406656,6488064,574208,77312,727040,728576,566016,630784,632064,550912,566272,598784,598016,0,2147483648u,27,1065353216,0,0,0,0,0,0,0,0,0,0,0,
105472,768,2147483648u,398400,398400,29509632,65536,29640704,5,0,2252800,2228224,0,768,12513280,2147483648u,2147483648u,12681216,24182784,30081024,6471680,6291456,590592,71168,729088,730624,567040,631808,550016,552960,565248,599808,598016,0,2147483648u,24,0,0,0,0,0,0,0,0,0,0,0,0,
105472,768,2147483648u,398560,398560,29837312,65536,29968384,5,0,2253824,2260992,0,768,12515328,2147483648u,2147483648u,12771328,24272896,30097408,6472192,6356992,606976,73216,731136,732672,567040,631808,632576,548864,565248,599808,598016,0,2147483648u,25,0,0,0,0,0,0,0,0,0,0,0,0,
105472,768,2147483648u,295680,312064,24453120,2048,24461312,4,0,2254848,2293760,0,768,12873728,2147483648u,2147483648u,12861440,24363008,30113792,6537216,6422528,623360,75264,733184,734720,567040,631808,550016,555008,566272,599808,598016,0,2147483648u,26,1065353216,1065353216,0,0,0,0,0,0,0,0,0,0,
105472,768,2147483648u,1344256,1360640,24489984,2048,24506368,4,0,2255872,2326528,0,768,12875776,2147483648u,2147483648u,12951552,24453120,30130176,6537728,6488064,639744,77312,735232,736768,567040,631808,632576,550912,566272,599808,598016,0,2147483648u,27,1065353216,0,0,0,0,0,0,0,0,0,0,0,
68864,256,2147483648u,706624,706624,29523968,65536,29655040,5,0,3284992,3145728,0,768,15388672,2147483648u,2147483648u,15204352,24182784,27459584,7389184,7340032,1049344,71168,786432,787968,574208,638976,550912,536576,573440,606976,606208,0,2147483648u,24,0,0,0,0,0,0,0,0,0,0,0,0,
68864,256,2147483648u,706784,706784,29851648,65536,29982720,5,0,3286016,3178496,0,768,15390720,2147483648u,2147483648u,15294464,24272896,27475968,7389696,7405568,1065728,73216,788480,790016,574208,638976,640256,548864,573440,606976,606208,0,2147483648u,25,0,0,0,0,0,0,0,0,0,0,0,0,
68864,256,2147483648u,525056,541440,24301568,2048,24317952,4,0,3287040,3211264,0,768,15749120,2147483648u,2147483648u,15384576,24363008,27492352,7454720,7471104,1082112,75264,790528,792064,574208,638976,550912,538624,574464,606976,606208,0,2147483648u,26,1065353216,1065353216,0,0,0,0,0,0,0,0,0,0,
68864,256,2147483648u,1377024,1393408,24522752,2048,24539136,4,2147483648u,3288064,3244032,0,768,15751168,2147483648u,2147483648u,15474688,24453120,27508736,7455232,7536640,1098496,77312,792576,794112,574208,638976,640256,550912,574464,606976,606208,0,2147483648u,27,1065353216,0,0,0,0,0,0,0,0,0,0,0,
81152,768,2147483648u,750656,750656,29526016,65536,29657088,5,0,3301376,3276800,0,768,15396864,2147483648u,2147483648u,15564800,24182784,30081024,7520256,7340032,1114880,71168,794624,796160,575232,640000,551040,552960,573440,608000,606208,0,2147483648u,24,0,0,0,0,0,0,0,0,0,0,0,0,
81152,768,2147483648u,750816,750816,29853696,65536,29984768,5,0,3302400,3309568,0,768,15398912,2147483648u,2147483648u,15654912,24272896,30097408,7520768,7405568,1131264,73216,796672,798208,575232,640000,640768,548864,573440,608000,606208,0,2147483648u,25,0,0,0,0,0,0,0,0,0,0,0,0,
81152,768,2147483648u,557824,574208,24334336,2048,24350720,4,0,3303424,3342336,0,768,15757312,2147483648u,2147483648u,15745024,24363008,30113792,7585792,7471104,1147648,75264,798720,800256,575232,640000,551040,555008,574464,608000,606208,0,2147483648u,26,1065353216,1065353216,0,0,0,0,0,0,0,0,0,0,
81152,768,2147483648u,1377024,1393408,24522752,2048,24539136,4,2147483648u,3304448,3375104,0,768,15759360,2147483648u,2147483648u,15835136,24453120,30130176,7586304,7536640,1164032,77312,800768,802304,575232,640000,640768,550912,574464,608000,606208,0,2147483648u,27,1065353216,0,0,0,0,0,0,0,0,0,0,0,
93440,256,2147483648u,1058880,1058880,29540352,65536,29671424,5,0,4333568,4194304,0,768,18272256,2147483648u,2147483648u,18087936,24182784,27459584,8437760,8388608,1573632,71168,851968,853504,582400,647168,551936,536576,581632,615168,614400,0,2147483648u,24,0,0,0,0,0,0,0,0,0,0,0,0,
93440,256,2147483648u,1059040,1059040,29868032,65536,29999104,5,0,4334592,4227072,0,768,18274304,2147483648u,2147483648u,18178048,24272896,27475968,8438272,8454144,1590016,73216,854016,855552,582400,647168,648448,548864,581632,615168,614400,0,2147483648u,25,0,0,0,0,0,0,0,0,0,0,0,0,
93440,256,2147483648u,787200,803584,24231936,2048,24248320,4,0,4335616,4259840,0,768,18632704,2147483648u,2147483648u,18268160,24363008,27492352,8503296,8519680,1606400,75264,856064,857600,582400,647168,551936,538624,582656,615168,614400,0,2147483648u,26,1065353216,1065353216,0,0,0,0,0,0,0,0,0,0,
93440,256,2147483648u,1377024,1393408,24522752,2048,24539136,4,2147483648u,4336640,4292608,0,768,18634752,2147483648u,2147483648u,18358272,24453120,27508736,8503808,8585216,1622784,77312,858112,859648,582400,647168,648448,550912,582656,615168,614400,0,2147483648u,27,1065353216,0,0,0,0,0,0,0,0,0,0,0,
105728,768,2147483648u,1102912,1102912,29542400,65536,29673472,5,0,4349952,4325376,0,768,18280448,2147483648u,2147483648u,18448384,24182784,30081024,8568832,8388608,1639168,71168,860160,861696,583424,648192,552064,552960,581632,616192,614400,0,2147483648u,24,0,0,0,0,0,0,0,0,0,0,0,0,
105728,768,2147483648u,1103072,1103072,29870080,65536,30001152,5,0,4350976,4358144,0,768,18282496,2147483648u,2147483648u,18538496,24272896,30097408,8569344,8454144,1655552,73216,862208,863744,583424,648192,648960,548864,581632,616192,614400,0,2147483648u,25,0,0,0,0,0,0,0,0,0,0,0,0,
105728,768,2147483648u,819968,836352,24264704,2048,24289280,4,0,4352000,4390912,0,768,18640896,2147483648u,2147483648u,18628608,24363008,30113792,8634368,8519680,1671936,75264,864256,865792,583424,648192,552064,555008,582656,616192,614400,0,2147483648u,26,1065353216,1065353216,0,0,0,0,0,0,0,0,0,0,
105728,768,2147483648u,1377024,1393408,24522752,2048,24539136,4,2147483648u,4353024,4423680,0,768,18642944,2147483648u,2147483648u,18718720,24453120,30130176,8634880,8585216,1688320,77312,866304,867840,583424,648192,648960,550912,582656,616192,614400,0,2147483648u,27,1065353216,0,0,0,0,0,0,0,0,0,0,0,
69120,384,2147483648u,2432,2432,30146560,65536,30277632,5,0,1191936,1048576,0,896,9711616,2147483648u,2147483648u,9437184,24543232,27787264,5300224,5242880,896,71424,655360,657152,557952,622592,552960,536576,557056,590720,589824,0,2147483648u,28,0,0,0,0,0,0,0,0,0,0,0,0,
69120,384,2147483648u,2592,2592,30474240,65536,30605312,5,0,1192960,1081344,0,896,9713664,2147483648u,2147483648u,9527296,24633344,27803648,5300736,5308416,17280,73472,657408,659200,557952,622592,624000,552960,557056,590720,589824,0,2147483648u,29,0,0,0,0,0,0,0,0,0,0,0,0,
69120,384,2147483648u,896,17280,24543232,2048,24551424,4,0,1193984,1114112,0,896,10072064,2147483648u,2147483648u,9617408,24723456,27820032,5365760,5373952,33664,75520,659456,661248,557952,622592,552960,538624,558080,590720,589824,0,2147483648u,30,1065353216,1065353216,0,0,0,0,0,0,0,0,0,0,
69120,384,2147483648u,1049472,1065856,24879104,2048,24895488,4,0,1195008,1146880,0,896,10074112,2147483648u,2147483648u,9707520,24813568,27836416,5366272,5439488,50048,77568,661504,663296,557952,622592,624000,555008,558080,590720,589824,0,2147483648u,31,1065353216,0,0,0,0,0,0,0,0,0,0,0,
81408,896,2147483648u,46464,46464,30148608,65536,30279680,5,0,1208320,1179648,0,896,9719808,2147483648u,2147483648u,9797632,24543232,30408704,5431296,5242880,66432,71424,663552,665344,558976,623616,553088,552960,557056,591744,589824,0,2147483648u,28,0,0,0,0,0,0,0,0,0,0,0,0,
81408,896,2147483648u,46624,46624,30476288,65536,30607360,5,0,1209344,1212416,0,896,9721856,2147483648u,2147483648u,9887744,24633344,30425088,5431808,5308416,82816,73472,665600,667392,558976,623616,624512,552960,557056,591744,589824,0,2147483648u,29,0,0,0,0,0,0,0,0,0,0,0,0,
81408,896,2147483648u,33664,50048,24567808,2048,24584192,4,0,1210368,1245184,0,896,10080256,2147483648u,2147483648u,9977856,24723456,30441472,5496832,5373952,99200,75520,667648,669440,558976,623616,553088,555008,558080,591744,589824,0,2147483648u,30,1065353216,1065353216,0,0,0,0,0,0,0,0,0,0,
81408,896,2147483648u,1082240,1098624,24563712,2048,24580096,4,0,1211392,1277952,0,896,10082304,2147483648u,2147483648u,10067968,24813568,30457856,5497344,5439488,115584,77568,669696,671488,558976,623616,624512,555008,558080,591744,589824,0,2147483648u,31,1065353216,0,0,0,0,0,0,0,0,0,0,0,
93696,384,2147483648u,354688,354688,30162944,65536,30294016,5,0,2240512,2097152,0,896,12595200,2147483648u,2147483648u,12320768,24543232,27787264,6348800,6291456,525184,71424,720896,722688,566144,630784,553984,536576,565248,598912,598016,0,2147483648u,28,0,0,0,0,0,0,0,0,0,0,0,0,
93696,384,2147483648u,354848,354848,30490624,65536,30621696,5,0,2241536,2129920,0,896,12597248,2147483648u,2147483648u,12410880,24633344,27803648,6349312,6356992,541568,73472,722944,724736,566144,630784,632192,552960,565248,598912,598016,0,2147483648u,29,0,0,0,0,0,0,0,0,0,0,0,0,
93696,384,2147483648u,263040,279424,24780800,2048,24797184,4,0,2242560,2162688,0,896,12955648,2147483648u,2147483648u,12500992,24723456,27820032,6414336,6422528,557952,75520,724992,726784,566144,630784,553984,538624,566272,598912,598016,0,2147483648u,30,1065353216,1065353216,0,0,0,0,0,0,0,0,0,0,
93696,384,2147483648u,1311616,1328000,24809472,2048,24834048,4,0,2243584,2195456,0,896,12957696,2147483648u,2147483648u,12591104,24813568,27836416,6414848,6488064,574336,77568,727040,728832,566144,630784,632192,555008,566272,598912,598016,0,2147483648u,31,1065353216,0,0,0,0,0,0,0,0,0,0,0,
105984,896,2147483648u,398720,398720,30164992,65536,30296064,5,0,2256896,2228224,0,896,12603392,2147483648u,2147483648u,12681216,24543232,30408704,6479872,6291456,590720,71424,729088,730880,567168,631808,554112,552960,565248,599936,598016,0,2147483648u,28,0,0,0,0,0,0,0,0,0,0,0,0,
105984,896,2147483648u,398880,398880,30492672,65536,30623744,5,0,2257920,2260992,0,896,12605440,2147483648u,2147483648u,12771328,24633344,30425088,6480384,6356992,607104,73472,731136,732928,567168,631808,632704,552960,565248,599936,598016,0,2147483648u,29,0,0,0,0,0,0,0,0,0,0,0,0,
105984,896,2147483648u,295808,312192,24813568,2048,24821760,4,0,2258944,2293760,0,896,12963840,2147483648u,2147483648u,12861440,24723456,30441472,6545408,6422528,623488,75520,733184,734976,567168,631808,554112,555008,566272,599936,598016,0,2147483648u,30,1065353216,1065353216,0,0,0,0,0,0,0,0,0,0,
105984,896,2147483648u,1344384,1360768,24850432,2048,24866816,4,0,2259968,2326528,0,896,12965888,2147483648u,2147483648u,12951552,24813568,30457856,6545920,6488064,639872,77568,735232,737024,567168,631808,632704,555008,566272,599936,598016,0,2147483648u,31,1065353216,0,0,0,0,0,0,0,0,0,0,0,
69376,384,2147483648u,706944,706944,30179328,65536,30310400,5,0,3289088,3145728,0,896,15478784,2147483648u,2147483648u,15204352,24543232,27787264,7397376,7340032,1049472,71424,786432,788224,574336,638976,555008,536576,573440,607104,606208,0,2147483648u,28,0,0,0,0,0,0,0,0,0,0,0,0,
69376,384,2147483648u,707104,707104,30507008,65536,30638080,5,0,3290112,3178496,0,896,15480832,2147483648u,2147483648u,15294464,24633344,27803648,7397888,7405568,1065856,73472,788480,790272,574336,638976,640384,552960,573440,607104,606208,0,2147483648u,29,0,0,0,0,0,0,0,0,0,0,0,0,
69376,384,2147483648u,525184,541568,24662016,2048,24678400,4,0,3291136,3211264,0,896,15839232,2147483648u,2147483648u,15384576,24723456,27820032,7462912,7471104,1082240,75520,790528,792320,574336,638976,555008,538624,574464,607104,606208,0,2147483648u,30,1065353216,1065353216,0,0,0,0,0,0,0,0,0,0,
69376,384,2147483648u,1377152,1393536,24883200,2048,24899584,4,2147483648u,3292160,3244032,0,896,15841280,2147483648u,2147483648u,15474688,24813568,27836416,7463424,7536640,1098624,77568,792576,794368,574336,638976,640384,555008,574464,607104,606208,0,2147483648u,31,1065353216,0,0,0,0,0,0,0,0,0,0,0,
81664,896,2147483648u,750976,750976,30181376,65536,30312448,5,0,3305472,3276800,0,896,15486976,2147483648u,2147483648u,15564800,24543232,30408704,7528448,7340032,1115008,71424,794624,796416,575360,640000,555136,552960,573440,608128,606208,0,2147483648u,28,0,0,0,0,0,0,0,0,0,0,0,0,
81664,896,2147483648u,751136,751136,30509056,65536,30640128,5,0,3306496,3309568,0,896,15489024,2147483648u,2147483648u,15654912,24633344,30425088,7528960,7405568,1131392,73472,796672,798464,575360,640000,640896,552960,573440,608128,606208,0,2147483648u,29,0,0,0,0,0,0,0,0,0,0,0,0,
81664,896,2147483648u,557952,574336,24694784,2048,24711168,4,0,3307520,3342336,0,896,15847424,2147483648u,2147483648u,15745024,24723456,30441472,7593984,7471104,1147776,75520,798720,800512,575360,640000,555136,555008,574464,608128,606208,0,2147483648u,30,1065353216,1065353216,0,0,0,0,0,0,0,0,0,0,
81664,896,2147483648u,1377152,1393536,24883200,2048,24899584,4,2147483648u,3308544,3375104,0,896,15849472,2147483648u,2147483648u,15835136,24813568,30457856,7594496,7536640,1164160,77568,800768,802560,575360,640000,640896,555008,574464,608128,606208,0,2147483648u,31,1065353216,0,0,0,0,0,0,0,0,0,0,0,
93952,384,2147483648u,1059200,1059200,30195712,65536,30326784,5,0,4337664,4194304,0,896,18362368,2147483648u,2147483648u,18087936,24543232,27787264,8445952,8388608,1573760,71424,851968,853760,582528,647168,556032,536576,581632,615296,614400,0,2147483648u,28,0,0,0,0,0,0,0,0,0,0,0,0,
93952,384,2147483648u,1059360,1059360,30523392,65536,30654464,5,0,4338688,4227072,0,896,18364416,2147483648u,2147483648u,18178048,24633344,27803648,8446464,8454144,1590144,73472,854016,855808,582528,647168,648576,552960,581632,615296,614400,0,2147483648u,29,0,0,0,0,0,0,0,0,0,0,0,0,
93952,384,2147483648u,787328,803712,24592384,2048,24608768,4,0,4339712,4259840,0,896,18722816,2147483648u,2147483648u,18268160,24723456,27820032,8511488,8519680,1606528,75520,856064,857856,582528,647168,556032,538624,582656,615296,614400,0,2147483648u,30,1065353216,1065353216,0,0,0,0,0,0,0,0,0,0,
93952,384,2147483648u,1377152,1393536,24883200,2048,24899584,4,2147483648u,4340736,4292608,0,896,18724864,2147483648u,2147483648u,18358272,24813568,27836416,8512000,8585216,1622912,77568,858112,859904,582528,647168,648576,555008,582656,615296,614400,0,2147483648u,31,1065353216,0,0,0,0,0,0,0,0,0,0,0,
106240,896,2147483648u,1103232,1103232,30197760,65536,30328832,5,0,4354048,4325376,0,896,18370560,2147483648u,2147483648u,18448384,24543232,30408704,8577024,8388608,1639296,71424,860160,861952,583552,648192,556160,552960,581632,616320,614400,0,2147483648u,28,0,0,0,0,0,0,0,0,0,0,0,0,
106240,896,2147483648u,1103392,1103392,30525440,65536,30656512,5,0,4355072,4358144,0,896,18372608,2147483648u,2147483648u,18538496,24633344,30425088,8577536,8454144,1655680,73472,862208,864000,583552,648192,649088,552960,581632,616320,614400,0,2147483648u,29,0,0,0,0,0,0,0,0,0,0,0,0,
106240,896,2147483648u,820096,836480,24625152,2048,24649728,4,0,4356096,4390912,0,896,18731008,2147483648u,2147483648u,18628608,24723456,30441472,8642560,8519680,1672064,75520,864256,866048,583552,648192,556160,555008,582656,616320,614400,0,2147483648u,30,1065353216,1065353216,0,0,0,0,0,0,0,0,0,0,
106240,896,2147483648u,1377152,1393536,24883200,2048,24899584,4,2147483648u,4357120,4423680,0,896,18733056,2147483648u,2147483648u,18718720,24813568,30457856,8643072,8585216,1688448,77568,866304,868096,583552,648192,649088,555008,582656,616320,614400,0,2147483648u,31,1065353216,0,0,0,0,0,0,0,0,0,0,0,
69632,0,0,88064,88064,25169920,65536,25300992,6,0,1310720,1310720,4,0,10158080,0,0,10158080,22020096,25165824,5505024,5505024,131072,69632,671744,671744,559104,624640,524544,524288,559104,591872,591872,0,2147483648u,32,0,0,0,0,0,0,0,0,0,0,0,0,
69632,0,0,88256,88256,25563136,65536,25694208,6,0,1311744,1343488,4,0,10160128,0,0,10248192,22110208,25182208,5505536,5570560,147456,71680,673792,673792,559104,624640,624640,524288,559104,591872,591872,0,2147483648u,33,0,0,0,0,0,0,0,0,0,0,0,0,
69632,0,0,65536,81920,22077440,2048,22093824,4,0,1312768,1376256,4,0,10518528,0,0,10338304,22200320,25198592,5570560,5636096,163840,73728,675840,675840,559104,624640,524544,526336,560128,591872,591872,0,2147483648u,34,1065353216,1065353216,0,0,0,0,0,0,0,0,0,0,
69632,0,0,1114112,1130496,22073344,2048,22089728,4,0,1313792,1409024,4,0,10520576,0,0,10428416,22290432,25214976,5571072,5701632,180224,75776,677888,677888,559104,624640,624640,526336,560128,591872,591872,0,2147483648u,35,1065353216,0,0,0,0,0,0,0,0,0,0,0,
81920,512,0,132096,132096,25171968,65536,25303040,6,0,1327104,1441792,4,0,10166272,2147483648u,2147483648u,10518528,22020096,28114944,5636096,5505024,196608,69632,679936,679936,560128,625664,524672,540672,559104,592896,591872,0,2147483648u,32,0,0,0,0,0,0,0,0,0,0,0,0,
81920,512,0,132288,132288,25565184,65536,25696256,6,0,1328128,1474560,4,0,10168320,2147483648u,2147483648u,10608640,22110208,28131328,5636608,5570560,212992,71680,681984,681984,560128,625664,625152,524288,559104,592896,591872,0,2147483648u,33,0,0,0,0,0,0,0,0,0,0,0,0,
81920,512,0,98304,114688,22110208,2048,22118400,4,0,1329152,1507328,4,0,10526720,2147483648u,2147483648u,10698752,22200320,28147712,5701632,5636096,229376,73728,684032,684032,560128,625664,524672,542720,560128,592896,591872,0,2147483648u,34,1065353216,1065353216,0,0,0,0,0,0,0,0,0,0,
81920,512,0,1146880,1163264,22106112,2048,22130688,4,0,1330176,1540096,4,0,10528768,2147483648u,2147483648u,10788864,22290432,28164096,5702144,5701632,245760,75776,686080,686080,560128,625664,625152,526336,560128,592896,591872,0,2147483648u,35,1065353216,0,0,0,0,0,0,0,0,0,0,0,
94208,0,0,440320,440320,25186304,65536,25317376,6,0,2359296,2359296,4,0,13041664,0,0,13041664,22020096,25165824,6553600,6553600,655360,69632,737280,737280,567296,632832,525568,524288,567296,600064,600064,0,2147483648u,32,0,0,0,0,0,0,0,0,0,0,0,0,
94208,0,0,440512,440512,25579520,65536,25710592,6,0,2360320,2392064,4,0,13043712,0,0,13131776,22110208,25182208,6554112,6619136,671744,71680,739328,739328,567296,632832,632832,524288,567296,600064,600064,0,2147483648u,33,0,0,0,0,0,0,0,0,0,0,0,0,
94208,0,0,327680,344064,22315008,2048,22331392,4,0,2361344,2424832,4,0,13402112,0,0,13221888,22200320,25198592,6619136,6684672,688128,73728,741376,741376,567296,632832,525568,526336,568320,600064,600064,0,2147483648u,34,1065353216,1065353216,0,0,0,0,0,0,0,0,0,0,
94208,0,0,1376256,1392640,22360064,2048,22376448,4,0,2362368,2457600,4,0,13404160,0,0,13312000,22290432,25214976,6619648,6750208,704512,75776,743424,743424,567296,632832,632832,526336,568320,600064,600064,0,2147483648u,35,1065353216,0,0,0,0,0,0,0,0,0,0,0,
106496,512,0,484352,484352,25188352,65536,25319424,6,0,2375680,2490368,4,0,13049856,2147483648u,2147483648u,13402112,22020096,28114944,6684672,6553600,720896,69632,745472,745472,568320,633856,525696,540672,567296,601088,600064,0,2147483648u,32,0,0,0,0,0,0,0,0,0,0,0,0,
106496,512,0,484544,484544,25581568,65536,25712640,6,0,2376704,2523136,4,0,13051904,2147483648u,2147483648u,13492224,22110208,28131328,6685184,6619136,737280,71680,747520,747520,568320,633856,633344,524288,567296,601088,600064,0,2147483648u,33,0,0,0,0,0,0,0,0,0,0,0,0,
106496,512,0,360448,376832,22347776,2048,22364160,4,0,2377728,2555904,4,0,13410304,2147483648u,2147483648u,13582336,22200320,28147712,6750208,6684672,753664,73728,749568,749568,568320,633856,525696,542720,568320,601088,600064,0,2147483648u,34,1065353216,1065353216,0,0,0,0,0,0,0,0,0,0,
106496,512,0,1376256,1392640,22360064,2048,22376448,4,2147483648u,2378752,2588672,4,0,13412352,2147483648u,2147483648u,13672448,22290432,28164096,6750720,6750208,770048,75776,751616,751616,568320,633856,633344,526336,568320,601088,600064,0,2147483648u,35,1065353216,0,0,0,0,0,0,0,0,0,0,0,
69888,0,0,792576,792576,25202688,65536,25333760,6,0,3407872,3407872,4,0,15925248,0,0,15925248,22020096,25165824,7602176,7602176,1179648,69632,802816,802816,575488,641024,526592,524288,575488,608256,608256,0,2147483648u,32,0,0,0,0,0,0,0,0,0,0,0,0,
69888,0,0,792768,792768,25595904,65536,25726976,6,0,3408896,3440640,4,0,15927296,0,0,16015360,22110208,25182208,7602688,7667712,1196032,71680,804864,804864,575488,641024,641024,524288,575488,608256,608256,0,2147483648u,33,0,0,0,0,0,0,0,0,0,0,0,0,
69888,0,0,589824,606208,22212608,2048,22228992,4,0,3409920,3473408,4,0,16285696,0,0,16105472,22200320,25198592,7667712,7733248,1212416,73728,806912,806912,575488,641024,526592,526336,576512,608256,608256,0,2147483648u,34,1065353216,1065353216,0,0,0,0,0,0,0,0,0,0,
69888,0,0,1376256,1392640,22360064,2048,22376448,4,2147483648u,3410944,3506176,4,0,16287744,0,0,16195584,22290432,25214976,7668224,7798784,1228800,75776,808960,808960,575488,641024,641024,526336,576512,608256,608256,0,2147483648u,35,1065353216,0,0,0,0,0,0,0,0,0,0,0,
82176,512,0,836608,836608,25204736,65536,25335808,6,0,3424256,3538944,4,0,15933440,2147483648u,2147483648u,16285696,22020096,28114944,7733248,7602176,1245184,69632,811008,811008,576512,642048,526720,540672,575488,609280,608256,0,2147483648u,32,0,0,0,0,0,0,0,0,0,0,0,0,
82176,512,0,836800,836800,25597952,65536,25729024,6,0,3425280,3571712,4,0,15935488,2147483648u,2147483648u,16375808,22110208,28131328,7733760,7667712,1261568,71680,813056,813056,576512,642048,641536,524288,575488,609280,608256,0,2147483648u,33,0,0,0,0,0,0,0,0,0,0,0,0,
82176,512,0,622592,638976,22245376,2048,22261760,4,0,3426304,3604480,4,0,16293888,2147483648u,2147483648u,16465920,22200320,28147712,7798784,7733248,1277952,73728,815104,815104,576512,642048,526720,542720,576512,609280,608256,0,2147483648u,34,1065353216,1065353216,0,0,0,0,0,0,0,0,0,0,
82176,512,0,1376256,1392640,22360064,2048,22376448,4,2147483648u,3427328,3637248,4,0,16295936,2147483648u,2147483648u,16556032,22290432,28164096,7799296,7798784,1294336,75776,817152,817152,576512,642048,641536,526336,576512,609280,608256,0,2147483648u,35,1065353216,0,0,0,0,0,0,0,0,0,0,0,
94464,0,0,1144832,1144832,25219072,65536,25350144,6,0,4456448,4456448,4,0,18808832,0,0,18808832,22020096,25165824,8650752,8650752,1703936,69632,868352,868352,583680,649216,527616,524288,583680,616448,616448,0,2147483648u,32,0,0,0,0,0,0,0,0,0,0,0,0,
94464,0,0,1145024,1145024,25612288,65536,25743360,6,0,4457472,4489216,4,0,18810880,0,0,18898944,22110208,25182208,8651264,8716288,1720320,71680,870400,870400,583680,649216,649216,524288,583680,616448,616448,0,2147483648u,33,0,0,0,0,0,0,0,0,0,0,0,0,
94464,0,0,851968,868352,22142976,2048,22159360,4,0,4458496,4521984,4,0,19169280,0,0,18989056,22200320,25198592,8716288,8781824,1736704,73728,872448,872448,583680,649216,527616,526336,584704,616448,616448,0,2147483648u,34,1065353216,1065353216,0,0,0,0,0,0,0,0,0,0,
94464,0,0,1376256,1392640,22360064,2048,22376448,4,2147483648u,4459520,4554752,4,0,19171328,0,0,19079168,22290432,25214976,8716800,8847360,1753088,75776,874496,874496,583680,649216,649216,526336,584704,616448,616448,0,2147483648u,35,1065353216,0,0,0,0,0,0,0,0,0,0,0,
106752,512,0,1188864,1188864,25221120,65536,25352192,6,0,4472832,4587520,4,0,18817024,2147483648u,2147483648u,19169280,22020096,28114944,8781824,8650752,1769472,69632,876544,876544,584704,650240,527744,540672,583680,617472,616448,0,2147483648u,32,0,0,0,0,0,0,0,0,0,0,0,0,
106752,512,0,1189056,1189056,25614336,65536,25745408,6,0,4473856,4620288,4,0,18819072,2147483648u,2147483648u,19259392,22110208,28131328,8782336,8716288,1785856,71680,878592,878592,584704,650240,649728,524288,583680,617472,616448,0,2147483648u,33,0,0,0,0,0,0,0,0,0,0,0,0,
106752,512,0,884736,901120,22175744,2048,22192128,4,0,4474880,4653056,4,0,19177472,2147483648u,2147483648u,19349504,22200320,28147712,8847360,8781824,1802240,73728,880640,880640,584704,650240,527744,542720,584704,617472,616448,0,2147483648u,34,1065353216,1065353216,0,0,0,0,0,0,0,0,0,0,
106752,512,0,1376256,1392640,22360064,2048,22376448,4,2147483648u,4475904,4685824,4,0,19179520,2147483648u,2147483648u,19439616,22290432,28164096,8847872,8847360,1818624,75776,882688,882688,584704,650240,649728,526336,584704,617472,616448,0,2147483648u,35,1065353216,0,0,0,0,0,0,0,0,0,0,0,
70144,128,0,88448,88448,25956352,65536,26087424,6,0,1314816,1310720,4,128,10248192,0,0,10158080,22380544,25559040,5513216,5505024,131200,69888,671744,672000,559232,624640,528640,524288,559104,592000,591872,0,2147483648u,36,0,0,0,0,0,0,0,0,0,0,0,0,
70144,128,0,88640,88640,26349568,65536,26480640,6,0,1315840,1343488,4,128,10250240,0,0,10248192,22470656,25575424,5513728,5570560,147584,71936,673792,674048,559232,624640,624768,528384,559104,592000,591872,0,2147483648u,37,0,0,0,0,0,0,0,0,0,0,0,0,
70144,128,0,65664,82048,22437888,2048,22454272,4,0,1316864,1376256,4,128,10608640,0,0,10338304,22560768,25591808,5578752,5636096,163968,73984,675840,676096,559232,624640,528640,526336,560128,592000,591872,0,2147483648u,38,1065353216,1065353216,0,0,0,0,0,0,0,0,0,0,
70144,128,0,1114240,1130624,22433792,2048,22450176,4,0,1317888,1409024,4,128,10610688,0,0,10428416,22650880,25608192,5579264,5701632,180352,76032,677888,678144,559232,624640,624768,530432,560128,592000,591872,0,2147483648u,39,1065353216,0,0,0,0,0,0,0,0,0,0,0,
82432,640,0,132480,132480,25958400,65536,26089472,6,0,1331200,1441792,4,128,10256384,2147483648u,2147483648u,10518528,22380544,28442624,5644288,5505024,196736,69888,679936,680192,560256,625664,528768,540672,559104,593024,591872,0,2147483648u,36,0,0,0,0,0,0,0,0,0,0,0,0,
82432,640,0,132672,132672,26351616,65536,26482688,6,0,1332224,1474560,4,128,10258432,2147483648u,2147483648u,10608640,22470656,28459008,5644800,5570560,213120,71936,681984,682240,560256,625664,625280,528384,559104,593024,591872,0,2147483648u,37,0,0,0,0,0,0,0,0,0,0,0,0,
82432,640,0,98432,114816,22470656,2048,22478848,4,0,1333248,1507328,4,128,10616832,2147483648u,2147483648u,10698752,22560768,28475392,5709824,5636096,229504,73984,684032,684288,560256,625664,528768,542720,560128,593024,591872,0,2147483648u,38,1065353216,1065353216,0,0,0,0,0,0,0,0,0,0,
82432,640,0,1147008,1163392,22466560,2048,22491136,4,0,1334272,1540096,4,128,10618880,2147483648u,2147483648u,10788864,22650880,28491776,5710336,5701632,245888,76032,686080,686336,560256,625664,625280,530432,560128,593024,591872,0,2147483648u,39,1065353216,0,0,0,0,0,0,0,0,0,0,0,
94720,128,0,440704,440704,25972736,65536,26103808,6,0,2363392,2359296,4,128,13131776,0,0,13041664,22380544,25559040,6561792,6553600,655488,69888,737280,737536,567424,632832,529664,524288,567296,600192,600064,0,2147483648u,36,0,0,0,0,0,0,0,0,0,0,0,0,
94720,128,0,440896,440896,26365952,65536,26497024,6,0,2364416,2392064,4,128,13133824,0,0,13131776,22470656,25575424,6562304,6619136,671872,71936,739328,739584,567424,632832,632960,528384,567296,600192,600064,0,2147483648u,37,0,0,0,0,0,0,0,0,0,0,0,0,
94720,128,0,327808,344192,22675456,2048,22691840,4,0,2365440,2424832,4,128,13492224,0,0,13221888,22560768,25591808,6627328,6684672,688256,73984,741376,741632,567424,632832,529664,526336,568320,600192,600064,0,2147483648u,38,1065353216,1065353216,0,0,0,0,0,0,0,0,0,0,
94720,128,0,1376384,1392768,22720512,2048,22736896,4,0,2366464,2457600,4,128,13494272,0,0,13312000,22650880,25608192,6627840,6750208,704640,76032,743424,743680,567424,632832,632960,530432,568320,600192,600064,0,2147483648u,39,1065353216,0,0,0,0,0,0,0,0,0,0,0,
107008,640,0,484736,484736,25974784,65536,26105856,6,0,2379776,2490368,4,128,13139968,2147483648u,2147483648u,13402112,22380544,28442624,6692864,6553600,721024,69888,745472,745728,568448,633856,529792,540672,567296,601216,600064,0,2147483648u,36,0,0,0,0,0,0,0,0,0,0,0,0,
107008,640,0,484928,484928,26368000,65536,26499072,6,0,2380800,2523136,4,128,13142016,2147483648u,2147483648u,13492224,22470656,28459008,6693376,6619136,737408,71936,747520,747776,568448,633856,633472,528384,567296,601216,600064,0,2147483648u,37,0,0,0,0,0,0,0,0,0,0,0,0,
107008,640,0,360576,376960,22708224,2048,22724608,4,0,2381824,2555904,4,128,13500416,2147483648u,2147483648u,13582336,22560768,28475392,6758400,6684672,753792,73984,749568,749824,568448,633856,529792,542720,568320,601216,600064,0,2147483648u,38,1065353216,1065353216,0,0,0,0,0,0,0,0,0,0,
107008,640,0,1376384,1392768,22720512,2048,22736896,4,2147483648u,2382848,2588672,4,128,13502464,2147483648u,2147483648u,13672448,22650880,28491776,6758912,6750208,770176,76032,751616,751872,568448,633856,633472,530432,568320,601216,600064,0,2147483648u,39,1065353216,0,0,0,0,0,0,0,0,0,0,0,
70400,128,0,792960,792960,25989120,65536,26120192,6,0,3411968,3407872,4,128,16015360,0,0,15925248,22380544,25559040,7610368,7602176,1179776,69888,802816,803072,575616,641024,530688,524288,575488,608384,608256,0,2147483648u,36,0,0,0,0,0,0,0,0,0,0,0,0,
70400,128,0,793152,793152,26382336,65536,26513408,6,0,3412992,3440640,4,128,16017408,0,0,16015360,22470656,25575424,7610880,7667712,1196160,71936,804864,805120,575616,641024,641152,528384,575488,608384,608256,0,2147483648u,37,0,0,0,0,0,0,0,0,0,0,0,0,
70400,128,0,589952,606336,22573056,2048,22589440,4,0,3414016,3473408,4,128,16375808,0,0,16105472,22560768,25591808,7675904,7733248,1212544,73984,806912,807168,575616,641024,530688,526336,576512,608384,608256,0,2147483648u,38,1065353216,1065353216,0,0,0,0,0,0,0,0,0,0,
70400,128,0,1376384,1392768,22720512,2048,22736896,4,2147483648u,3415040,3506176,4,128,16377856,0,0,16195584,22650880,25608192,7676416,7798784,1228928,76032,808960,809216,575616,641024,641152,530432,576512,608384,608256,0,2147483648u,39,1065353216,0,0,0,0,0,0,0,0,0,0,0,
82688,640,0,836992,836992,25991168,65536,26122240,6,0,3428352,3538944,4,128,16023552,2147483648u,2147483648u,16285696,22380544,28442624,7741440,7602176,1245312,69888,811008,811264,576640,642048,530816,540672,575488,609408,608256,0,2147483648u,36,0,0,0,0,0,0,0,0,0,0,0,0,
82688,640,0,837184,837184,26384384,65536,26515456,6,0,3429376,3571712,4,128,16025600,2147483648u,2147483648u,16375808,22470656,28459008,7741952,7667712,1261696,71936,813056,813312,576640,642048,641664,528384,575488,609408,608256,0,2147483648u,37,0,0,0,0,0,0,0,0,0,0,0,0,
82688,640,0,622720,639104,22605824,2048,22622208,4,0,3430400,3604480,4,128,16384000,2147483648u,2147483648u,16465920,22560768,28475392,7806976,7733248,1278080,73984,815104,815360,576640,642048,530816,542720,576512,609408,608256,0,2147483648u,38,1065353216,1065353216,0,0,0,0,0,0,0,0,0,0,
82688,640,0,1376384,1392768,22720512,2048,22736896,4,2147483648u,3431424,3637248,4,128,16386048,2147483648u,2147483648u,16556032,22650880,28491776,7807488,7798784,1294464,76032,817152,817408,576640,642048,641664,530432,576512,609408,608256,0,2147483648u,39,1065353216,0,0,0,0,0,0,0,0,0,0,0,
94976,128,0,1145216,1145216,26005504,65536,26136576,6,0,4460544,4456448,4,128,18898944,0,0,18808832,22380544,25559040,8658944,8650752,1704064,69888,868352,868608,583808,649216,531712,524288,583680,616576,616448,0,2147483648u,36,0,0,0,0,0,0,0,0,0,0,0,0,
94976,128,0,1145408,1145408,26398720,65536,26529792,6,0,4461568,4489216,4,128,18900992,0,0,18898944,22470656,25575424,8659456,8716288,1720448,71936,870400,870656,583808,649216,649344,528384,583680,616576,616448,0,2147483648u,37,0,0,0,0,0,0,0,0,0,0,0,0,
94976,128,0,852096,868480,22503424,2048,22519808,4,0,4462592,4521984,4,128,19259392,0,0,18989056,22560768,25591808,8724480,8781824,1736832,73984,872448,872704,583808,649216,531712,526336,584704,616576,616448,0,2147483648u,38,1065353216,1065353216,0,0,0,0,0,0,0,0,0,0,
94976,128,0,1376384,1392768,22720512,2048,22736896,4,2147483648u,4463616,4554752,4,128,19261440,0,0,19079168,22650880,25608192,8724992,8847360,1753216,76032,874496,874752,583808,649216,649344,530432,584704,616576,616448,0,2147483648u,39,1065353216,0,0,0,0,0,0,0,0,0,0,0,
107264,640,0,1189248,1189248,26007552,65536,26138624,6,0,4476928,4587520,4,128,18907136,2147483648u,2147483648u,19169280,22380544,28442624,8790016,8650752,1769600,69888,876544,876800,584832,650240,531840,540672,583680,617600,616448,0,2147483648u,36,0,0,0,0,0,0,0,0,0,0,0,0,
107264,640,0,1189440,1189440,26400768,65536,26531840,6,0,4477952,4620288,4,128,18909184,2147483648u,2147483648u,19259392,22470656,28459008,8790528,8716288,1785984,71936,878592,878848,584832,650240,649856,528384,583680,617600,616448,0,2147483648u,37,0,0,0,0,0,0,0,0,0,0,0,0,
107264,640,0,884864,901248,22536192,2048,22552576,4,0,4478976,4653056,4,128,19267584,2147483648u,2147483648u,19349504,22560768,28475392,8855552,8781824,1802368,73984,880640,880896,584832,650240,531840,542720,584704,617600,616448,0,2147483648u,38,1065353216,1065353216,0,0,0,0,0,0,0,0,0,0,
107264,640,0,1376384,1392768,22720512,2048,22736896,4,2147483648u,4480000,4685824,4,128,19269632,2147483648u,2147483648u,19439616,22650880,28491776,8856064,8847360,1818752,76032,882688,882944,584832,650240,649856,530432,584704,617600,616448,0,2147483648u,39,1065353216,0,0,0,0,0,0,0,0,0,0,0,
70656,256,0,88832,88832,26742784,65536,26873856,6,0,1318912,1310720,4,256,10338304,0,0,10158080,22740992,25952256,5521408,5505024,131328,70144,671744,672256,559360,624640,532736,528384,559104,592128,591872,0,2147483648u,40,0,0,0,0,0,0,0,0,0,0,0,0,
70656,256,0,89024,89024,27136000,65536,27267072,6,0,1319936,1343488,4,256,10340352,0,0,10248192,22831104,25968640,5521920,5570560,147712,72192,673792,674304,559360,624640,624896,532480,559104,592128,591872,0,2147483648u,41,0,0,0,0,0,0,0,0,0,0,0,0,
70656,256,0,65792,82176,22798336,2048,22814720,4,0,1320960,1376256,4,256,10698752,0,0,10338304,22921216,25985024,5586944,5636096,164096,74240,675840,676352,559360,624640,532736,530432,560128,592128,591872,0,2147483648u,42,1065353216,1065353216,0,0,0,0,0,0,0,0,0,0,
70656,256,0,1114368,1130752,22794240,2048,22810624,4,0,1321984,1409024,4,256,10700800,0,0,10428416,23011328,26001408,5587456,5701632,180480,76288,677888,678400,559360,624640,624896,534528,560128,592128,591872,0,2147483648u,43,1065353216,0,0,0,0,0,0,0,0,0,0,0,
82944,768,0,132864,132864,26744832,65536,26875904,6,0,1335296,1441792,4,256,10346496,2147483648u,2147483648u,10518528,22740992,28770304,5652480,5505024,196864,70144,679936,680448,560384,625664,532864,544768,559104,593152,591872,0,2147483648u,40,0,0,0,0,0,0,0,0,0,0,0,0,
82944,768,0,133056,133056,27138048,65536,27269120,6,0,1336320,1474560,4,256,10348544,2147483648u,2147483648u,10608640,22831104,28786688,5652992,5570560,213248,72192,681984,682496,560384,625664,625408,532480,559104,593152,591872,0,2147483648u,41,0,0,0,0,0,0,0,0,0,0,0,0,
82944,768,0,98560,114944,22831104,2048,22839296,4,0,1337344,1507328,4,256,10706944,2147483648u,2147483648u,10698752,22921216,28803072,5718016,5636096,229632,74240,684032,684544,560384,625664,532864,546816,560128,593152,591872,0,2147483648u,42,1065353216,1065353216,0,0,0,0,0,0,0,0,0,0,
82944,768,0,1147136,1163520,22827008,2048,22851584,4,0,1338368,1540096,4,256,10708992,2147483648u,2147483648u,10788864,23011328,28819456,5718528,5701632,246016,76288,686080,686592,560384,625664,625408,534528,560128,593152,591872,0,2147483648u,43,1065353216,0,0,0,0,0,0,0,0,0,0,0,
95232,256,0,441088,441088,26759168,65536,26890240,6,0,2367488,2359296,4,256,13221888,0,0,13041664,22740992,25952256,6569984,6553600,655616,70144,737280,737792,567552,632832,533760,528384,567296,600320,600064,0,2147483648u,40,0,0,0,0,0,0,0,0,0,0,0,0,
95232,256,0,441280,441280,27152384,65536,27283456,6,0,2368512,2392064,4,256,13223936,0,0,13131776,22831104,25968640,6570496,6619136,672000,72192,739328,739840,567552,632832,633088,532480,567296,600320,600064,0,2147483648u,41,0,0,0,0,0,0,0,0,0,0,0,0,
95232,256,0,327936,344320,23035904,2048,23052288,4,0,2369536,2424832,4,256,13582336,0,0,13221888,22921216,25985024,6635520,6684672,688384,74240,741376,741888,567552,632832,533760,530432,568320,600320,600064,0,2147483648u,42,1065353216,1065353216,0,0,0,0,0,0,0,0,0,0,
95232,256,0,1376512,1392896,23080960,2048,23097344,4,0,2370560,2457600,4,256,13584384,0,0,13312000,23011328,26001408,6636032,6750208,704768,76288,743424,743936,567552,632832,633088,534528,568320,600320,600064,0,2147483648u,43,1065353216,0,0,0,0,0,0,0,0,0,0,0,
107520,768,0,485120,485120,26761216,65536,26892288,6,0,2383872,2490368,4,256,13230080,2147483648u,2147483648u,13402112,22740992,28770304,6701056,6553600,721152,70144,745472,745984,568576,633856,533888,544768,567296,601344,600064,0,2147483648u,40,0,0,0,0,0,0,0,0,0,0,0,0,
107520,768,0,485312,485312,27154432,65536,27285504,6,0,2384896,2523136,4,256,13232128,2147483648u,2147483648u,13492224,22831104,28786688,6701568,6619136,737536,72192,747520,748032,568576,633856,633600,532480,567296,601344,600064,0,2147483648u,41,0,0,0,0,0,0,0,0,0,0,0,0,
107520,768,0,360704,377088,23068672,2048,23085056,4,0,2385920,2555904,4,256,13590528,2147483648u,2147483648u,13582336,22921216,28803072,6766592,6684672,753920,74240,749568,750080,568576,633856,533888,546816,568320,601344,600064,0,2147483648u,42,1065353216,1065353216,0,0,0,0,0,0,0,0,0,0,
107520,768,0,1376512,1392896,23080960,2048,23097344,4,2147483648u,2386944,2588672,4,256,13592576,2147483648u,2147483648u,13672448,23011328,28819456,6767104,6750208,770304,76288,751616,752128,568576,633856,633600,534528,568320,601344,600064,0,2147483648u,43,1065353216,0,0,0,0,0,0,0,0,0,0,0,
70912,256,0,793344,793344,26775552,65536,26906624,6,0,3416064,3407872,4,256,16105472,0,0,15925248,22740992,25952256,7618560,7602176,1179904,70144,802816,803328,575744,641024,534784,528384,575488,608512,608256,0,2147483648u,40,0,0,0,0,0,0,0,0,0,0,0,0,
70912,256,0,793536,793536,27168768,65536,27299840,6,0,3417088,3440640,4,256,16107520,0,0,16015360,22831104,25968640,7619072,7667712,1196288,72192,804864,805376,575744,641024,641280,532480,575488,608512,608256,0,2147483648u,41,0,0,0,0,0,0,0,0,0,0,0,0,
70912,256,0,590080,606464,22933504,2048,22949888,4,0,3418112,3473408,4,256,16465920,0,0,16105472,22921216,25985024,7684096,7733248,1212672,74240,806912,807424,575744,641024,534784,530432,576512,608512,608256,0,2147483648u,42,1065353216,1065353216,0,0,0,0,0,0,0,0,0,0,
70912,256,0,1376512,1392896,23080960,2048,23097344,4,2147483648u,3419136,3506176,4,256,16467968,0,0,16195584,23011328,26001408,7684608,7798784,1229056,76288,808960,809472,575744,641024,641280,534528,576512,608512,608256,0,2147483648u,43,1065353216,0,0,0,0,0,0,0,0,0,0,0,
83200,768,0,837376,837376,26777600,65536,26908672,6,0,3432448,3538944,4,256,16113664,2147483648u,2147483648u,16285696,22740992,28770304,7749632,7602176,1245440,70144,811008,811520,576768,642048,534912,544768,575488,609536,608256,0,2147483648u,40,0,0,0,0,0,0,0,0,0,0,0,0,
83200,768,0,837568,837568,27170816,65536,27301888,6,0,3433472,3571712,4,256,16115712,2147483648u,2147483648u,16375808,22831104,28786688,7750144,7667712,1261824,72192,813056,813568,576768,642048,641792,532480,575488,609536,608256,0,2147483648u,41,0,0,0,0,0,0,0,0,0,0,0,0,
83200,768,0,622848,639232,22966272,2048,22982656,4,0,3434496,3604480,4,256,16474112,2147483648u,2147483648u,16465920,22921216,28803072,7815168,7733248,1278208,74240,815104,815616,576768,642048,534912,546816,576512,609536,608256,0,2147483648u,42,1065353216,1065353216,0,0,0,0,0,0,0,0,0,0,
83200,768,0,1376512,1392896,23080960,2048,23097344,4,2147483648u,3435520,3637248,4,256,16476160,2147483648u,2147483648u,16556032,23011328,28819456,7815680,7798784,1294592,76288,817152,817664,576768,642048,641792,534528,576512,609536,608256,0,2147483648u,43,1065353216,0,0,0,0,0,0,0,0,0,0,0,
95488,256,0,1145600,1145600,26791936,65536,26923008,6,0,4464640,4456448,4,256,18989056,0,0,18808832,22740992,25952256,8667136,8650752,1704192,70144,868352,868864,583936,649216,535808,528384,583680,616704,616448,0,2147483648u,40,0,0,0,0,0,0,0,0,0,0,0,0,
95488,256,0,1145792,1145792,27185152,65536,27316224,6,0,4465664,4489216,4,256,18991104,0,0,18898944,22831104,25968640,8667648,8716288,1720576,72192,870400,870912,583936,649216,649472,532480,583680,616704,616448,0,2147483648u,41,0,0,0,0,0,0,0,0,0,0,0,0,
95488,256,0,852224,868608,22863872,2048,22880256,4,0,4466688,4521984,4,256,19349504,0,0,18989056,22921216,25985024,8732672,8781824,1736960,74240,872448,872960,583936,649216,535808,530432,584704,616704,616448,0,2147483648u,42,1065353216,1065353216,0,0,0,0,0,0,0,0,0,0,
95488,256,0,1376512,1392896,23080960,2048,23097344,4,2147483648u,4467712,4554752,4,256,19351552,0,0,19079168,23011328,26001408,8733184,8847360,1753344,76288,874496,875008,583936,649216,649472,534528,584704,616704,616448,0,2147483648u,43,1065353216,0,0,0,0,0,0,0,0,0,0,0,
107776,768,0,1189632,1189632,26793984,65536,26925056,6,0,4481024,4587520,4,256,18997248,2147483648u,2147483648u,19169280,22740992,28770304,8798208,8650752,1769728,70144,876544,877056,584960,650240,535936,544768,583680,617728,616448,0,2147483648u,40,0,0,0,0,0,0,0,0,0,0,0,0,
107776,768,0,1189824,1189824,27187200,65536,27318272,6,0,4482048,4620288,4,256,18999296,2147483648u,2147483648u,19259392,22831104,28786688,8798720,8716288,1786112,72192,878592,879104,584960,650240,649984,532480,583680,617728,616448,0,2147483648u,41,0,0,0,0,0,0,0,0,0,0,0,0,
107776,768,0,884992,901376,22896640,2048,22913024,4,0,4483072,4653056,4,256,19357696,2147483648u,2147483648u,19349504,22921216,28803072,8863744,8781824,1802496,74240,880640,881152,584960,650240,535936,546816,584704,617728,616448,0,2147483648u,42,1065353216,1065353216,0,0,0,0,0,0,0,0,0,0,
107776,768,0,1376512,1392896,23080960,2048,23097344,4,2147483648u,4484096,4685824,4,256,19359744,2147483648u,2147483648u,19439616,23011328,28819456,8864256,8847360,1818880,76288,882688,883200,584960,650240,649984,534528,584704,617728,616448,0,2147483648u,43,1065353216,0,0,0,0,0,0,0,0,0,0,0,
71168,384,0,89216,89216,27529216,65536,27660288,5,0,1323008,1310720,4,384,10428416,0,0,10158080,23101440,26345472,5529600,5505024,131456,70400,671744,672512,559488,624640,536832,528384,559104,592256,591872,0,2147483648u,44,0,0,0,0,0,0,0,0,0,0,0,0,
71168,384,0,89376,89376,27856896,65536,27987968,5,0,1324032,1343488,4,384,10430464,0,0,10248192,23191552,26361856,5530112,5570560,147840,72448,673792,674560,559488,624640,625024,536576,559104,592256,591872,0,2147483648u,45,0,0,0,0,0,0,0,0,0,0,0,0,
71168,384,0,65920,82304,23158784,2048,23175168,4,0,1325056,1376256,4,384,10788864,0,0,10338304,23281664,26378240,5595136,5636096,164224,74496,675840,676608,559488,624640,536832,530432,560128,592256,591872,0,2147483648u,46,1065353216,1065353216,0,0,0,0,0,0,0,0,0,0,
71168,384,0,1114496,1130880,23154688,2048,23171072,4,0,1326080,1409024,4,384,10790912,0,0,10428416,23371776,26394624,5595648,5701632,180608,76544,677888,678656,559488,624640,625024,538624,560128,592256,591872,0,2147483648u,47,1065353216,0,0,0,0,0,0,0,0,0,0,0,
83456,896,0,133248,133248,27531264,65536,27662336,5,0,1339392,1441792,4,384,10436608,2147483648u,2147483648u,10518528,23101440,29097984,5660672,5505024,196992,70400,679936,680704,560512,625664,536960,544768,559104,593280,591872,0,2147483648u,44,0,0,0,0,0,0,0,0,0,0,0,0,
83456,896,0,133408,133408,27858944,65536,27990016,5,0,1340416,1474560,4,384,10438656,2147483648u,2147483648u,10608640,23191552,29114368,5661184,5570560,213376,72448,681984,682752,560512,625664,625536,536576,559104,593280,591872,0,2147483648u,45,0,0,0,0,0,0,0,0,0,0,0,0,
83456,896,0,98688,115072,23191552,2048,23199744,4,0,1341440,1507328,4,384,10797056,2147483648u,2147483648u,10698752,23281664,29130752,5726208,5636096,229760,74496,684032,684800,560512,625664,536960,546816,560128,593280,591872,0,2147483648u,46,1065353216,1065353216,0,0,0,0,0,0,0,0,0,0,
83456,896,0,1147264,1163648,23187456,2048,23212032,4,0,1342464,1540096,4,384,10799104,2147483648u,2147483648u,10788864,23371776,29147136,5726720,5701632,246144,76544,686080,686848,560512,625664,625536,538624,560128,593280,591872,0,2147483648u,47,1065353216,0,0,0,0,0,0,0,0,0,0,0,
95744,384,0,441472,441472,27545600,65536,27676672,5,0,2371584,2359296,4,384,13312000,0,0,13041664,23101440,26345472,6578176,6553600,655744,70400,737280,738048,567680,632832,537856,528384,567296,600448,600064,0,2147483648u,44,0,0,0,0,0,0,0,0,0,0,0,0,
95744,384,0,441632,441632,27873280,65536,28004352,5,0,2372608,2392064,4,384,13314048,0,0,13131776,23191552,26361856,6578688,6619136,672128,72448,739328,740096,567680,632832,633216,536576,567296,600448,600064,0,2147483648u,45,0,0,0,0,0,0,0,0,0,0,0,0,
95744,384,0,328064,344448,23396352,2048,23412736,4,0,2373632,2424832,4,384,13672448,0,0,13221888,23281664,26378240,6643712,6684672,688512,74496,741376,742144,567680,632832,537856,530432,568320,600448,600064,0,2147483648u,46,1065353216,1065353216,0,0,0,0,0,0,0,0,0,0,
95744,384,0,1376640,1393024,23441408,2048,23457792,4,0,2374656,2457600,4,384,13674496,0,0,13312000,23371776,26394624,6644224,6750208,704896,76544,743424,744192,567680,632832,633216,538624,568320,600448,600064,0,2147483648u,47,1065353216,0,0,0,0,0,0,0,0,0,0,0,
108032,896,0,485504,485504,27547648,65536,27678720,5,0,2387968,2490368,4,384,13320192,2147483648u,2147483648u,13402112,23101440,29097984,6709248,6553600,721280,70400,745472,746240,568704,633856,537984,544768,567296,601472,600064,0,2147483648u,44,0,0,0,0,0,0,0,0,0,0,0,0,
108032,896,0,485664,485664,27875328,65536,28006400,5,0,2388992,2523136,4,384,13322240,2147483648u,2147483648u,13492224,23191552,29114368,6709760,6619136,737664,72448,747520,748288,568704,633856,633728,536576,567296,601472,600064,0,2147483648u,45,0,0,0,0,0,0,0,0,0,0,0,0,
108032,896,0,360832,377216,23429120,2048,23445504,4,0,2390016,2555904,4,384,13680640,2147483648u,2147483648u,13582336,23281664,29130752,6774784,6684672,754048,74496,749568,750336,568704,633856,537984,546816,568320,601472,600064,0,2147483648u,46,1065353216,1065353216,0,0,0,0,0,0,0,0,0,0,
108032,896,0,1376640,1393024,23441408,2048,23457792,4,2147483648u,2391040,2588672,4,384,13682688,2147483648u,2147483648u,13672448,23371776,29147136,6775296,6750208,770432,76544,751616,752384,568704,633856,633728,538624,568320,601472,600064,0,2147483648u,47,1065353216,0,0,0,0,0,0,0,0,0,0,0,
71424,384,0,793728,793728,27561984,65536,27693056,5,0,3420160,3407872,4,384,16195584,0,0,15925248,23101440,26345472,7626752,7602176,1180032,70400,802816,803584,575872,641024,538880,528384,575488,608640,608256,0,2147483648u,44,0,0,0,0,0,0,0,0,0,0,0,0,
71424,384,0,793888,793888,27889664,65536,28020736,5,0,3421184,3440640,4,384,16197632,0,0,16015360,23191552,26361856,7627264,7667712,1196416,72448,804864,805632,575872,641024,641408,536576,575488,608640,608256,0,2147483648u,45,0,0,0,0,0,0,0,0,0,0,0,0,
71424,384,0,590208,606592,23293952,2048,23310336,4,0,3422208,3473408,4,384,16556032,0,0,16105472,23281664,26378240,7692288,7733248,1212800,74496,806912,807680,575872,641024,538880,530432,576512,608640,608256,0,2147483648u,46,1065353216,1065353216,0,0,0,0,0,0,0,0,0,0,
71424,384,0,1376640,1393024,23441408,2048,23457792,4,2147483648u,3423232,3506176,4,384,16558080,0,0,16195584,23371776,26394624,7692800,7798784,1229184,76544,808960,809728,575872,641024,641408,538624,576512,608640,608256,0,2147483648u,47,1065353216,0,0,0,0,0,0,0,0,0,0,0,
83712,896,0,837760,837760,27564032,65536,27695104,5,0,3436544,3538944,4,384,16203776,2147483648u,2147483648u,16285696,23101440,29097984,7757824,7602176,1245568,70400,811008,811776,576896,642048,539008,544768,575488,609664,608256,0,2147483648u,44,0,0,0,0,0,0,0,0,0,0,0,0,
83712,896,0,837920,837920,27891712,65536,28022784,5,0,3437568,3571712,4,384,16205824,2147483648u,2147483648u,16375808,23191552,29114368,7758336,7667712,1261952,72448,813056,813824,576896,642048,641920,536576,575488,609664,608256,0,2147483648u,45,0,0,0,0,0,0,0,0,0,0,0,0,
83712,896,0,622976,639360,23326720,2048,23343104,4,0,3438592,3604480,4,384,16564224,2147483648u,2147483648u,16465920,23281664,29130752,7823360,7733248,1278336,74496,815104,815872,576896,642048,539008,546816,576512,609664,608256,0,2147483648u,46,1065353216,1065353216,0,0,0,0,0,0,0,0,0,0,
83712,896,0,1376640,1393024,23441408,2048,23457792,4,2147483648u,3439616,3637248,4,384,16566272,2147483648u,2147483648u,16556032,23371776,29147136,7823872,7798784,1294720,76544,817152,817920,576896,642048,641920,538624,576512,609664,608256,0,2147483648u,47,1065353216,0,0,0,0,0,0,0,0,0,0,0,
96000,384,0,1145984,1145984,27578368,65536,27709440,5,0,4468736,4456448,4,384,19079168,0,0,18808832,23101440,26345472,8675328,8650752,1704320,70400,868352,869120,584064,649216,539904,528384,583680,616832,616448,0,2147483648u,44,0,0,0,0,0,0,0,0,0,0,0,0,
96000,384,0,1146144,1146144,27906048,65536,28037120,5,0,4469760,4489216,4,384,19081216,0,0,18898944,23191552,26361856,8675840,8716288,1720704,72448,870400,871168,584064,649216,649600,536576,583680,616832,616448,0,2147483648u,45,0,0,0,0,0,0,0,0,0,0,0,0,
96000,384,0,852352,868736,23224320,2048,23240704,4,0,4470784,4521984,4,384,19439616,0,0,18989056,23281664,26378240,8740864,8781824,1737088,74496,872448,873216,584064,649216,539904,530432,584704,616832,616448,0,2147483648u,46,1065353216,1065353216,0,0,0,0,0,0,0,0,0,0,
96000,384,0,1376640,1393024,23441408,2048,23457792,4,2147483648u,4471808,4554752,4,384,19441664,0,0,19079168,23371776,26394624,8741376,8847360,1753472,76544,874496,875264,584064,649216,649600,538624,584704,616832,616448,0,2147483648u,47,1065353216,0,0,0,0,0,0,0,0,0,0,0,
108288,896,0,1190016,1190016,27580416,65536,27711488,5,0,4485120,4587520,4,384,19087360,2147483648u,2147483648u,19169280,23101440,29097984,8806400,8650752,1769856,70400,876544,877312,585088,650240,540032,544768,583680,617856,616448,0,2147483648u,44,0,0,0,0,0,0,0,0,0,0,0,0,
108288,896,0,1190176,1190176,27908096,65536,28039168,5,0,4486144,4620288,4,384,19089408,2147483648u,2147483648u,19259392,23191552,29114368,8806912,8716288,1786240,72448,878592,879360,585088,650240,650112,536576,583680,617856,616448,0,2147483648u,45,0,0,0,0,0,0,0,0,0,0,0,0,
108288,896,0,885120,901504,23257088,2048,23273472,4,0,4487168,4653056,4,384,19447808,2147483648u,2147483648u,19349504,23281664,29130752,8871936,8781824,1802624,74496,880640,881408,585088,650240,540032,546816,584704,617856,616448,0,2147483648u,46,1065353216,1065353216,0,0,0,0,0,0,0,0,0,0,
108288,896,0,1376640,1393024,23441408,2048,23457792,4,2147483648u,4488192,4685824,4,384,19449856,2147483648u,2147483648u,19439616,23371776,29147136,8872448,8847360,1819008,76544,882688,883456,585088,650240,650112,538624,584704,617856,616448,0,2147483648u,47,1065353216,0,0,0,0,0,0,0,0,0,0,0,
71680,0,0,89536,89536,28184576,65536,28315648,5,0,1441792,1310720,4,512,10162176,0,0,10158080,23461888,26738688,5537792,5505024,131584,70656,671744,672768,559616,624640,540928,532480,559104,592384,591872,0,2147483648u,48,0,0,0,0,0,0,0,0,0,0,0,0,
71680,0,0,89696,89696,28512256,65536,28643328,5,0,1442816,1343488,4,512,10164224,0,0,10248192,23552000,26755072,5538304,5570560,147968,72704,673792,674816,559616,624640,625664,540672,559104,592384,591872,0,2147483648u,49,0,0,0,0,0,0,0,0,0,0,0,0,
71680,0,0,66048,82432,23519232,2048,23535616,4,0,1443840,1376256,4,512,10522624,0,0,10338304,23642112,26771456,5603328,5636096,164352,74752,675840,676864,559616,624640,540928,534528,560128,592384,591872,0,2147483648u,50,1065353216,1065353216,0,0,0,0,0,0,0,0,0,0,
71680,0,0,1114624,1131008,23515136,2048,23531520,4,0,1444864,1409024,4,512,10524672,0,0,10428416,23732224,26787840,5603840,5701632,180736,76800,677888,678912,559616,624640,625664,542720,560128,592384,591872,0,2147483648u,51,1065353216,0,0,0,0,0,0,0,0,0,0,0,
83968,512,0,133568,133568,28186624,65536,28317696,5,0,1458176,1441792,4,512,10170368,2147483648u,2147483648u,10518528,23461888,29425664,5668864,5505024,197120,70656,679936,680960,560640,625664,541056,548864,559104,593408,591872,0,2147483648u,48,0,0,0,0,0,0,0,0,0,0,0,0,
83968,512,0,133728,133728,28514304,65536,28645376,5,0,1459200,1474560,4,512,10172416,2147483648u,2147483648u,10608640,23552000,29442048,5669376,5570560,213504,72704,681984,683008,560640,625664,626176,540672,559104,593408,591872,0,2147483648u,49,0,0,0,0,0,0,0,0,0,0,0,0,
83968,512,0,98816,115200,23552000,2048,23560192,4,0,1460224,1507328,4,512,10530816,2147483648u,2147483648u,10698752,23642112,29458432,5734400,5636096,229888,74752,684032,685056,560640,625664,541056,550912,560128,593408,591872,0,2147483648u,50,1065353216,1065353216,0,0,0,0,0,0,0,0,0,0,
83968,512,0,1147392,1163776,23547904,2048,23572480,4,0,1461248,1540096,4,512,10532864,2147483648u,2147483648u,10788864,23732224,29474816,5734912,5701632,246272,76800,686080,687104,560640,625664,626176,542720,560128,593408,591872,0,2147483648u,51,1065353216,0,0,0,0,0,0,0,0,0,0,0,
96256,0,0,441792,441792,28200960,65536,28332032,5,0,2490368,2359296,4,512,13045760,0,0,13041664,23461888,26738688,6586368,6553600,655872,70656,737280,738304,567808,632832,541952,532480,567296,600576,600064,0,2147483648u,48,0,0,0,0,0,0,0,0,0,0,0,0,
96256,0,0,441952,441952,28528640,65536,28659712,5,0,2491392,2392064,4,512,13047808,0,0,13131776,23552000,26755072,6586880,6619136,672256,72704,739328,740352,567808,632832,633856,540672,567296,600576,600064,0,2147483648u,49,0,0,0,0,0,0,0,0,0,0,0,0,
96256,0,0,328192,344576,23756800,2048,23773184,4,0,2492416,2424832,4,512,13406208,0,0,13221888,23642112,26771456,6651904,6684672,688640,74752,741376,742400,567808,632832,541952,534528,568320,600576,600064,0,2147483648u,50,1065353216,1065353216,0,0,0,0,0,0,0,0,0,0,
96256,0,0,1376768,1393152,23801856,2048,23818240,4,0,2493440,2457600,4,512,13408256,0,0,13312000,23732224,26787840,6652416,6750208,705024,76800,743424,744448,567808,632832,633856,542720,568320,600576,600064,0,2147483648u,51,1065353216,0,0,0,0,0,0,0,0,0,0,0,
108544,512,0,485824,485824,28203008,65536,28334080,5,0,2506752,2490368,4,512,13053952,2147483648u,2147483648u,13402112,23461888,29425664,6717440,6553600,721408,70656,745472,746496,568832,633856,542080,548864,567296,601600,600064,0,2147483648u,48,0,0,0,0,0,0,0,0,0,0,0,0,
108544,512,0,485984,485984,28530688,65536,28661760,5,0,2507776,2523136,4,512,13056000,2147483648u,2147483648u,13492224,23552000,29442048,6717952,6619136,737792,72704,747520,748544,568832,633856,634368,540672,567296,601600,600064,0,2147483648u,49,0,0,0,0,0,0,0,0,0,0,0,0,
108544,512,0,360960,377344,23789568,2048,23805952,4,0,2508800,2555904,4,512,13414400,2147483648u,2147483648u,13582336,23642112,29458432,6782976,6684672,754176,74752,749568,750592,568832,633856,542080,550912,568320,601600,600064,0,2147483648u,50,1065353216,1065353216,0,0,0,0,0,0,0,0,0,0,
108544,512,0,1376768,1393152,23801856,2048,23818240,4,2147483648u,2509824,2588672,4,512,13416448,2147483648u,2147483648u,13672448,23732224,29474816,6783488,6750208,770560,76800,751616,752640,568832,633856,634368,542720,568320,601600,600064,0,2147483648u,51,1065353216,0,0,0,0,0,0,0,0,0,0,0,
71936,0,0,794048,794048,28217344,65536,28348416,5,0,3538944,3407872,4,512,15929344,0,0,15925248,23461888,26738688,7634944,7602176,1180160,70656,802816,803840,576000,641024,542976,532480,575488,608768,608256,0,2147483648u,48,0,0,0,0,0,0,0,0,0,0,0,0,
71936,0,0,794208,794208,28545024,65536,28676096,5,0,3539968,3440640,4,512,15931392,0,0,16015360,23552000,26755072,7635456,7667712,1196544,72704,804864,805888,576000,641024,642048,540672,575488,608768,608256,0,2147483648u,49,0,0,0,0,0,0,0,0,0,0,0,0,
71936,0,0,590336,606720,23654400,2048,23670784,4,0,3540992,3473408,4,512,16289792,0,0,16105472,23642112,26771456,7700480,7733248,1212928,74752,806912,807936,576000,641024,542976,534528,576512,608768,608256,0,2147483648u,50,1065353216,1065353216,0,0,0,0,0,0,0,0,0,0,
71936,0,0,1376768,1393152,23801856,2048,23818240,4,2147483648u,3542016,3506176,4,512,16291840,0,0,16195584,23732224,26787840,7700992,7798784,1229312,76800,808960,809984,576000,641024,642048,542720,576512,608768,608256,0,2147483648u,51,1065353216,0,0,0,0,0,0,0,0,0,0,0,
84224,512,0,838080,838080,28219392,65536,28350464,5,0,3555328,3538944,4,512,15937536,2147483648u,2147483648u,16285696,23461888,29425664,7766016,7602176,1245696,70656,811008,812032,577024,642048,543104,548864,575488,609792,608256,0,2147483648u,48,0,0,0,0,0,0,0,0,0,0,0,0,
84224,512,0,838240,838240,28547072,65536,28678144,5,0,3556352,3571712,4,512,15939584,2147483648u,2147483648u,16375808,23552000,29442048,7766528,7667712,1262080,72704,813056,814080,577024,642048,642560,540672,575488,609792,608256,0,2147483648u,49,0,0,0,0,0,0,0,0,0,0,0,0,
84224,512,0,623104,639488,23687168,2048,23703552,4,0,3557376,3604480,4,512,16297984,2147483648u,2147483648u,16465920,23642112,29458432,7831552,7733248,1278464,74752,815104,816128,577024,642048,543104,550912,576512,609792,608256,0,2147483648u,50,1065353216,1065353216,0,0,0,0,0,0,0,0,0,0,
84224,512,0,1376768,1393152,23801856,2048,23818240,4,2147483648u,3558400,3637248,4,512,16300032,2147483648u,2147483648u,16556032,23732224,29474816,7832064,7798784,1294848,76800,817152,818176,577024,642048,642560,542720,576512,609792,608256,0,2147483648u,51,1065353216,0,0,0,0,0,0,0,0,0,0,0,
96512,0,0,1146304,1146304,28233728,65536,28364800,5,0,4587520,4456448,4,512,18812928,0,0,18808832,23461888,26738688,8683520,8650752,1704448,70656,868352,869376,584192,649216,544000,532480,583680,616960,616448,0,2147483648u,48,0,0,0,0,0,0,0,0,0,0,0,0,
96512,0,0,1146464,1146464,28561408,65536,28692480,5,0,4588544,4489216,4,512,18814976,0,0,18898944,23552000,26755072,8684032,8716288,1720832,72704,870400,871424,584192,649216,650240,540672,583680,616960,616448,0,2147483648u,49,0,0,0,0,0,0,0,0,0,0,0,0,
96512,0,0,852480,868864,23584768,2048,23601152,4,0,4589568,4521984,4,512,19173376,0,0,18989056,23642112,26771456,8749056,8781824,1737216,74752,872448,873472,584192,649216,544000,534528,584704,616960,616448,0,2147483648u,50,1065353216,1065353216,0,0,0,0,0,0,0,0,0,0,
96512,0,0,1376768,1393152,23801856,2048,23818240,4,2147483648u,4590592,4554752,4,512,19175424,0,0,19079168,23732224,26787840,8749568,8847360,1753600,76800,874496,875520,584192,649216,650240,542720,584704,616960,616448,0,2147483648u,51,1065353216,0,0,0,0,0,0,0,0,0,0,0,
108800,512,0,1190336,1190336,28235776,65536,28366848,5,0,4603904,4587520,4,512,18821120,2147483648u,2147483648u,19169280,23461888,29425664,8814592,8650752,1769984,70656,876544,877568,585216,650240,544128,548864,583680,617984,616448,0,2147483648u,48,0,0,0,0,0,0,0,0,0,0,0,0,
108800,512,0,1190496,1190496,28563456,65536,28694528,5,0,4604928,4620288,4,512,18823168,2147483648u,2147483648u,19259392,23552000,29442048,8815104,8716288,1786368,72704,878592,879616,585216,650240,650752,540672,583680,617984,616448,0,2147483648u,49,0,0,0,0,0,0,0,0,0,0,0,0,
108800,512,0,885248,901632,23617536,2048,23633920,4,0,4605952,4653056,4,512,19181568,2147483648u,2147483648u,19349504,23642112,29458432,8880128,8781824,1802752,74752,880640,881664,585216,650240,544128,550912,584704,617984,616448,0,2147483648u,50,1065353216,1065353216,0,0,0,0,0,0,0,0,0,0,
108800,512,0,1376768,1393152,23801856,2048,23818240,4,2147483648u,4606976,4685824,4,512,19183616,2147483648u,2147483648u,19439616,23732224,29474816,8880640,8847360,1819136,76800,882688,883712,585216,650240,650752,542720,584704,617984,616448,0,2147483648u,51,1065353216,0,0,0,0,0,0,0,0,0,0,0,
72192,128,0,89856,89856,28839936,65536,28971008,5,0,1445888,1310720,4,640,10252288,0,0,10158080,23822336,27131904,5545984,5505024,131712,70912,671744,673024,559744,624640,545024,532480,559104,592512,591872,0,2147483648u,52,0,0,0,0,0,0,0,0,0,0,0,0,
72192,128,0,90016,90016,29167616,65536,29298688,5,0,1446912,1343488,4,640,10254336,0,0,10248192,23912448,27148288,5546496,5570560,148096,72960,673792,675072,559744,624640,625792,544768,559104,592512,591872,0,2147483648u,53,0,0,0,0,0,0,0,0,0,0,0,0,
72192,128,0,66176,82560,23879680,2048,23896064,4,0,1447936,1376256,4,640,10612736,0,0,10338304,24002560,27164672,5611520,5636096,164480,75008,675840,677120,559744,624640,545024,534528,560128,592512,591872,0,2147483648u,54,1065353216,1065353216,0,0,0,0,0,0,0,0,0,0,
72192,128,0,1114752,1131136,23875584,2048,23891968,4,0,1448960,1409024,4,640,10614784,0,0,10428416,24092672,27181056,5612032,5701632,180864,77056,677888,679168,559744,624640,625792,546816,560128,592512,591872,0,2147483648u,55,1065353216,0,0,0,0,0,0,0,0,0,0,0,
84480,640,0,133888,133888,28841984,65536,28973056,5,0,1462272,1441792,4,640,10260480,2147483648u,2147483648u,10518528,23822336,29753344,5677056,5505024,197248,70912,679936,681216,560768,625664,545152,548864,559104,593536,591872,0,2147483648u,52,0,0,0,0,0,0,0,0,0,0,0,0,
84480,640,0,134048,134048,29169664,65536,29300736,5,0,1463296,1474560,4,640,10262528,2147483648u,2147483648u,10608640,23912448,29769728,5677568,5570560,213632,72960,681984,683264,560768,625664,626304,544768,559104,593536,591872,0,2147483648u,53,0,0,0,0,0,0,0,0,0,0,0,0,
84480,640,0,98944,115328,23912448,2048,23920640,4,0,1464320,1507328,4,640,10620928,2147483648u,2147483648u,10698752,24002560,29786112,5742592,5636096,230016,75008,684032,685312,560768,625664,545152,550912,560128,593536,591872,0,2147483648u,54,1065353216,1065353216,0,0,0,0,0,0,0,0,0,0,
84480,640,0,1147520,1163904,23908352,2048,23932928,4,0,1465344,1540096,4,640,10622976,2147483648u,2147483648u,10788864,24092672,29802496,5743104,5701632,246400,77056,686080,687360,560768,625664,626304,546816,560128,593536,591872,0,2147483648u,55,1065353216,0,0,0,0,0,0,0,0,0,0,0,
96768,128,0,442112,442112,28856320,65536,28987392,5,0,2494464,2359296,4,640,13135872,0,0,13041664,23822336,27131904,6594560,6553600,656000,70912,737280,738560,567936,632832,546048,532480,567296,600704,600064,0,2147483648u,52,0,0,0,0,0,0,0,0,0,0,0,0,
96768,128,0,442272,442272,29184000,65536,29315072,5,0,2495488,2392064,4,640,13137920,0,0,13131776,23912448,27148288,6595072,6619136,672384,72960,739328,740608,567936,632832,633984,544768,567296,600704,600064,0,2147483648u,53,0,0,0,0,0,0,0,0,0,0,0,0,
96768,128,0,328320,344704,24117248,2048,24133632,4,0,2496512,2424832,4,640,13496320,0,0,13221888,24002560,27164672,6660096,6684672,688768,75008,741376,742656,567936,632832,546048,534528,568320,600704,600064,0,2147483648u,54,1065353216,1065353216,0,0,0,0,0,0,0,0,0,0,
96768,128,0,1376896,1393280,24162304,2048,24178688,4,0,2497536,2457600,4,640,13498368,0,0,13312000,24092672,27181056,6660608,6750208,705152,77056,743424,744704,567936,632832,633984,546816,568320,600704,600064,0,2147483648u,55,1065353216,0,0,0,0,0,0,0,0,0,0,0,
109056,640,0,486144,486144,28858368,65536,28989440,5,0,2510848,2490368,4,640,13144064,2147483648u,2147483648u,13402112,23822336,29753344,6725632,6553600,721536,70912,745472,746752,568960,633856,546176,548864,567296,601728,600064,0,2147483648u,52,0,0,0,0,0,0,0,0,0,0,0,0,
109056,640,0,486304,486304,29186048,65536,29317120,5,0,2511872,2523136,4,640,13146112,2147483648u,2147483648u,13492224,23912448,29769728,6726144,6619136,737920,72960,747520,748800,568960,633856,634496,544768,567296,601728,600064,0,2147483648u,53,0,0,0,0,0,0,0,0,0,0,0,0,
109056,640,0,361088,377472,24150016,2048,24166400,4,0,2512896,2555904,4,640,13504512,2147483648u,2147483648u,13582336,24002560,29786112,6791168,6684672,754304,75008,749568,750848,568960,633856,546176,550912,568320,601728,600064,0,2147483648u,54,1065353216,1065353216,0,0,0,0,0,0,0,0,0,0,
109056,640,0,1376896,1393280,24162304,2048,24178688,4,2147483648u,2513920,2588672,4,640,13506560,2147483648u,2147483648u,13672448,24092672,29802496,6791680,6750208,770688,77056,751616,752896,568960,633856,634496,546816,568320,601728,600064,0,2147483648u,55,1065353216,0,0,0,0,0,0,0,0,0,0,0,
72448,128,0,794368,794368,28872704,65536,29003776,5,0,3543040,3407872,4,640,16019456,0,0,15925248,23822336,27131904,7643136,7602176,1180288,70912,802816,804096,576128,641024,547072,532480,575488,608896,608256,0,2147483648u,52,0,0,0,0,0,0,0,0,0,0,0,0,
72448,128,0,794528,794528,29200384,65536,29331456,5,0,3544064,3440640,4,640,16021504,0,0,16015360,23912448,27148288,7643648,7667712,1196672,72960,804864,806144,576128,641024,642176,544768,575488,608896,608256,0,2147483648u,53,0,0,0,0,0,0,0,0,0,0,0,0,
72448,128,0,590464,606848,24014848,2048,24031232,4,0,3545088,3473408,4,640,16379904,0,0,16105472,24002560,27164672,7708672,7733248,1213056,75008,806912,808192,576128,641024,547072,534528,576512,608896,608256,0,2147483648u,54,1065353216,1065353216,0,0,0,0,0,0,0,0,0,0,
72448,128,0,1376896,1393280,24162304,2048,24178688,4,2147483648u,3546112,3506176,4,640,16381952,0,0,16195584,24092672,27181056,7709184,7798784,1229440,77056,808960,810240,576128,641024,642176,546816,576512,608896,608256,0,2147483648u,55,1065353216,0,0,0,0,0,0,0,0,0,0,0,
84736,640,0,838400,838400,28874752,65536,29005824,5,0,3559424,3538944,4,640,16027648,2147483648u,2147483648u,16285696,23822336,29753344,7774208,7602176,1245824,70912,811008,812288,577152,642048,547200,548864,575488,609920,608256,0,2147483648u,52,0,0,0,0,0,0,0,0,0,0,0,0,
84736,640,0,838560,838560,29202432,65536,29333504,5,0,3560448,3571712,4,640,16029696,2147483648u,2147483648u,16375808,23912448,29769728,7774720,7667712,1262208,72960,813056,814336,577152,642048,642688,544768,575488,609920,608256,0,2147483648u,53,0,0,0,0,0,0,0,0,0,0,0,0,
84736,640,0,623232,639616,24047616,2048,24064000,4,0,3561472,3604480,4,640,16388096,2147483648u,2147483648u,16465920,24002560,29786112,7839744,7733248,1278592,75008,815104,816384,577152,642048,547200,550912,576512,609920,608256,0,2147483648u,54,1065353216,1065353216,0,0,0,0,0,0,0,0,0,0,
84736,640,0,1376896,1393280,24162304,2048,24178688,4,2147483648u,3562496,3637248,4,640,16390144,2147483648u,2147483648u,16556032,24092672,29802496,7840256,7798784,1294976,77056,817152,818432,577152,642048,642688,546816,576512,609920,608256,0,2147483648u,55,1065353216,0,0,0,0,0,0,0,0,0,0,0,
97024,128,0,1146624,1146624,28889088,65536,29020160,5,0,4591616,4456448,4,640,18903040,0,0,18808832,23822336,27131904,8691712,8650752,1704576,70912,868352,869632,584320,649216,548096,532480,583680,617088,616448,0,2147483648u,52,0,0,0,0,0,0,0,0,0,0,0,0,
97024,128,0,1146784,1146784,29216768,65536,29347840,5,0,4592640,4489216,4,640,18905088,0,0,18898944,23912448,27148288,8692224,8716288,1720960,72960,870400,871680,584320,649216,650368,544768,583680,617088,616448,0,2147483648u,53,0,0,0,0,0,0,0,0,0,0,0,0,
97024,128,0,852608,868992,23945216,2048,23961600,4,0,4593664,4521984,4,640,19263488,0,0,18989056,24002560,27164672,8757248,8781824,1737344,75008,872448,873728,584320,649216,548096,534528,584704,617088,616448,0,2147483648u,54,1065353216,1065353216,0,0,0,0,0,0,0,0,0,0,
97024,128,0,1376896,1393280,24162304,2048,24178688,4,2147483648u,4594688,4554752,4,640,19265536,0,0,19079168,24092672,27181056,8757760,8847360,1753728,77056,874496,875776,584320,649216,650368,546816,584704,617088,616448,0,2147483648u,55,1065353216,0,0,0,0,0,0,0,0,0,0,0,
109312,640,0,1190656,1190656,28891136,65536,29022208,5,0,4608000,4587520,4,640,18911232,2147483648u,2147483648u,19169280,23822336,29753344,8822784,8650752,1770112,70912,876544,877824,585344,650240,548224,548864,583680,618112,616448,0,2147483648u,52,0,0,0,0,0,0,0,0,0,0,0,0,
109312,640,0,1190816,1190816,29218816,65536,29349888,5,0,4609024,4620288,4,640,18913280,2147483648u,2147483648u,19259392,23912448,29769728,8823296,8716288,1786496,72960,878592,879872,585344,650240,650880,544768,583680,618112,616448,0,2147483648u,53,0,0,0,0,0,0,0,0,0,0,0,0,
109312,640,0,885376,901760,23977984,2048,23994368,4,0,4610048,4653056,4,640,19271680,2147483648u,2147483648u,19349504,24002560,29786112,8888320,8781824,1802880,75008,880640,881920,585344,650240,548224,550912,584704,618112,616448,0,2147483648u,54,1065353216,1065353216,0,0,0,0,0,0,0,0,0,0,
109312,640,0,1376896,1393280,24162304,2048,24178688,4,2147483648u,4611072,4685824,4,640,19273728,2147483648u,2147483648u,19439616,24092672,29802496,8888832,8847360,1819264,77056,882688,883968,585344,650240,650880,546816,584704,618112,616448,0,2147483648u,55,1065353216,0,0,0,0,0,0,0,0,0,0,0,
72704,256,0,90176,90176,29495296,65536,29626368,5,0,1449984,1310720,4,768,10342400,2147483648u,2147483648u,10158080,24182784,27459584,5554176,5505024,131840,71168,671744,673280,559872,624640,549120,536576,559104,592640,591872,0,2147483648u,56,0,0,0,0,0,0,0,0,0,0,0,0,
72704,256,0,90336,90336,29822976,65536,29954048,5,0,1451008,1343488,4,768,10344448,2147483648u,2147483648u,10248192,24272896,27475968,5554688,5570560,148224,73216,673792,675328,559872,624640,625920,548864,559104,592640,591872,0,2147483648u,57,0,0,0,0,0,0,0,0,0,0,0,0,
72704,256,0,66304,82688,24240128,2048,24256512,4,0,1452032,1376256,4,768,10702848,2147483648u,2147483648u,10338304,24363008,27492352,5619712,5636096,164608,75264,675840,677376,559872,624640,549120,538624,560128,592640,591872,0,2147483648u,58,1065353216,1065353216,0,0,0,0,0,0,0,0,0,0,
72704,256,0,1114880,1131264,24236032,2048,24252416,4,0,1453056,1409024,4,768,10704896,2147483648u,2147483648u,10428416,24453120,27508736,5620224,5701632,180992,77312,677888,679424,559872,624640,625920,550912,560128,592640,591872,0,2147483648u,59,1065353216,0,0,0,0,0,0,0,0,0,0,0,
84992,768,0,134208,134208,29497344,65536,29628416,5,0,1466368,1441792,4,768,10350592,2147483648u,2147483648u,10518528,24182784,30081024,5685248,5505024,197376,71168,679936,681472,560896,625664,549248,552960,559104,593664,591872,0,2147483648u,56,0,0,0,0,0,0,0,0,0,0,0,0,
84992,768,0,134368,134368,29825024,65536,29956096,5,0,1467392,1474560,4,768,10352640,2147483648u,2147483648u,10608640,24272896,30097408,5685760,5570560,213760,73216,681984,683520,560896,625664,626432,548864,559104,593664,591872,0,2147483648u,57,0,0,0,0,0,0,0,0,0,0,0,0,
84992,768,0,99072,115456,24272896,2048,24281088,4,0,1468416,1507328,4,768,10711040,2147483648u,2147483648u,10698752,24363008,30113792,5750784,5636096,230144,75264,684032,685568,560896,625664,549248,555008,560128,593664,591872,0,2147483648u,58,1065353216,1065353216,0,0,0,0,0,0,0,0,0,0,
84992,768,0,1147648,1164032,24268800,2048,24293376,4,0,1469440,1540096,4,768,10713088,2147483648u,2147483648u,10788864,24453120,30130176,5751296,5701632,246528,77312,686080,687616,560896,625664,626432,550912,560128,593664,591872,0,2147483648u,59,1065353216,0,0,0,0,0,0,0,0,0,0,0,
97280,256,0,442432,442432,29511680,65536,29642752,5,0,2498560,2359296,4,768,13225984,2147483648u,2147483648u,13041664,24182784,27459584,6602752,6553600,656128,71168,737280,738816,568064,632832,550144,536576,567296,600832,600064,0,2147483648u,56,0,0,0,0,0,0,0,0,0,0,0,0,
97280,256,0,442592,442592,29839360,65536,29970432,5,0,2499584,2392064,4,768,13228032,2147483648u,2147483648u,13131776,24272896,27475968,6603264,6619136,672512,73216,739328,740864,568064,632832,634112,548864,567296,600832,600064,0,2147483648u,57,0,0,0,0,0,0,0,0,0,0,0,0,
97280,256,0,328448,344832,24477696,2048,24494080,4,0,2500608,2424832,4,768,13586432,2147483648u,2147483648u,13221888,24363008,27492352,6668288,6684672,688896,75264,741376,742912,568064,632832,550144,538624,568320,600832,600064,0,2147483648u,58,1065353216,1065353216,0,0,0,0,0,0,0,0,0,0,
97280,256,0,1377024,1393408,24522752,2048,24539136,4,0,2501632,2457600,4,768,13588480,2147483648u,2147483648u,13312000,24453120,27508736,6668800,6750208,705280,77312,743424,744960,568064,632832,634112,550912,568320,600832,600064,0,2147483648u,59,1065353216,0,0,0,0,0,0,0,0,0,0,0,
109568,768,0,486464,486464,29513728,65536,29644800,5,0,2514944,2490368,4,768,13234176,2147483648u,2147483648u,13402112,24182784,30081024,6733824,6553600,721664,71168,745472,747008,569088,633856,550272,552960,567296,601856,600064,0,2147483648u,56,0,0,0,0,0,0,0,0,0,0,0,0,
109568,768,0,486624,486624,29841408,65536,29972480,5,0,2515968,2523136,4,768,13236224,2147483648u,2147483648u,13492224,24272896,30097408,6734336,6619136,738048,73216,747520,749056,569088,633856,634624,548864,567296,601856,600064,0,2147483648u,57,0,0,0,0,0,0,0,0,0,0,0,0,
109568,768,0,361216,377600,24510464,2048,24526848,4,0,2516992,2555904,4,768,13594624,2147483648u,2147483648u,13582336,24363008,30113792,6799360,6684672,754432,75264,749568,751104,569088,633856,550272,555008,568320,601856,600064,0,2147483648u,58,1065353216,1065353216,0,0,0,0,0,0,0,0,0,0,
109568,768,0,1377024,1393408,24522752,2048,24539136,4,2147483648u,2518016,2588672,4,768,13596672,2147483648u,2147483648u,13672448,24453120,30130176,6799872,6750208,770816,77312,751616,753152,569088,633856,634624,550912,568320,601856,600064,0,2147483648u,59,1065353216,0,0,0,0,0,0,0,0,0,0,0,
72960,256,0,794688,794688,29528064,65536,29659136,5,0,3547136,3407872,4,768,16109568,2147483648u,2147483648u,15925248,24182784,27459584,7651328,7602176,1180416,71168,802816,804352,576256,641024,551168,536576,575488,609024,608256,0,2147483648u,56,0,0,0,0,0,0,0,0,0,0,0,0,
72960,256,0,794848,794848,29855744,65536,29986816,5,0,3548160,3440640,4,768,16111616,2147483648u,2147483648u,16015360,24272896,27475968,7651840,7667712,1196800,73216,804864,806400,576256,641024,642304,548864,575488,609024,608256,0,2147483648u,57,0,0,0,0,0,0,0,0,0,0,0,0,
72960,256,0,590592,606976,24375296,2048,24391680,4,0,3549184,3473408,4,768,16470016,2147483648u,2147483648u,16105472,24363008,27492352,7716864,7733248,1213184,75264,806912,808448,576256,641024,551168,538624,576512,609024,608256,0,2147483648u,58,1065353216,1065353216,0,0,0,0,0,0,0,0,0,0,
72960,256,0,1377024,1393408,24522752,2048,24539136,4,2147483648u,3550208,3506176,4,768,16472064,2147483648u,2147483648u,16195584,24453120,27508736,7717376,7798784,1229568,77312,808960,810496,576256,641024,642304,550912,576512,609024,608256,0,2147483648u,59,1065353216,0,0,0,0,0,0,0,0,0,0,0,
85248,768,0,838720,838720,29530112,65536,29661184,5,0,3563520,3538944,4,768,16117760,2147483648u,2147483648u,16285696,24182784,30081024,7782400,7602176,1245952,71168,811008,812544,577280,642048,551296,552960,575488,610048,608256,0,2147483648u,56,0,0,0,0,0,0,0,0,0,0,0,0,
85248,768,0,838880,838880,29857792,65536,29988864,5,0,3564544,3571712,4,768,16119808,2147483648u,2147483648u,16375808,24272896,30097408,7782912,7667712,1262336,73216,813056,814592,577280,642048,642816,548864,575488,610048,608256,0,2147483648u,57,0,0,0,0,0,0,0,0,0,0,0,0,
85248,768,0,623360,639744,24408064,2048,24424448,4,0,3565568,3604480,4,768,16478208,2147483648u,2147483648u,16465920,24363008,30113792,7847936,7733248,1278720,75264,815104,816640,577280,642048,551296,555008,576512,610048,608256,0,2147483648u,58,1065353216,1065353216,0,0,0,0,0,0,0,0,0,0,
85248,768,0,1377024,1393408,24522752,2048,24539136,4,2147483648u,3566592,3637248,4,768,16480256,2147483648u,2147483648u,16556032,24453120,30130176,7848448,7798784,1295104,77312,817152,818688,577280,642048,642816,550912,576512,610048,608256,0,2147483648u,59,1065353216,0,0,0,0,0,0,0,0,0,0,0,
97536,256,0,1146944,1146944,29544448,65536,29675520,5,0,4595712,4456448,4,768,18993152,2147483648u,2147483648u,18808832,24182784,27459584,8699904,8650752,1704704,71168,868352,869888,584448,649216,552192,536576,583680,617216,616448,0,2147483648u,56,0,0,0,0,0,0,0,0,0,0,0,0,
97536,256,0,1147104,1147104,29872128,65536,30003200,5,0,4596736,4489216,4,768,18995200,2147483648u,2147483648u,18898944,24272896,27475968,8700416,8716288,1721088,73216,870400,871936,584448,649216,650496,548864,583680,617216,616448,0,2147483648u,57,0,0,0,0,0,0,0,0,0,0,0,0,
97536,256,0,852736,869120,24305664,2048,24322048,4,0,4597760,4521984,4,768,19353600,2147483648u,2147483648u,18989056,24363008,27492352,8765440,8781824,1737472,75264,872448,873984,584448,649216,552192,538624,584704,617216,616448,0,2147483648u,58,1065353216,1065353216,0,0,0,0,0,0,0,0,0,0,
97536,256,0,1377024,1393408,24522752,2048,24539136,4,2147483648u,4598784,4554752,4,768,19355648,2147483648u,2147483648u,19079168,24453120,27508736,8765952,8847360,1753856,77312,874496,876032,584448,649216,650496,550912,584704,617216,616448,0,2147483648u,59,1065353216,0,0,0,0,0,0,0,0,0,0,0,
109824,768,0,1190976,1190976,29546496,65536,29677568,5,0,4612096,4587520,4,768,19001344,2147483648u,2147483648u,19169280,24182784,30081024,8830976,8650752,1770240,71168,876544,878080,585472,650240,552320,552960,583680,618240,616448,0,2147483648u,56,0,0,0,0,0,0,0,0,0,0,0,0,
109824,768,0,1191136,1191136,29874176,65536,30005248,5,0,4613120,4620288,4,768,19003392,2147483648u,2147483648u,19259392,24272896,30097408,8831488,8716288,1786624,73216,878592,880128,585472,650240,651008,548864,583680,618240,616448,0,2147483648u,57,0,0,0,0,0,0,0,0,0,0,0,0,
109824,768,0,885504,901888,24338432,2048,24354816,4,0,4614144,4653056,4,768,19361792,2147483648u,2147483648u,19349504,24363008,30113792,8896512,8781824,1803008,75264,880640,882176,585472,650240,552320,555008,584704,618240,616448,0,2147483648u,58,1065353216,1065353216,0,0,0,0,0,0,0,0,0,0,
109824,768,0,1377024,1393408,24522752,2048,24539136,4,2147483648u,4615168,4685824,4,768,19363840,2147483648u,2147483648u,19439616,24453120,30130176,8897024,8847360,1819392,77312,882688,884224,585472,650240,651008,550912,584704,618240,616448,0,2147483648u,59,1065353216,0,0,0,0,0,0,0,0,0,0,0,
73216,384,0,90496,90496,30150656,65536,30281728,5,0,1454080,1310720,4,896,10432512,2147483648u,2147483648u,10158080,24543232,27787264,5562368,5505024,131968,71424,671744,673536,560000,624640,553216,536576,559104,592768,591872,0,2147483648u,60,0,0,0,0,0,0,0,0,0,0,0,0,
73216,384,0,90656,90656,30478336,65536,30609408,5,0,1455104,1343488,4,896,10434560,2147483648u,2147483648u,10248192,24633344,27803648,5562880,5570560,148352,73472,673792,675584,560000,624640,626048,552960,559104,592768,591872,0,2147483648u,61,0,0,0,0,0,0,0,0,0,0,0,0,
73216,384,0,66432,82816,24600576,2048,24616960,4,0,1456128,1376256,4,896,10792960,2147483648u,2147483648u,10338304,24723456,27820032,5627904,5636096,164736,75520,675840,677632,560000,624640,553216,538624,560128,592768,591872,0,2147483648u,62,1065353216,1065353216,0,0,0,0,0,0,0,0,0,0,
73216,384,0,1115008,1131392,24596480,2048,24612864,4,0,1457152,1409024,4,896,10795008,2147483648u,2147483648u,10428416,24813568,27836416,5628416,5701632,181120,77568,677888,679680,560000,624640,626048,555008,560128,592768,591872,0,2147483648u,63,1065353216,0,0,0,0,0,0,0,0,0,0,0,
85504,896,0,134528,134528,30152704,65536,30283776,5,0,1470464,1441792,4,896,10440704,2147483648u,2147483648u,10518528,24543232,30408704,5693440,5505024,197504,71424,679936,681728,561024,625664,553344,552960,559104,593792,591872,0,2147483648u,60,0,0,0,0,0,0,0,0,0,0,0,0,
85504,896,0,134688,134688,30480384,65536,30611456,5,0,1471488,1474560,4,896,10442752,2147483648u,2147483648u,10608640,24633344,30425088,5693952,5570560,213888,73472,681984,683776,561024,625664,626560,552960,559104,593792,591872,0,2147483648u,61,0,0,0,0,0,0,0,0,0,0,0,0,
85504,896,0,99200,115584,24633344,2048,24641536,4,0,1472512,1507328,4,896,10801152,2147483648u,2147483648u,10698752,24723456,30441472,5758976,5636096,230272,75520,684032,685824,561024,625664,553344,555008,560128,593792,591872,0,2147483648u,62,1065353216,1065353216,0,0,0,0,0,0,0,0,0,0,
85504,896,0,1147776,1164160,24629248,2048,24653824,4,0,1473536,1540096,4,896,10803200,2147483648u,2147483648u,10788864,24813568,30457856,5759488,5701632,246656,77568,686080,687872,561024,625664,626560,555008,560128,593792,591872,0,2147483648u,63,1065353216,0,0,0,0,0,0,0,0,0,0,0,
97792,384,0,442752,442752,30167040,65536,30298112,5,0,2502656,2359296,4,896,13316096,2147483648u,2147483648u,13041664,24543232,27787264,6610944,6553600,656256,71424,737280,739072,568192,632832,554240,536576,567296,600960,600064,0,2147483648u,60,0,0,0,0,0,0,0,0,0,0,0,0,
97792,384,0,442912,442912,30494720,65536,30625792,5,0,2503680,2392064,4,896,13318144,2147483648u,2147483648u,13131776,24633344,27803648,6611456,6619136,672640,73472,739328,741120,568192,632832,634240,552960,567296,600960,600064,0,2147483648u,61,0,0,0,0,0,0,0,0,0,0,0,0,
97792,384,0,328576,344960,24838144,2048,24854528,4,0,2504704,2424832,4,896,13676544,2147483648u,2147483648u,13221888,24723456,27820032,6676480,6684672,689024,75520,741376,743168,568192,632832,554240,538624,568320,600960,600064,0,2147483648u,62,1065353216,1065353216,0,0,0,0,0,0,0,0,0,0,
97792,384,0,1377152,1393536,24883200,2048,24899584,4,0,2505728,2457600,4,896,13678592,2147483648u,2147483648u,13312000,24813568,27836416,6676992,6750208,705408,77568,743424,745216,568192,632832,634240,555008,568320,600960,600064,0,2147483648u,63,1065353216,0,0,0,0,0,0,0,0,0,0,0,
110080,896,0,486784,486784,30169088,65536,30300160,5,0,2519040,2490368,4,896,13324288,2147483648u,2147483648u,13402112,24543232,30408704,6742016,6553600,721792,71424,745472,747264,569216,633856,554368,552960,567296,601984,600064,0,2147483648u,60,0,0,0,0,0,0,0,0,0,0,0,0,
110080,896,0,486944,486944,30496768,65536,30627840,5,0,2520064,2523136,4,896,13326336,2147483648u,2147483648u,13492224,24633344,30425088,6742528,6619136,738176,73472,747520,749312,569216,633856,634752,552960,567296,601984,600064,0,2147483648u,61,0,0,0,0,0,0,0,0,0,0,0,0,
110080,896,0,361344,377728,24870912,2048,24887296,4,0,2521088,2555904,4,896,13684736,2147483648u,2147483648u,13582336,24723456,30441472,6807552,6684672,754560,75520,749568,751360,569216,633856,554368,555008,568320,601984,600064,0,2147483648u,62,1065353216,1065353216,0,0,0,0,0,0,0,0,0,0,
110080,896,0,1377152,1393536,24883200,2048,24899584,4,2147483648u,2522112,2588672,4,896,13686784,2147483648u,2147483648u,13672448,24813568,30457856,6808064,6750208,770944,77568,751616,753408,569216,633856,634752,555008,568320,601984,600064,0,2147483648u,63,1065353216,0,0,0,0,0,0,0,0,0,0,0,
73472,384,0,795008,795008,30183424,65536,30314496,5,0,3551232,3407872,4,896,16199680,2147483648u,2147483648u,15925248,24543232,27787264,7659520,7602176,1180544,71424,802816,804608,576384,641024,555264,536576,575488,609152,608256,0,2147483648u,60,0,0,0,0,0,0,0,0,0,0,0,0,
73472,384,0,795168,795168,30511104,65536,30642176,5,0,3552256,3440640,4,896,16201728,2147483648u,2147483648u,16015360,24633344,27803648,7660032,7667712,1196928,73472,804864,806656,576384,641024,642432,552960,575488,609152,608256,0,2147483648u,61,0,0,0,0,0,0,0,0,0,0,0,0,
73472,384,0,590720,607104,24735744,2048,24752128,4,0,3553280,3473408,4,896,16560128,2147483648u,2147483648u,16105472,24723456,27820032,7725056,7733248,1213312,75520,806912,808704,576384,641024,555264,538624,576512,609152,608256,0,2147483648u,62,1065353216,1065353216,0,0,0,0,0,0,0,0,0,0,
73472,384,0,1377152,1393536,24883200,2048,24899584,4,2147483648u,3554304,3506176,4,896,16562176,2147483648u,2147483648u,16195584,24813568,27836416,7725568,7798784,1229696,77568,808960,810752,576384,641024,642432,555008,576512,609152,608256,0,2147483648u,63,1065353216,0,0,0,0,0,0,0,0,0,0,0,
85760,896,0,839040,839040,30185472,65536,30316544,5,0,3567616,3538944,4,896,16207872,2147483648u,2147483648u,16285696,24543232,30408704,7790592,7602176,1246080,71424,811008,812800,577408,642048,555392,552960,575488,610176,608256,0,2147483648u,60,0,0,0,0,0,0,0,0,0,0,0,0,
85760,896,0,839200,839200,30513152,65536,30644224,5,0,3568640,3571712,4,896,16209920,2147483648u,2147483648u,16375808,24633344,30425088,7791104,7667712,1262464,73472,813056,814848,577408,642048,642944,552960,575488,610176,608256,0,2147483648u,61,0,0,0,0,0,0,0,0,0,0,0,0,
85760,896,0,623488,639872,24768512,2048,24784896,4,0,3569664,3604480,4,896,16568320,2147483648u,2147483648u,16465920,24723456,30441472,7856128,7733248,1278848,75520,815104,816896,577408,642048,555392,555008,576512,610176,608256,0,2147483648u,62,1065353216,1065353216,0,0,0,0,0,0,0,0,0,0,
85760,896,0,1377152,1393536,24883200,2048,24899584,4,2147483648u,3570688,3637248,4,896,16570368,2147483648u,2147483648u,16556032,24813568,30457856,7856640,7798784,1295232,77568,817152,818944,577408,642048,642944,555008,576512,610176,608256,0,2147483648u,63,1065353216,0,0,0,0,0,0,0,0,0,0,0,
98048,384,0,1147264,1147264,30199808,65536,30330880,5,0,4599808,4456448,4,896,19083264,2147483648u,2147483648u,18808832,24543232,27787264,8708096,8650752,1704832,71424,868352,870144,584576,649216,556288,536576,583680,617344,616448,0,2147483648u,60,0,0,0,0,0,0,0,0,0,0,0,0,
98048,384,0,1147424,1147424,30527488,65536,30658560,5,0,4600832,4489216,4,896,19085312,2147483648u,2147483648u,18898944,24633344,27803648,8708608,8716288,1721216,73472,870400,872192,584576,649216,650624,552960,583680,617344,616448,0,2147483648u,61,0,0,0,0,0,0,0,0,0,0,0,0,
98048,384,0,852864,869248,24666112,2048,24682496,4,0,4601856,4521984,4,896,19443712,2147483648u,2147483648u,18989056,24723456,27820032,8773632,8781824,1737600,75520,872448,874240,584576,649216,556288,538624,584704,617344,616448,0,2147483648u,62,1065353216,1065353216,0,0,0,0,0,0,0,0,0,0,
98048,384,0,1377152,1393536,24883200,2048,24899584,4,2147483648u,4602880,4554752,4,896,19445760,2147483648u,2147483648u,19079168,24813568,27836416,8774144,8847360,1753984,77568,874496,876288,584576,649216,650624,555008,584704,617344,616448,0,2147483648u,63,1065353216,0,0,0,0,0,0,0,0,0,0,0,
110336,896,0,1191296,1191296,30201856,65536,30332928,5,0,4616192,4587520,4,896,19091456,2147483648u,2147483648u,19169280,24543232,30408704,8839168,8650752,1770368,71424,876544,878336,585600,650240,556416,552960,583680,618368,616448,0,2147483648u,60,0,0,0,0,0,0,0,0,0,0,0,0,
110336,896,0,1191456,1191456,30529536,65536,30660608,5,0,4617216,4620288,4,896,19093504,2147483648u,2147483648u,19259392,24633344,30425088,8839680,8716288,1786752,73472,878592,880384,585600,650240,651136,552960,583680,618368,616448,0,2147483648u,61,0,0,0,0,0,0,0,0,0,0,0,0,
110336,896,0,885632,902016,24698880,2048,24715264,4,0,4618240,4653056,4,896,19451904,2147483648u,2147483648u,19349504,24723456,30441472,8904704,8781824,1803136,75520,880640,882432,585600,650240,556416,555008,584704,618368,616448,0,2147483648u,62,1065353216,1065353216,0,0,0,0,0,0,0,0,0,0,
110336,896,0,1377152,1393536,24883200,2048,24899584,4,2147483648u,4619264,4685824,4,896,19453952,2147483648u,2147483648u,19439616,24813568,30457856,8905216,8847360,1819520,77568,882688,884480,585600,650240,651136,555008,584704,618368,616448,0,2147483648u,63,1065353216,0,0,0,0,0,0,0,0,0,0,0,
73728,0,0,176128,176128,25174016,65536,25305088,6,0,1572864,1572864,8,0,10878976,0,0,10878976,22020096,25165824,5767168,5767168,262144,69632,688128,688128,561152,626688,524800,524288,561152,593920,593920,0,2147483648u,64,0,0,0,0,0,0,0,0,0,0,0,0,
73728,0,0,176320,176320,25567232,65536,25698304,6,0,1573888,1605632,8,0,10881024,0,0,10969088,22110208,25182208,5767680,5832704,278528,71680,690176,690176,561152,626688,626688,524288,561152,593920,593920,0,2147483648u,65,0,0,0,0,0,0,0,0,0,0,0,0,
73728,0,0,131072,147456,22134784,2048,22151168,4,0,1574912,1638400,8,0,11239424,0,0,11059200,22200320,25198592,5832704,5898240,294912,73728,692224,692224,561152,626688,524800,526336,562176,593920,593920,0,2147483648u,66,1065353216,1065353216,0,0,0,0,0,0,0,0,0,0,
73728,0,0,1179648,1196032,22147072,2048,22163456,4,0,1575936,1671168,8,0,11241472,0,0,11149312,22290432,25214976,5833216,5963776,311296,75776,694272,694272,561152,626688,626688,526336,562176,593920,593920,0,2147483648u,67,1065353216,0,0,0,0,0,0,0,0,0,0,0,
86016,512,0,220160,220160,25176064,65536,25307136,6,0,1589248,1703936,8,0,10887168,2147483648u,2147483648u,11239424,22020096,28114944,5898240,5767168,327680,69632,696320,696320,562176,627712,524928,540672,561152,594944,593920,0,2147483648u,64,0,0,0,0,0,0,0,0,0,0,0,0,
86016,512,0,220352,220352,25569280,65536,25700352,6,0,1590272,1736704,8,0,10889216,2147483648u,2147483648u,11329536,22110208,28131328,5898752,5832704,344064,71680,698368,698368,562176,627712,627200,524288,561152,594944,593920,0,2147483648u,65,0,0,0,0,0,0,0,0,0,0,0,0,
86016,512,0,163840,180224,22167552,2048,22183936,4,0,1591296,1769472,8,0,11247616,2147483648u,2147483648u,11419648,22200320,28147712,5963776,5898240,360448,73728,700416,700416,562176,627712,524928,542720,562176,594944,593920,0,2147483648u,66,1065353216,1065353216,0,0,0,0,0,0,0,0,0,0,
86016,512,0,1212416,1228800,22179840,2048,22196224,4,0,1592320,1802240,8,0,11249664,2147483648u,2147483648u,11509760,22290432,28164096,5964288,5963776,376832,75776,702464,702464,562176,627712,627200,526336,562176,594944,593920,0,2147483648u,67,1065353216,0,0,0,0,0,0,0,0,0,0,0,
98304,0,0,528384,528384,25190400,65536,25321472,6,0,2621440,2621440,8,0,13762560,0,0,13762560,22020096,25165824,6815744,6815744,786432,69632,753664,753664,569344,634880,525824,524288,569344,602112,602112,0,2147483648u,64,0,0,0,0,0,0,0,0,0,0,0,0,
98304,0,0,528576,528576,25583616,65536,25714688,6,0,2622464,2654208,8,0,13764608,0,0,13852672,22110208,25182208,6816256,6881280,802816,71680,755712,755712,569344,634880,634880,524288,569344,602112,602112,0,2147483648u,65,0,0,0,0,0,0,0,0,0,0,0,0,
98304,0,0,393216,409600,22024192,2048,22032384,4,0,2623488,2686976,8,0,14123008,0,0,13942784,22200320,25198592,6881280,6946816,819200,73728,757760,757760,569344,634880,525824,526336,570368,602112,602112,0,2147483648u,66,1065353216,1065353216,0,0,0,0,0,0,0,0,0,0,
98304,0,0,1376256,1392640,22360064,2048,22376448,4,2147483648u,2624512,2719744,8,0,14125056,0,0,14032896,22290432,25214976,6881792,7012352,835584,75776,759808,759808,569344,634880,634880,526336,570368,602112,602112,0,2147483648u,67,1065353216,0,0,0,0,0,0,0,0,0,0,0,
110592,512,0,572416,572416,25192448,65536,25323520,6,0,2637824,2752512,8,0,13770752,2147483648u,2147483648u,14123008,22020096,28114944,6946816,6815744,851968,69632,761856,761856,570368,635904,525952,540672,569344,603136,602112,0,2147483648u,64,0,0,0,0,0,0,0,0,0,0,0,0,
110592,512,0,572608,572608,25585664,65536,25716736,6,0,2638848,2785280,8,0,13772800,2147483648u,2147483648u,14213120,22110208,28131328,6947328,6881280,868352,71680,763904,763904,570368,635904,635392,524288,569344,603136,602112,0,2147483648u,65,0,0,0,0,0,0,0,0,0,0,0,0,
110592,512,0,425984,442368,22048768,2048,22065152,4,0,2639872,2818048,8,0,14131200,2147483648u,2147483648u,14303232,22200320,28147712,7012352,6946816,884736,73728,765952,765952,570368,635904,525952,542720,570368,603136,602112,0,2147483648u,66,1065353216,1065353216,0,0,0,0,0,0,0,0,0,0,
110592,512,0,1376256,1392640,22360064,2048,22376448,4,2147483648u,2640896,2850816,8,0,14133248,2147483648u,2147483648u,14393344,22290432,28164096,7012864,7012352,901120,75776,768000,768000,570368,635904,635392,526336,570368,603136,602112,0,2147483648u,67,1065353216,0,0,0,0,0,0,0,0,0,0,0,
73984,0,0,880640,880640,25206784,65536,25337856,6,0,3670016,3670016,8,0,16646144,0,0,16646144,22020096,25165824,7864320,7864320,1310720,69632,819200,819200,577536,643072,526848,524288,577536,610304,610304,0,2147483648u,64,0,0,0,0,0,0,0,0,0,0,0,0,
73984,0,0,880832,880832,25600000,65536,25731072,6,0,3671040,3702784,8,0,16648192,0,0,16736256,22110208,25182208,7864832,7929856,1327104,71680,821248,821248,577536,643072,643072,524288,577536,610304,610304,0,2147483648u,65,0,0,0,0,0,0,0,0,0,0,0,0,
73984,0,0,655360,671744,22278144,2048,22302720,4,0,3672064,3735552,8,0,17006592,0,0,16826368,22200320,25198592,7929856,7995392,1343488,73728,823296,823296,577536,643072,526848,526336,578560,610304,610304,0,2147483648u,66,1065353216,1065353216,0,0,0,0,0,0,0,0,0,0,
73984,0,0,1376256,1392640,22360064,2048,22376448,4,2147483648u,3673088,3768320,8,0,17008640,0,0,16916480,22290432,25214976,7930368,8060928,1359872,75776,825344,825344,577536,643072,643072,526336,578560,610304,610304,0,2147483648u,67,1065353216,0,0,0,0,0,0,0,0,0,0,0,
86272,512,0,924672,924672,25208832,65536,25339904,6,0,3686400,3801088,8,0,16654336,2147483648u,2147483648u,17006592,22020096,28114944,7995392,7864320,1376256,69632,827392,827392,578560,644096,526976,540672,577536,611328,610304,0,2147483648u,64,0,0,0,0,0,0,0,0,0,0,0,0,
86272,512,0,924864,924864,25602048,65536,25733120,6,0,3687424,3833856,8,0,16656384,2147483648u,2147483648u,17096704,22110208,28131328,7995904,7929856,1392640,71680,829440,829440,578560,644096,643584,524288,577536,611328,610304,0,2147483648u,65,0,0,0,0,0,0,0,0,0,0,0,0,
86272,512,0,688128,704512,22319104,2048,22335488,4,0,3688448,3866624,8,0,17014784,2147483648u,2147483648u,17186816,22200320,28147712,8060928,7995392,1409024,73728,831488,831488,578560,644096,526976,542720,578560,611328,610304,0,2147483648u,66,1065353216,1065353216,0,0,0,0,0,0,0,0,0,0,
86272,512,0,1376256,1392640,22360064,2048,22376448,4,2147483648u,3689472,3899392,8,0,17016832,2147483648u,2147483648u,17276928,22290432,28164096,8061440,8060928,1425408,75776,833536,833536,578560,644096,643584,526336,578560,611328,610304,0,2147483648u,67,1065353216,0,0,0,0,0,0,0,0,0,0,0,
98560,0,0,1232896,1232896,25223168,65536,25354240,6,0,4718592,4718592,8,0,19529728,0,0,19529728,22020096,25165824,8912896,8912896,1835008,69632,884736,884736,585728,651264,527872,524288,585728,618496,618496,0,2147483648u,64,0,0,0,0,0,0,0,0,0,0,0,0,
98560,0,0,1233088,1233088,25616384,65536,25747456,6,0,4719616,4751360,8,0,19531776,0,0,19619840,22110208,25182208,8913408,8978432,1851392,71680,886784,886784,585728,651264,651264,524288,585728,618496,618496,0,2147483648u,65,0,0,0,0,0,0,0,0,0,0,0,0,
98560,0,0,917504,933888,22216704,2048,22233088,4,0,4720640,4784128,8,0,19890176,0,0,19709952,22200320,25198592,8978432,9043968,1867776,73728,888832,888832,585728,651264,527872,526336,586752,618496,618496,0,2147483648u,66,1065353216,1065353216,0,0,0,0,0,0,0,0,0,0,
98560,0,0,1376256,1392640,22360064,2048,22376448,4,2147483648u,4721664,4816896,8,0,19892224,0,0,19800064,22290432,25214976,8978944,9109504,1884160,75776,890880,890880,585728,651264,651264,526336,586752,618496,618496,0,2147483648u,67,1065353216,0,0,0,0,0,0,0,0,0,0,0,
110848,512,0,1276928,1276928,25225216,65536,25356288,6,0,4734976,4849664,8,0,19537920,2147483648u,2147483648u,19890176,22020096,28114944,9043968,8912896,1900544,69632,892928,892928,586752,652288,528000,540672,585728,619520,618496,0,2147483648u,64,0,0,0,0,0,0,0,0,0,0,0,0,
110848,512,0,1277120,1277120,25618432,65536,25749504,6,0,4736000,4882432,8,0,19539968,2147483648u,2147483648u,19980288,22110208,28131328,9044480,8978432,1916928,71680,894976,894976,586752,652288,651776,524288,585728,619520,618496,0,2147483648u,65,0,0,0,0,0,0,0,0,0,0,0,0,
110848,512,0,950272,966656,22249472,2048,22265856,4,0,4737024,4915200,8,0,19898368,2147483648u,2147483648u,20070400,22200320,28147712,9109504,9043968,1933312,73728,897024,897024,586752,652288,528000,542720,586752,619520,618496,0,2147483648u,66,1065353216,1065353216,0,0,0,0,0,0,0,0,0,0,
110848,512,0,1376256,1392640,22360064,2048,22376448,4,2147483648u,4738048,4947968,8,0,19900416,2147483648u,2147483648u,20160512,22290432,28164096,9110016,9109504,1949696,75776,899072,899072,586752,652288,651776,526336,586752,619520,618496,0,2147483648u,67,1065353216,0,0,0,0,0,0,0,0,0,0,0,
74240,128,0,176512,176512,25960448,65536,26091520,6,0,1576960,1572864,8,128,10969088,0,0,10878976,22380544,25559040,5775360,5767168,262272,69888,688128,688384,561280,626688,528896,524288,561152,594048,593920,0,2147483648u,68,0,0,0,0,0,0,0,0,0,0,0,0,
74240,128,0,176704,176704,26353664,65536,26484736,6,0,1577984,1605632,8,128,10971136,0,0,10969088,22470656,25575424,5775872,5832704,278656,71936,690176,690432,561280,626688,626816,528384,561152,594048,593920,0,2147483648u,69,0,0,0,0,0,0,0,0,0,0,0,0,
74240,128,0,131200,147584,22495232,2048,22511616,4,0,1579008,1638400,8,128,11329536,0,0,11059200,22560768,25591808,5840896,5898240,295040,73984,692224,692480,561280,626688,528896,526336,562176,594048,593920,0,2147483648u,70,1065353216,1065353216,0,0,0,0,0,0,0,0,0,0,
74240,128,0,1179776,1196160,22507520,2048,22523904,4,0,1580032,1671168,8,128,11331584,0,0,11149312,22650880,25608192,5841408,5963776,311424,76032,694272,694528,561280,626688,626816,530432,562176,594048,593920,0,2147483648u,71,1065353216,0,0,0,0,0,0,0,0,0,0,0,
86528,640,0,220544,220544,25962496,65536,26093568,6,0,1593344,1703936,8,128,10977280,2147483648u,2147483648u,11239424,22380544,28442624,5906432,5767168,327808,69888,696320,696576,562304,627712,529024,540672,561152,595072,593920,0,2147483648u,68,0,0,0,0,0,0,0,0,0,0,0,0,
86528,640,0,220736,220736,26355712,65536,26486784,6,0,1594368,1736704,8,128,10979328,2147483648u,2147483648u,11329536,22470656,28459008,5906944,5832704,344192,71936,698368,698624,562304,627712,627328,528384,561152,595072,593920,0,2147483648u,69,0,0,0,0,0,0,0,0,0,0,0,0,
86528,640,0,163968,180352,22528000,2048,22544384,4,0,1595392,1769472,8,128,11337728,2147483648u,2147483648u,11419648,22560768,28475392,5971968,5898240,360576,73984,700416,700672,562304,627712,529024,542720,562176,595072,593920,0,2147483648u,70,1065353216,1065353216,0,0,0,0,0,0,0,0,0,0,
86528,640,0,1212544,1228928,22540288,2048,22556672,4,0,1596416,1802240,8,128,11339776,2147483648u,2147483648u,11509760,22650880,28491776,5972480,5963776,376960,76032,702464,702720,562304,627712,627328,530432,562176,595072,593920,0,2147483648u,71,1065353216,0,0,0,0,0,0,0,0,0,0,0,
98816,128,0,528768,528768,25976832,65536,26107904,6,0,2625536,2621440,8,128,13852672,0,0,13762560,22380544,25559040,6823936,6815744,786560,69888,753664,753920,569472,634880,529920,524288,569344,602240,602112,0,2147483648u,68,0,0,0,0,0,0,0,0,0,0,0,0,
98816,128,0,528960,528960,26370048,65536,26501120,6,0,2626560,2654208,8,128,13854720,0,0,13852672,22470656,25575424,6824448,6881280,802944,71936,755712,755968,569472,634880,635008,528384,569344,602240,602112,0,2147483648u,69,0,0,0,0,0,0,0,0,0,0,0,0,
98816,128,0,393344,409728,22384640,2048,22392832,4,0,2627584,2686976,8,128,14213120,0,0,13942784,22560768,25591808,6889472,6946816,819328,73984,757760,758016,569472,634880,529920,526336,570368,602240,602112,0,2147483648u,70,1065353216,1065353216,0,0,0,0,0,0,0,0,0,0,
98816,128,0,1376384,1392768,22720512,2048,22736896,4,2147483648u,2628608,2719744,8,128,14215168,0,0,14032896,22650880,25608192,6889984,7012352,835712,76032,759808,760064,569472,634880,635008,530432,570368,602240,602112,0,2147483648u,71,1065353216,0,0,0,0,0,0,0,0,0,0,0,
111104,640,0,572800,572800,25978880,65536,26109952,6,0,2641920,2752512,8,128,13860864,2147483648u,2147483648u,14123008,22380544,28442624,6955008,6815744,852096,69888,761856,762112,570496,635904,530048,540672,569344,603264,602112,0,2147483648u,68,0,0,0,0,0,0,0,0,0,0,0,0,
111104,640,0,572992,572992,26372096,65536,26503168,6,0,2642944,2785280,8,128,13862912,2147483648u,2147483648u,14213120,22470656,28459008,6955520,6881280,868480,71936,763904,764160,570496,635904,635520,528384,569344,603264,602112,0,2147483648u,69,0,0,0,0,0,0,0,0,0,0,0,0,
111104,640,0,426112,442496,22409216,2048,22425600,4,0,2643968,2818048,8,128,14221312,2147483648u,2147483648u,14303232,22560768,28475392,7020544,6946816,884864,73984,765952,766208,570496,635904,530048,542720,570368,603264,602112,0,2147483648u,70,1065353216,1065353216,0,0,0,0,0,0,0,0,0,0,
111104,640,0,1376384,1392768,22720512,2048,22736896,4,2147483648u,2644992,2850816,8,128,14223360,2147483648u,2147483648u,14393344,22650880,28491776,7021056,7012352,901248,76032,768000,768256,570496,635904,635520,530432,570368,603264,602112,0,2147483648u,71,1065353216,0,0,0,0,0,0,0,0,0,0,0,
74496,128,0,881024,881024,25993216,65536,26124288,6,0,3674112,3670016,8,128,16736256,0,0,16646144,22380544,25559040,7872512,7864320,1310848,69888,819200,819456,577664,643072,530944,524288,577536,610432,610304,0,2147483648u,68,0,0,0,0,0,0,0,0,0,0,0,0,
74496,128,0,881216,881216,26386432,65536,26517504,6,0,3675136,3702784,8,128,16738304,0,0,16736256,22470656,25575424,7873024,7929856,1327232,71936,821248,821504,577664,643072,643200,528384,577536,610432,610304,0,2147483648u,69,0,0,0,0,0,0,0,0,0,0,0,0,
74496,128,0,655488,671872,22638592,2048,22663168,4,0,3676160,3735552,8,128,17096704,0,0,16826368,22560768,25591808,7938048,7995392,1343616,73984,823296,823552,577664,643072,530944,526336,578560,610432,610304,0,2147483648u,70,1065353216,1065353216,0,0,0,0,0,0,0,0,0,0,
74496,128,0,1376384,1392768,22720512,2048,22736896,4,2147483648u,3677184,3768320,8,128,17098752,0,0,16916480,22650880,25608192,7938560,8060928,1360000,76032,825344,825600,577664,643072,643200,530432,578560,610432,610304,0,2147483648u,71,1065353216,0,0,0,0,0,0,0,0,0,0,0,
86784,640,0,925056,925056,25995264,65536,26126336,6,0,3690496,3801088,8,128,16744448,2147483648u,2147483648u,17006592,22380544,28442624,8003584,7864320,1376384,69888,827392,827648,578688,644096,531072,540672,577536,611456,610304,0,2147483648u,68,0,0,0,0,0,0,0,0,0,0,0,0,
86784,640,0,925248,925248,26388480,65536,26519552,6,0,3691520,3833856,8,128,16746496,2147483648u,2147483648u,17096704,22470656,28459008,8004096,7929856,1392768,71936,829440,829696,578688,644096,643712,528384,577536,611456,610304,0,2147483648u,69,0,0,0,0,0,0,0,0,0,0,0,0,
86784,640,0,688256,704640,22679552,2048,22695936,4,0,3692544,3866624,8,128,17104896,2147483648u,2147483648u,17186816,22560768,28475392,8069120,7995392,1409152,73984,831488,831744,578688,644096,531072,542720,578560,611456,610304,0,2147483648u,70,1065353216,1065353216,0,0,0,0,0,0,0,0,0,0,
86784,640,0,1376384,1392768,22720512,2048,22736896,4,2147483648u,3693568,3899392,8,128,17106944,2147483648u,2147483648u,17276928,22650880,28491776,8069632,8060928,1425536,76032,833536,833792,578688,644096,643712,530432,578560,611456,610304,0,2147483648u,71,1065353216,0,0,0,0,0,0,0,0,0,0,0,
99072,128,0,1233280,1233280,26009600,65536,26140672,6,0,4722688,4718592,8,128,19619840,0,0,19529728,22380544,25559040,8921088,8912896,1835136,69888,884736,884992,585856,651264,531968,524288,585728,618624,618496,0,2147483648u,68,0,0,0,0,0,0,0,0,0,0,0,0,
99072,128,0,1233472,1233472,26402816,65536,26533888,6,0,4723712,4751360,8,128,19621888,0,0,19619840,22470656,25575424,8921600,8978432,1851520,71936,886784,887040,585856,651264,651392,528384,585728,618624,618496,0,2147483648u,69,0,0,0,0,0,0,0,0,0,0,0,0,
99072,128,0,917632,934016,22577152,2048,22593536,4,0,4724736,4784128,8,128,19980288,0,0,19709952,22560768,25591808,8986624,9043968,1867904,73984,888832,889088,585856,651264,531968,526336,586752,618624,618496,0,2147483648u,70,1065353216,1065353216,0,0,0,0,0,0,0,0,0,0,
99072,128,0,1376384,1392768,22720512,2048,22736896,4,2147483648u,4725760,4816896,8,128,19982336,0,0,19800064,22650880,25608192,8987136,9109504,1884288,76032,890880,891136,585856,651264,651392,530432,586752,618624,618496,0,2147483648u,71,1065353216,0,0,0,0,0,0,0,0,0,0,0,
111360,640,0,1277312,1277312,26011648,65536,26142720,6,0,4739072,4849664,8,128,19628032,2147483648u,2147483648u,19890176,22380544,28442624,9052160,8912896,1900672,69888,892928,893184,586880,652288,532096,540672,585728,619648,618496,0,2147483648u,68,0,0,0,0,0,0,0,0,0,0,0,0,
111360,640,0,1277504,1277504,26404864,65536,26535936,6,0,4740096,4882432,8,128,19630080,2147483648u,2147483648u,19980288,22470656,28459008,9052672,8978432,1917056,71936,894976,895232,586880,652288,651904,528384,585728,619648,618496,0,2147483648u,69,0,0,0,0,0,0,0,0,0,0,0,0,
111360,640,0,950400,966784,22609920,2048,22626304,4,0,4741120,4915200,8,128,19988480,2147483648u,2147483648u,20070400,22560768,28475392,9117696,9043968,1933440,73984,897024,897280,586880,652288,532096,542720,586752,619648,618496,0,2147483648u,70,1065353216,1065353216,0,0,0,0,0,0,0,0,0,0,
111360,640,0,1376384,1392768,22720512,2048,22736896,4,2147483648u,4742144,4947968,8,128,19990528,2147483648u,2147483648u,20160512,22650880,28491776,9118208,9109504,1949824,76032,899072,899328,586880,652288,651904,530432,586752,619648,618496,0,2147483648u,71,1065353216,0,0,0,0,0,0,0,0,0,0,0,
74752,256,0,176896,176896,26746880,65536,26877952,6,0,1581056,1572864,8,256,11059200,0,0,10878976,22740992,25952256,5783552,5767168,262400,70144,688128,688640,561408,626688,532992,528384,561152,594176,593920,0,2147483648u,72,0,0,0,0,0,0,0,0,0,0,0,0,
74752,256,0,177088,177088,27140096,65536,27271168,6,0,1582080,1605632,8,256,11061248,0,0,10969088,22831104,25968640,5784064,5832704,278784,72192,690176,690688,561408,626688,626944,532480,561152,594176,593920,0,2147483648u,73,0,0,0,0,0,0,0,0,0,0,0,0,
74752,256,0,131328,147712,22855680,2048,22872064,4,0,1583104,1638400,8,256,11419648,0,0,11059200,22921216,25985024,5849088,5898240,295168,74240,692224,692736,561408,626688,532992,530432,562176,594176,593920,0,2147483648u,74,1065353216,1065353216,0,0,0,0,0,0,0,0,0,0,
74752,256,0,1179904,1196288,22867968,2048,22884352,4,0,1584128,1671168,8,256,11421696,0,0,11149312,23011328,26001408,5849600,5963776,311552,76288,694272,694784,561408,626688,626944,534528,562176,594176,593920,0,2147483648u,75,1065353216,0,0,0,0,0,0,0,0,0,0,0,
87040,768,0,220928,220928,26748928,65536,26880000,6,0,1597440,1703936,8,256,11067392,2147483648u,2147483648u,11239424,22740992,28770304,5914624,5767168,327936,70144,696320,696832,562432,627712,533120,544768,561152,595200,593920,0,2147483648u,72,0,0,0,0,0,0,0,0,0,0,0,0,
87040,768,0,221120,221120,27142144,65536,27273216,6,0,1598464,1736704,8,256,11069440,2147483648u,2147483648u,11329536,22831104,28786688,5915136,5832704,344320,72192,698368,698880,562432,627712,627456,532480,561152,595200,593920,0,2147483648u,73,0,0,0,0,0,0,0,0,0,0,0,0,
87040,768,0,164096,180480,22888448,2048,22904832,4,0,1599488,1769472,8,256,11427840,2147483648u,2147483648u,11419648,22921216,28803072,5980160,5898240,360704,74240,700416,700928,562432,627712,533120,546816,562176,595200,593920,0,2147483648u,74,1065353216,1065353216,0,0,0,0,0,0,0,0,0,0,
87040,768,0,1212672,1229056,22900736,2048,22917120,4,0,1600512,1802240,8,256,11429888,2147483648u,2147483648u,11509760,23011328,28819456,5980672,5963776,377088,76288,702464,702976,562432,627712,627456,534528,562176,595200,593920,0,2147483648u,75,1065353216,0,0,0,0,0,0,0,0,0,0,0,
99328,256,0,529152,529152,26763264,65536,26894336,6,0,2629632,2621440,8,256,13942784,0,0,13762560,22740992,25952256,6832128,6815744,786688,70144,753664,754176,569600,634880,534016,528384,569344,602368,602112,0,2147483648u,72,0,0,0,0,0,0,0,0,0,0,0,0,
99328,256,0,529344,529344,27156480,65536,27287552,6,0,2630656,2654208,8,256,13944832,0,0,13852672,22831104,25968640,6832640,6881280,803072,72192,755712,756224,569600,634880,635136,532480,569344,602368,602112,0,2147483648u,73,0,0,0,0,0,0,0,0,0,0,0,0,
99328,256,0,393472,409856,22745088,2048,22753280,4,0,2631680,2686976,8,256,14303232,0,0,13942784,22921216,25985024,6897664,6946816,819456,74240,757760,758272,569600,634880,534016,530432,570368,602368,602112,0,2147483648u,74,1065353216,1065353216,0,0,0,0,0,0,0,0,0,0,
99328,256,0,1376512,1392896,23080960,2048,23097344,4,2147483648u,2632704,2719744,8,256,14305280,0,0,14032896,23011328,26001408,6898176,7012352,835840,76288,759808,760320,569600,634880,635136,534528,570368,602368,602112,0,2147483648u,75,1065353216,0,0,0,0,0,0,0,0,0,0,0,
111616,768,0,573184,573184,26765312,65536,26896384,6,0,2646016,2752512,8,256,13950976,2147483648u,2147483648u,14123008,22740992,28770304,6963200,6815744,852224,70144,761856,762368,570624,635904,534144,544768,569344,603392,602112,0,2147483648u,72,0,0,0,0,0,0,0,0,0,0,0,0,
111616,768,0,573376,573376,27158528,65536,27289600,6,0,2647040,2785280,8,256,13953024,2147483648u,2147483648u,14213120,22831104,28786688,6963712,6881280,868608,72192,763904,764416,570624,635904,635648,532480,569344,603392,602112,0,2147483648u,73,0,0,0,0,0,0,0,0,0,0,0,0,
111616,768,0,426240,442624,22769664,2048,22786048,4,0,2648064,2818048,8,256,14311424,2147483648u,2147483648u,14303232,22921216,28803072,7028736,6946816,884992,74240,765952,766464,570624,635904,534144,546816,570368,603392,602112,0,2147483648u,74,1065353216,1065353216,0,0,0,0,0,0,0,0,0,0,
111616,768,0,1376512,1392896,23080960,2048,23097344,4,2147483648u,2649088,2850816,8,256,14313472,2147483648u,2147483648u,14393344,23011328,28819456,7029248,7012352,901376,76288,768000,768512,570624,635904,635648,534528,570368,603392,602112,0,2147483648u,75,1065353216,0,0,0,0,0,0,0,0,0,0,0,
75008,256,0,881408,881408,26779648,65536,26910720,6,0,3678208,3670016,8,256,16826368,0,0,16646144,22740992,25952256,7880704,7864320,1310976,70144,819200,819712,577792,643072,535040,528384,577536,610560,610304,0,2147483648u,72,0,0,0,0,0,0,0,0,0,0,0,0,
75008,256,0,881600,881600,27172864,65536,27303936,6,0,3679232,3702784,8,256,16828416,0,0,16736256,22831104,25968640,7881216,7929856,1327360,72192,821248,821760,577792,643072,643328,532480,577536,610560,610304,0,2147483648u,73,0,0,0,0,0,0,0,0,0,0,0,0,
75008,256,0,655616,672000,22999040,2048,23023616,4,0,3680256,3735552,8,256,17186816,0,0,16826368,22921216,25985024,7946240,7995392,1343744,74240,823296,823808,577792,643072,535040,530432,578560,610560,610304,0,2147483648u,74,1065353216,1065353216,0,0,0,0,0,0,0,0,0,0,
75008,256,0,1376512,1392896,23080960,2048,23097344,4,2147483648u,3681280,3768320,8,256,17188864,0,0,16916480,23011328,26001408,7946752,8060928,1360128,76288,825344,825856,577792,643072,643328,534528,578560,610560,610304,0,2147483648u,75,1065353216,0,0,0,0,0,0,0,0,0,0,0,
87296,768,0,925440,925440,26781696,65536,26912768,6,0,3694592,3801088,8,256,16834560,2147483648u,2147483648u,17006592,22740992,28770304,8011776,7864320,1376512,70144,827392,827904,578816,644096,535168,544768,577536,611584,610304,0,2147483648u,72,0,0,0,0,0,0,0,0,0,0,0,0,
87296,768,0,925632,925632,27174912,65536,27305984,6,0,3695616,3833856,8,256,16836608,2147483648u,2147483648u,17096704,22831104,28786688,8012288,7929856,1392896,72192,829440,829952,578816,644096,643840,532480,577536,611584,610304,0,2147483648u,73,0,0,0,0,0,0,0,0,0,0,0,0,
87296,768,0,688384,704768,23040000,2048,23056384,4,0,3696640,3866624,8,256,17195008,2147483648u,2147483648u,17186816,22921216,28803072,8077312,7995392,1409280,74240,831488,832000,578816,644096,535168,546816,578560,611584,610304,0,2147483648u,74,1065353216,1065353216,0,0,0,0,0,0,0,0,0,0,
87296,768,0,1376512,1392896,23080960,2048,23097344,4,2147483648u,3697664,3899392,8,256,17197056,2147483648u,2147483648u,17276928,23011328,28819456,8077824,8060928,1425664,76288,833536,834048,578816,644096,643840,534528,578560,611584,610304,0,2147483648u,75,1065353216,0,0,0,0,0,0,0,0,0,0,0,
99584,256,0,1233664,1233664,26796032,65536,26927104,6,0,4726784,4718592,8,256,19709952,0,0,19529728,22740992,25952256,8929280,8912896,1835264,70144,884736,885248,585984,651264,536064,528384,585728,618752,618496,0,2147483648u,72,0,0,0,0,0,0,0,0,0,0,0,0,
99584,256,0,1233856,1233856,27189248,65536,27320320,6,0,4727808,4751360,8,256,19712000,0,0,19619840,22831104,25968640,8929792,8978432,1851648,72192,886784,887296,585984,651264,651520,532480,585728,618752,618496,0,2147483648u,73,0,0,0,0,0,0,0,0,0,0,0,0,
99584,256,0,917760,934144,22937600,2048,22953984,4,0,4728832,4784128,8,256,20070400,0,0,19709952,22921216,25985024,8994816,9043968,1868032,74240,888832,889344,585984,651264,536064,530432,586752,618752,618496,0,2147483648u,74,1065353216,1065353216,0,0,0,0,0,0,0,0,0,0,
99584,256,0,1376512,1392896,23080960,2048,23097344,4,2147483648u,4729856,4816896,8,256,20072448,0,0,19800064,23011328,26001408,8995328,9109504,1884416,76288,890880,891392,585984,651264,651520,534528,586752,618752,618496,0,2147483648u,75,1065353216,0,0,0,0,0,0,0,0,0,0,0,
111872,768,0,1277696,1277696,26798080,65536,26929152,6,0,4743168,4849664,8,256,19718144,2147483648u,2147483648u,19890176,22740992,28770304,9060352,8912896,1900800,70144,892928,893440,587008,652288,536192,544768,585728,619776,618496,0,2147483648u,72,0,0,0,0,0,0,0,0,0,0,0,0,
111872,768,0,1277888,1277888,27191296,65536,27322368,6,0,4744192,4882432,8,256,19720192,2147483648u,2147483648u,19980288,22831104,28786688,9060864,8978432,1917184,72192,894976,895488,587008,652288,652032,532480,585728,619776,618496,0,2147483648u,73,0,0,0,0,0,0,0,0,0,0,0,0,
111872,768,0,950528,966912,22970368,2048,22986752,4,0,4745216,4915200,8,256,20078592,2147483648u,2147483648u,20070400,22921216,28803072,9125888,9043968,1933568,74240,897024,897536,587008,652288,536192,546816,586752,619776,618496,0,2147483648u,74,1065353216,1065353216,0,0,0,0,0,0,0,0,0,0,
111872,768,0,1376512,1392896,23080960,2048,23097344,4,2147483648u,4746240,4947968,8,256,20080640,2147483648u,2147483648u,20160512,23011328,28819456,9126400,9109504,1949952,76288,899072,899584,587008,652288,652032,534528,586752,619776,618496,0,2147483648u,75,1065353216,0,0,0,0,0,0,0,0,0,0,0,
75264,384,0,177280,177280,27533312,65536,27664384,5,0,1585152,1572864,8,384,11149312,0,0,10878976,23101440,26345472,5791744,5767168,262528,70400,688128,688896,561536,626688,537088,528384,561152,594304,593920,0,2147483648u,76,0,0,0,0,0,0,0,0,0,0,0,0,
75264,384,0,177440,177440,27860992,65536,27992064,5,0,1586176,1605632,8,384,11151360,0,0,10969088,23191552,26361856,5792256,5832704,278912,72448,690176,690944,561536,626688,627072,536576,561152,594304,593920,0,2147483648u,77,0,0,0,0,0,0,0,0,0,0,0,0,
75264,384,0,131456,147840,23216128,2048,23232512,4,0,1587200,1638400,8,384,11509760,0,0,11059200,23281664,26378240,5857280,5898240,295296,74496,692224,692992,561536,626688,537088,530432,562176,594304,593920,0,2147483648u,78,1065353216,1065353216,0,0,0,0,0,0,0,0,0,0,
75264,384,0,1180032,1196416,23228416,2048,23244800,4,0,1588224,1671168,8,384,11511808,0,0,11149312,23371776,26394624,5857792,5963776,311680,76544,694272,695040,561536,626688,627072,538624,562176,594304,593920,0,2147483648u,79,1065353216,0,0,0,0,0,0,0,0,0,0,0,
87552,896,0,221312,221312,27535360,65536,27666432,5,0,1601536,1703936,8,384,11157504,2147483648u,2147483648u,11239424,23101440,29097984,5922816,5767168,328064,70400,696320,697088,562560,627712,537216,544768,561152,595328,593920,0,2147483648u,76,0,0,0,0,0,0,0,0,0,0,0,0,
87552,896,0,221472,221472,27863040,65536,27994112,5,0,1602560,1736704,8,384,11159552,2147483648u,2147483648u,11329536,23191552,29114368,5923328,5832704,344448,72448,698368,699136,562560,627712,627584,536576,561152,595328,593920,0,2147483648u,77,0,0,0,0,0,0,0,0,0,0,0,0,
87552,896,0,164224,180608,23248896,2048,23265280,4,0,1603584,1769472,8,384,11517952,2147483648u,2147483648u,11419648,23281664,29130752,5988352,5898240,360832,74496,700416,701184,562560,627712,537216,546816,562176,595328,593920,0,2147483648u,78,1065353216,1065353216,0,0,0,0,0,0,0,0,0,0,
87552,896,0,1212800,1229184,23261184,2048,23277568,4,0,1604608,1802240,8,384,11520000,2147483648u,2147483648u,11509760,23371776,29147136,5988864,5963776,377216,76544,702464,703232,562560,627712,627584,538624,562176,595328,593920,0,2147483648u,79,1065353216,0,0,0,0,0,0,0,0,0,0,0,
99840,384,0,529536,529536,27549696,65536,27680768,5,0,2633728,2621440,8,384,14032896,0,0,13762560,23101440,26345472,6840320,6815744,786816,70400,753664,754432,569728,634880,538112,528384,569344,602496,602112,0,2147483648u,76,0,0,0,0,0,0,0,0,0,0,0,0,
99840,384,0,529696,529696,27877376,65536,28008448,5,0,2634752,2654208,8,384,14034944,0,0,13852672,23191552,26361856,6840832,6881280,803200,72448,755712,756480,569728,634880,635264,536576,569344,602496,602112,0,2147483648u,77,0,0,0,0,0,0,0,0,0,0,0,0,
99840,384,0,393600,409984,23105536,2048,23113728,4,0,2635776,2686976,8,384,14393344,0,0,13942784,23281664,26378240,6905856,6946816,819584,74496,757760,758528,569728,634880,538112,530432,570368,602496,602112,0,2147483648u,78,1065353216,1065353216,0,0,0,0,0,0,0,0,0,0,
99840,384,0,1376640,1393024,23441408,2048,23457792,4,2147483648u,2636800,2719744,8,384,14395392,0,0,14032896,23371776,26394624,6906368,7012352,835968,76544,759808,760576,569728,634880,635264,538624,570368,602496,602112,0,2147483648u,79,1065353216,0,0,0,0,0,0,0,0,0,0,0,
112128,896,0,573568,573568,27551744,65536,27682816,5,0,2650112,2752512,8,384,14041088,2147483648u,2147483648u,14123008,23101440,29097984,6971392,6815744,852352,70400,761856,762624,570752,635904,538240,544768,569344,603520,602112,0,2147483648u,76,0,0,0,0,0,0,0,0,0,0,0,0,
112128,896,0,573728,573728,27879424,65536,28010496,5,0,2651136,2785280,8,384,14043136,2147483648u,2147483648u,14213120,23191552,29114368,6971904,6881280,868736,72448,763904,764672,570752,635904,635776,536576,569344,603520,602112,0,2147483648u,77,0,0,0,0,0,0,0,0,0,0,0,0,
112128,896,0,426368,442752,23130112,2048,23146496,4,0,2652160,2818048,8,384,14401536,2147483648u,2147483648u,14303232,23281664,29130752,7036928,6946816,885120,74496,765952,766720,570752,635904,538240,546816,570368,603520,602112,0,2147483648u,78,1065353216,1065353216,0,0,0,0,0,0,0,0,0,0,
112128,896,0,1376640,1393024,23441408,2048,23457792,4,2147483648u,2653184,2850816,8,384,14403584,2147483648u,2147483648u,14393344,23371776,29147136,7037440,7012352,901504,76544,768000,768768,570752,635904,635776,538624,570368,603520,602112,0,2147483648u,79,1065353216,0,0,0,0,0,0,0,0,0,0,0,
75520,384,0,881792,881792,27566080,65536,27697152,5,0,3682304,3670016,8,384,16916480,0,0,16646144,23101440,26345472,7888896,7864320,1311104,70400,819200,819968,577920,643072,539136,528384,577536,610688,610304,0,2147483648u,76,0,0,0,0,0,0,0,0,0,0,0,0,
75520,384,0,881952,881952,27893760,65536,28024832,5,0,3683328,3702784,8,384,16918528,0,0,16736256,23191552,26361856,7889408,7929856,1327488,72448,821248,822016,577920,643072,643456,536576,577536,610688,610304,0,2147483648u,77,0,0,0,0,0,0,0,0,0,0,0,0,
75520,384,0,655744,672128,23359488,2048,23384064,4,0,3684352,3735552,8,384,17276928,0,0,16826368,23281664,26378240,7954432,7995392,1343872,74496,823296,824064,577920,643072,539136,530432,578560,610688,610304,0,2147483648u,78,1065353216,1065353216,0,0,0,0,0,0,0,0,0,0,
75520,384,0,1376640,1393024,23441408,2048,23457792,4,2147483648u,3685376,3768320,8,384,17278976,0,0,16916480,23371776,26394624,7954944,8060928,1360256,76544,825344,826112,577920,643072,643456,538624,578560,610688,610304,0,2147483648u,79,1065353216,0,0,0,0,0,0,0,0,0,0,0,
87808,896,0,925824,925824,27568128,65536,27699200,5,0,3698688,3801088,8,384,16924672,2147483648u,2147483648u,17006592,23101440,29097984,8019968,7864320,1376640,70400,827392,828160,578944,644096,539264,544768,577536,611712,610304,0,2147483648u,76,0,0,0,0,0,0,0,0,0,0,0,0,
87808,896,0,925984,925984,27895808,65536,28026880,5,0,3699712,3833856,8,384,16926720,2147483648u,2147483648u,17096704,23191552,29114368,8020480,7929856,1393024,72448,829440,830208,578944,644096,643968,536576,577536,611712,610304,0,2147483648u,77,0,0,0,0,0,0,0,0,0,0,0,0,
87808,896,0,688512,704896,23400448,2048,23416832,4,0,3700736,3866624,8,384,17285120,2147483648u,2147483648u,17186816,23281664,29130752,8085504,7995392,1409408,74496,831488,832256,578944,644096,539264,546816,578560,611712,610304,0,2147483648u,78,1065353216,1065353216,0,0,0,0,0,0,0,0,0,0,
87808,896,0,1376640,1393024,23441408,2048,23457792,4,2147483648u,3701760,3899392,8,384,17287168,2147483648u,2147483648u,17276928,23371776,29147136,8086016,8060928,1425792,76544,833536,834304,578944,644096,643968,538624,578560,611712,610304,0,2147483648u,79,1065353216,0,0,0,0,0,0,0,0,0,0,0,
100096,384,0,1234048,1234048,27582464,65536,27713536,5,0,4730880,4718592,8,384,19800064,0,0,19529728,23101440,26345472,8937472,8912896,1835392,70400,884736,885504,586112,651264,540160,528384,585728,618880,618496,0,2147483648u,76,0,0,0,0,0,0,0,0,0,0,0,0,
100096,384,0,1234208,1234208,27910144,65536,28041216,5,0,4731904,4751360,8,384,19802112,0,0,19619840,23191552,26361856,8937984,8978432,1851776,72448,886784,887552,586112,651264,651648,536576,585728,618880,618496,0,2147483648u,77,0,0,0,0,0,0,0,0,0,0,0,0,
100096,384,0,917888,934272,23298048,2048,23314432,4,0,4732928,4784128,8,384,20160512,0,0,19709952,23281664,26378240,9003008,9043968,1868160,74496,888832,889600,586112,651264,540160,530432,586752,618880,618496,0,2147483648u,78,1065353216,1065353216,0,0,0,0,0,0,0,0,0,0,
100096,384,0,1376640,1393024,23441408,2048,23457792,4,2147483648u,4733952,4816896,8,384,20162560,0,0,19800064,23371776,26394624,9003520,9109504,1884544,76544,890880,891648,586112,651264,651648,538624,586752,618880,618496,0,2147483648u,79,1065353216,0,0,0,0,0,0,0,0,0,0,0,
112384,896,0,1278080,1278080,27584512,65536,27715584,5,0,4747264,4849664,8,384,19808256,2147483648u,2147483648u,19890176,23101440,29097984,9068544,8912896,1900928,70400,892928,893696,587136,652288,540288,544768,585728,619904,618496,0,2147483648u,76,0,0,0,0,0,0,0,0,0,0,0,0,
112384,896,0,1278240,1278240,27912192,65536,28043264,5,0,4748288,4882432,8,384,19810304,2147483648u,2147483648u,19980288,23191552,29114368,9069056,8978432,1917312,72448,894976,895744,587136,652288,652160,536576,585728,619904,618496,0,2147483648u,77,0,0,0,0,0,0,0,0,0,0,0,0,
112384,896,0,950656,967040,23330816,2048,23347200,4,0,4749312,4915200,8,384,20168704,2147483648u,2147483648u,20070400,23281664,29130752,9134080,9043968,1933696,74496,897024,897792,587136,652288,540288,546816,586752,619904,618496,0,2147483648u,78,1065353216,1065353216,0,0,0,0,0,0,0,0,0,0,
112384,896,0,1376640,1393024,23441408,2048,23457792,4,2147483648u,4750336,4947968,8,384,20170752,2147483648u,2147483648u,20160512,23371776,29147136,9134592,9109504,1950080,76544,899072,899840,587136,652288,652160,538624,586752,619904,618496,0,2147483648u,79,1065353216,0,0,0,0,0,0,0,0,0,0,0,
75776,0,0,177600,177600,28188672,65536,28319744,5,0,1703936,1572864,8,512,10883072,0,0,10878976,23461888,26738688,5799936,5767168,262656,70656,688128,689152,561664,626688,541184,532480,561152,594432,593920,0,2147483648u,80,0,0,0,0,0,0,0,0,0,0,0,0,
75776,0,0,177760,177760,28516352,65536,28647424,5,0,1704960,1605632,8,512,10885120,0,0,10969088,23552000,26755072,5800448,5832704,279040,72704,690176,691200,561664,626688,627712,540672,561152,594432,593920,0,2147483648u,81,0,0,0,0,0,0,0,0,0,0,0,0,
75776,0,0,131584,147968,23576576,2048,23592960,4,0,1705984,1638400,8,512,11243520,0,0,11059200,23642112,26771456,5865472,5898240,295424,74752,692224,693248,561664,626688,541184,534528,562176,594432,593920,0,2147483648u,82,1065353216,1065353216,0,0,0,0,0,0,0,0,0,0,
75776,0,0,1180160,1196544,23588864,2048,23605248,4,0,1707008,1671168,8,512,11245568,0,0,11149312,23732224,26787840,5865984,5963776,311808,76800,694272,695296,561664,626688,627712,542720,562176,594432,593920,0,2147483648u,83,1065353216,0,0,0,0,0,0,0,0,0,0,0,
88064,512,0,221632,221632,28190720,65536,28321792,5,0,1720320,1703936,8,512,10891264,2147483648u,2147483648u,11239424,23461888,29425664,5931008,5767168,328192,70656,696320,697344,562688,627712,541312,548864,561152,595456,593920,0,2147483648u,80,0,0,0,0,0,0,0,0,0,0,0,0,
88064,512,0,221792,221792,28518400,65536,28649472,5,0,1721344,1736704,8,512,10893312,2147483648u,2147483648u,11329536,23552000,29442048,5931520,5832704,344576,72704,698368,699392,562688,627712,628224,540672,561152,595456,593920,0,2147483648u,81,0,0,0,0,0,0,0,0,0,0,0,0,
88064,512,0,164352,180736,23609344,2048,23625728,4,0,1722368,1769472,8,512,11251712,2147483648u,2147483648u,11419648,23642112,29458432,5996544,5898240,360960,74752,700416,701440,562688,627712,541312,550912,562176,595456,593920,0,2147483648u,82,1065353216,1065353216,0,0,0,0,0,0,0,0,0,0,
88064,512,0,1212928,1229312,23621632,2048,23638016,4,0,1723392,1802240,8,512,11253760,2147483648u,2147483648u,11509760,23732224,29474816,5997056,5963776,377344,76800,702464,703488,562688,627712,628224,542720,562176,595456,593920,0,2147483648u,83,1065353216,0,0,0,0,0,0,0,0,0,0,0,
100352,0,0,529856,529856,28205056,65536,28336128,5,0,2752512,2621440,8,512,13766656,0,0,13762560,23461888,26738688,6848512,6815744,786944,70656,753664,754688,569856,634880,542208,532480,569344,602624,602112,0,2147483648u,80,0,0,0,0,0,0,0,0,0,0,0,0,
100352,0,0,530016,530016,28532736,65536,28663808,5,0,2753536,2654208,8,512,13768704,0,0,13852672,23552000,26755072,6849024,6881280,803328,72704,755712,756736,569856,634880,635904,540672,569344,602624,602112,0,2147483648u,81,0,0,0,0,0,0,0,0,0,0,0,0,
100352,0,0,393728,410112,23465984,2048,23474176,4,0,2754560,2686976,8,512,14127104,0,0,13942784,23642112,26771456,6914048,6946816,819712,74752,757760,758784,569856,634880,542208,534528,570368,602624,602112,0,2147483648u,82,1065353216,1065353216,0,0,0,0,0,0,0,0,0,0,
100352,0,0,1376768,1393152,23801856,2048,23818240,4,2147483648u,2755584,2719744,8,512,14129152,0,0,14032896,23732224,26787840,6914560,7012352,836096,76800,759808,760832,569856,634880,635904,542720,570368,602624,602112,0,2147483648u,83,1065353216,0,0,0,0,0,0,0,0,0,0,0,
112640,512,0,573888,573888,28207104,65536,28338176,5,0,2768896,2752512,8,512,13774848,2147483648u,2147483648u,14123008,23461888,29425664,6979584,6815744,852480,70656,761856,762880,570880,635904,542336,548864,569344,603648,602112,0,2147483648u,80,0,0,0,0,0,0,0,0,0,0,0,0,
112640,512,0,574048,574048,28534784,65536,28665856,5,0,2769920,2785280,8,512,13776896,2147483648u,2147483648u,14213120,23552000,29442048,6980096,6881280,868864,72704,763904,764928,570880,635904,636416,540672,569344,603648,602112,0,2147483648u,81,0,0,0,0,0,0,0,0,0,0,0,0,
112640,512,0,426496,442880,23490560,2048,23506944,4,0,2770944,2818048,8,512,14135296,2147483648u,2147483648u,14303232,23642112,29458432,7045120,6946816,885248,74752,765952,766976,570880,635904,542336,550912,570368,603648,602112,0,2147483648u,82,1065353216,1065353216,0,0,0,0,0,0,0,0,0,0,
112640,512,0,1376768,1393152,23801856,2048,23818240,4,2147483648u,2771968,2850816,8,512,14137344,2147483648u,2147483648u,14393344,23732224,29474816,7045632,7012352,901632,76800,768000,769024,570880,635904,636416,542720,570368,603648,602112,0,2147483648u,83,1065353216,0,0,0,0,0,0,0,0,0,0,0,
76032,0,0,882112,882112,28221440,65536,28352512,5,0,3801088,3670016,8,512,16650240,0,0,16646144,23461888,26738688,7897088,7864320,1311232,70656,819200,820224,578048,643072,543232,532480,577536,610816,610304,0,2147483648u,80,0,0,0,0,0,0,0,0,0,0,0,0,
76032,0,0,882272,882272,28549120,65536,28680192,5,0,3802112,3702784,8,512,16652288,0,0,16736256,23552000,26755072,7897600,7929856,1327616,72704,821248,822272,578048,643072,644096,540672,577536,610816,610304,0,2147483648u,81,0,0,0,0,0,0,0,0,0,0,0,0,
76032,0,0,655872,672256,23719936,2048,23744512,4,0,3803136,3735552,8,512,17010688,0,0,16826368,23642112,26771456,7962624,7995392,1344000,74752,823296,824320,578048,643072,543232,534528,578560,610816,610304,0,2147483648u,82,1065353216,1065353216,0,0,0,0,0,0,0,0,0,0,
76032,0,0,1376768,1393152,23801856,2048,23818240,4,2147483648u,3804160,3768320,8,512,17012736,0,0,16916480,23732224,26787840,7963136,8060928,1360384,76800,825344,826368,578048,643072,644096,542720,578560,610816,610304,0,2147483648u,83,1065353216,0,0,0,0,0,0,0,0,0,0,0,
88320,512,0,926144,926144,28223488,65536,28354560,5,0,3817472,3801088,8,512,16658432,2147483648u,2147483648u,17006592,23461888,29425664,8028160,7864320,1376768,70656,827392,828416,579072,644096,543360,548864,577536,611840,610304,0,2147483648u,80,0,0,0,0,0,0,0,0,0,0,0,0,
88320,512,0,926304,926304,28551168,65536,28682240,5,0,3818496,3833856,8,512,16660480,2147483648u,2147483648u,17096704,23552000,29442048,8028672,7929856,1393152,72704,829440,830464,579072,644096,644608,540672,577536,611840,610304,0,2147483648u,81,0,0,0,0,0,0,0,0,0,0,0,0,
88320,512,0,688640,705024,23760896,2048,23777280,4,0,3819520,3866624,8,512,17018880,2147483648u,2147483648u,17186816,23642112,29458432,8093696,7995392,1409536,74752,831488,832512,579072,644096,543360,550912,578560,611840,610304,0,2147483648u,82,1065353216,1065353216,0,0,0,0,0,0,0,0,0,0,
88320,512,0,1376768,1393152,23801856,2048,23818240,4,2147483648u,3820544,3899392,8,512,17020928,2147483648u,2147483648u,17276928,23732224,29474816,8094208,8060928,1425920,76800,833536,834560,579072,644096,644608,542720,578560,611840,610304,0,2147483648u,83,1065353216,0,0,0,0,0,0,0,0,0,0,0,
100608,0,0,1234368,1234368,28237824,65536,28368896,5,0,4849664,4718592,8,512,19533824,0,0,19529728,23461888,26738688,8945664,8912896,1835520,70656,884736,885760,586240,651264,544256,532480,585728,619008,618496,0,2147483648u,80,0,0,0,0,0,0,0,0,0,0,0,0,
100608,0,0,1234528,1234528,28565504,65536,28696576,5,0,4850688,4751360,8,512,19535872,0,0,19619840,23552000,26755072,8946176,8978432,1851904,72704,886784,887808,586240,651264,652288,540672,585728,619008,618496,0,2147483648u,81,0,0,0,0,0,0,0,0,0,0,0,0,
100608,0,0,918016,934400,23658496,2048,23674880,4,0,4851712,4784128,8,512,19894272,0,0,19709952,23642112,26771456,9011200,9043968,1868288,74752,888832,889856,586240,651264,544256,534528,586752,619008,618496,0,2147483648u,82,1065353216,1065353216,0,0,0,0,0,0,0,0,0,0,
100608,0,0,1376768,1393152,23801856,2048,23818240,4,2147483648u,4852736,4816896,8,512,19896320,0,0,19800064,23732224,26787840,9011712,9109504,1884672,76800,890880,891904,586240,651264,652288,542720,586752,619008,618496,0,2147483648u,83,1065353216,0,0,0,0,0,0,0,0,0,0,0,
112896,512,0,1278400,1278400,28239872,65536,28370944,5,0,4866048,4849664,8,512,19542016,2147483648u,2147483648u,19890176,23461888,29425664,9076736,8912896,1901056,70656,892928,893952,587264,652288,544384,548864,585728,620032,618496,0,2147483648u,80,0,0,0,0,0,0,0,0,0,0,0,0,
112896,512,0,1278560,1278560,28567552,65536,28698624,5,0,4867072,4882432,8,512,19544064,2147483648u,2147483648u,19980288,23552000,29442048,9077248,8978432,1917440,72704,894976,896000,587264,652288,652800,540672,585728,620032,618496,0,2147483648u,81,0,0,0,0,0,0,0,0,0,0,0,0,
112896,512,0,950784,967168,23691264,2048,23707648,4,0,4868096,4915200,8,512,19902464,2147483648u,2147483648u,20070400,23642112,29458432,9142272,9043968,1933824,74752,897024,898048,587264,652288,544384,550912,586752,620032,618496,0,2147483648u,82,1065353216,1065353216,0,0,0,0,0,0,0,0,0,0,
112896,512,0,1376768,1393152,23801856,2048,23818240,4,2147483648u,4869120,4947968,8,512,19904512,2147483648u,2147483648u,20160512,23732224,29474816,9142784,9109504,1950208,76800,899072,900096,587264,652288,652800,542720,586752,620032,618496,0,2147483648u,83,1065353216,0,0,0,0,0,0,0,0,0,0,0,
76288,128,0,177920,177920,28844032,65536,28975104,5,0,1708032,1572864,8,640,10973184,0,0,10878976,23822336,27131904,5808128,5767168,262784,70912,688128,689408,561792,626688,545280,532480,561152,594560,593920,0,2147483648u,84,0,0,0,0,0,0,0,0,0,0,0,0,
76288,128,0,178080,178080,29171712,65536,29302784,5,0,1709056,1605632,8,640,10975232,0,0,10969088,23912448,27148288,5808640,5832704,279168,72960,690176,691456,561792,626688,627840,544768,561152,594560,593920,0,2147483648u,85,0,0,0,0,0,0,0,0,0,0,0,0,
76288,128,0,131712,148096,23937024,2048,23953408,4,0,1710080,1638400,8,640,11333632,0,0,11059200,24002560,27164672,5873664,5898240,295552,75008,692224,693504,561792,626688,545280,534528,562176,594560,593920,0,2147483648u,86,1065353216,1065353216,0,0,0,0,0,0,0,0,0,0,
76288,128,0,1180288,1196672,23949312,2048,23965696,4,0,1711104,1671168,8,640,11335680,0,0,11149312,24092672,27181056,5874176,5963776,311936,77056,694272,695552,561792,626688,627840,546816,562176,594560,593920,0,2147483648u,87,1065353216,0,0,0,0,0,0,0,0,0,0,0,
88576,640,0,221952,221952,28846080,65536,28977152,5,0,1724416,1703936,8,640,10981376,2147483648u,2147483648u,11239424,23822336,29753344,5939200,5767168,328320,70912,696320,697600,562816,627712,545408,548864,561152,595584,593920,0,2147483648u,84,0,0,0,0,0,0,0,0,0,0,0,0,
88576,640,0,222112,222112,29173760,65536,29304832,5,0,1725440,1736704,8,640,10983424,2147483648u,2147483648u,11329536,23912448,29769728,5939712,5832704,344704,72960,698368,699648,562816,627712,628352,544768,561152,595584,593920,0,2147483648u,85,0,0,0,0,0,0,0,0,0,0,0,0,
88576,640,0,164480,180864,23969792,2048,23986176,4,0,1726464,1769472,8,640,11341824,2147483648u,2147483648u,11419648,24002560,29786112,6004736,5898240,361088,75008,700416,701696,562816,627712,545408,550912,562176,595584,593920,0,2147483648u,86,1065353216,1065353216,0,0,0,0,0,0,0,0,0,0,
88576,640,0,1213056,1229440,23982080,2048,23998464,4,0,1727488,1802240,8,640,11343872,2147483648u,2147483648u,11509760,24092672,29802496,6005248,5963776,377472,77056,702464,703744,562816,627712,628352,546816,562176,595584,593920,0,2147483648u,87,1065353216,0,0,0,0,0,0,0,0,0,0,0,
100864,128,0,530176,530176,28860416,65536,28991488,5,0,2756608,2621440,8,640,13856768,0,0,13762560,23822336,27131904,6856704,6815744,787072,70912,753664,754944,569984,634880,546304,532480,569344,602752,602112,0,2147483648u,84,0,0,0,0,0,0,0,0,0,0,0,0,
100864,128,0,530336,530336,29188096,65536,29319168,5,0,2757632,2654208,8,640,13858816,0,0,13852672,23912448,27148288,6857216,6881280,803456,72960,755712,756992,569984,634880,636032,544768,569344,602752,602112,0,2147483648u,85,0,0,0,0,0,0,0,0,0,0,0,0,
100864,128,0,393856,410240,23826432,2048,23834624,4,0,2758656,2686976,8,640,14217216,0,0,13942784,24002560,27164672,6922240,6946816,819840,75008,757760,759040,569984,634880,546304,534528,570368,602752,602112,0,2147483648u,86,1065353216,1065353216,0,0,0,0,0,0,0,0,0,0,
100864,128,0,1376896,1393280,24162304,2048,24178688,4,2147483648u,2759680,2719744,8,640,14219264,0,0,14032896,24092672,27181056,6922752,7012352,836224,77056,759808,761088,569984,634880,636032,546816,570368,602752,602112,0,2147483648u,87,1065353216,0,0,0,0,0,0,0,0,0,0,0,
113152,640,0,574208,574208,28862464,65536,28993536,5,0,2772992,2752512,8,640,13864960,2147483648u,2147483648u,14123008,23822336,29753344,6987776,6815744,852608,70912,761856,763136,571008,635904,546432,548864,569344,603776,602112,0,2147483648u,84,0,0,0,0,0,0,0,0,0,0,0,0,
113152,640,0,574368,574368,29190144,65536,29321216,5,0,2774016,2785280,8,640,13867008,2147483648u,2147483648u,14213120,23912448,29769728,6988288,6881280,868992,72960,763904,765184,571008,635904,636544,544768,569344,603776,602112,0,2147483648u,85,0,0,0,0,0,0,0,0,0,0,0,0,
113152,640,0,426624,443008,23851008,2048,23867392,4,0,2775040,2818048,8,640,14225408,2147483648u,2147483648u,14303232,24002560,29786112,7053312,6946816,885376,75008,765952,767232,571008,635904,546432,550912,570368,603776,602112,0,2147483648u,86,1065353216,1065353216,0,0,0,0,0,0,0,0,0,0,
113152,640,0,1376896,1393280,24162304,2048,24178688,4,2147483648u,2776064,2850816,8,640,14227456,2147483648u,2147483648u,14393344,24092672,29802496,7053824,7012352,901760,77056,768000,769280,571008,635904,636544,546816,570368,603776,602112,0,2147483648u,87,1065353216,0,0,0,0,0,0,0,0,0,0,0,
76544,128,0,882432,882432,28876800,65536,29007872,5,0,3805184,3670016,8,640,16740352,0,0,16646144,23822336,27131904,7905280,7864320,1311360,70912,819200,820480,578176,643072,547328,532480,577536,610944,610304,0,2147483648u,84,0,0,0,0,0,0,0,0,0,0,0,0,
76544,128,0,882592,882592,29204480,65536,29335552,5,0,3806208,3702784,8,640,16742400,0,0,16736256,23912448,27148288,7905792,7929856,1327744,72960,821248,822528,578176,643072,644224,544768,577536,610944,610304,0,2147483648u,85,0,0,0,0,0,0,0,0,0,0,0,0,
76544,128,0,656000,672384,24080384,2048,24104960,4,0,3807232,3735552,8,640,17100800,0,0,16826368,24002560,27164672,7970816,7995392,1344128,75008,823296,824576,578176,643072,547328,534528,578560,610944,610304,0,2147483648u,86,1065353216,1065353216,0,0,0,0,0,0,0,0,0,0,
76544,128,0,1376896,1393280,24162304,2048,24178688,4,2147483648u,3808256,3768320,8,640,17102848,0,0,16916480,24092672,27181056,7971328,8060928,1360512,77056,825344,826624,578176,643072,644224,546816,578560,610944,610304,0,2147483648u,87,1065353216,0,0,0,0,0,0,0,0,0,0,0,
88832,640,0,926464,926464,28878848,65536,29009920,5,0,3821568,3801088,8,640,16748544,2147483648u,2147483648u,17006592,23822336,29753344,8036352,7864320,1376896,70912,827392,828672,579200,644096,547456,548864,577536,611968,610304,0,2147483648u,84,0,0,0,0,0,0,0,0,0,0,0,0,
88832,640,0,926624,926624,29206528,65536,29337600,5,0,3822592,3833856,8,640,16750592,2147483648u,2147483648u,17096704,23912448,29769728,8036864,7929856,1393280,72960,829440,830720,579200,644096,644736,544768,577536,611968,610304,0,2147483648u,85,0,0,0,0,0,0,0,0,0,0,0,0,
88832,640,0,688768,705152,24121344,2048,24137728,4,0,3823616,3866624,8,640,17108992,2147483648u,2147483648u,17186816,24002560,29786112,8101888,7995392,1409664,75008,831488,832768,579200,644096,547456,550912,578560,611968,610304,0,2147483648u,86,1065353216,1065353216,0,0,0,0,0,0,0,0,0,0,
88832,640,0,1376896,1393280,24162304,2048,24178688,4,2147483648u,3824640,3899392,8,640,17111040,2147483648u,2147483648u,17276928,24092672,29802496,8102400,8060928,1426048,77056,833536,834816,579200,644096,644736,546816,578560,611968,610304,0,2147483648u,87,1065353216,0,0,0,0,0,0,0,0,0,0,0,
101120,128,0,1234688,1234688,28893184,65536,29024256,5,0,4853760,4718592,8,640,19623936,0,0,19529728,23822336,27131904,8953856,8912896,1835648,70912,884736,886016,586368,651264,548352,532480,585728,619136,618496,0,2147483648u,84,0,0,0,0,0,0,0,0,0,0,0,0,
101120,128,0,1234848,1234848,29220864,65536,29351936,5,0,4854784,4751360,8,640,19625984,0,0,19619840,23912448,27148288,8954368,8978432,1852032,72960,886784,888064,586368,651264,652416,544768,585728,619136,618496,0,2147483648u,85,0,0,0,0,0,0,0,0,0,0,0,0,
101120,128,0,918144,934528,24018944,2048,24035328,4,0,4855808,4784128,8,640,19984384,0,0,19709952,24002560,27164672,9019392,9043968,1868416,75008,888832,890112,586368,651264,548352,534528,586752,619136,618496,0,2147483648u,86,1065353216,1065353216,0,0,0,0,0,0,0,0,0,0,
101120,128,0,1376896,1393280,24162304,2048,24178688,4,2147483648u,4856832,4816896,8,640,19986432,0,0,19800064,24092672,27181056,9019904,9109504,1884800,77056,890880,892160,586368,651264,652416,546816,586752,619136,618496,0,2147483648u,87,1065353216,0,0,0,0,0,0,0,0,0,0,0,
113408,640,0,1278720,1278720,28895232,65536,29026304,5,0,4870144,4849664,8,640,19632128,2147483648u,2147483648u,19890176,23822336,29753344,9084928,8912896,1901184,70912,892928,894208,587392,652288,548480,548864,585728,620160,618496,0,2147483648u,84,0,0,0,0,0,0,0,0,0,0,0,0,
113408,640,0,1278880,1278880,29222912,65536,29353984,5,0,4871168,4882432,8,640,19634176,2147483648u,2147483648u,19980288,23912448,29769728,9085440,8978432,1917568,72960,894976,896256,587392,652288,652928,544768,585728,620160,618496,0,2147483648u,85,0,0,0,0,0,0,0,0,0,0,0,0,
113408,640,0,950912,967296,24051712,2048,24068096,4,0,4872192,4915200,8,640,19992576,2147483648u,2147483648u,20070400,24002560,29786112,9150464,9043968,1933952,75008,897024,898304,587392,652288,548480,550912,586752,620160,618496,0,2147483648u,86,1065353216,1065353216,0,0,0,0,0,0,0,0,0,0,
113408,640,0,1376896,1393280,24162304,2048,24178688,4,2147483648u,4873216,4947968,8,640,19994624,2147483648u,2147483648u,20160512,24092672,29802496,9150976,9109504,1950336,77056,899072,900352,587392,652288,652928,546816,586752,620160,618496,0,2147483648u,87,1065353216,0,0,0,0,0,0,0,0,0,0,0,
76800,256,0,178240,178240,29499392,65536,29630464,5,0,1712128,1572864,8,768,11063296,2147483648u,2147483648u,10878976,24182784,27459584,5816320,5767168,262912,71168,688128,689664,561920,626688,549376,536576,561152,594688,593920,0,2147483648u,88,0,0,0,0,0,0,0,0,0,0,0,0,
76800,256,0,178400,178400,29827072,65536,29958144,5,0,1713152,1605632,8,768,11065344,2147483648u,2147483648u,10969088,24272896,27475968,5816832,5832704,279296,73216,690176,691712,561920,626688,627968,548864,561152,594688,593920,0,2147483648u,89,0,0,0,0,0,0,0,0,0,0,0,0,
76800,256,0,131840,148224,24297472,2048,24313856,4,0,1714176,1638400,8,768,11423744,2147483648u,2147483648u,11059200,24363008,27492352,5881856,5898240,295680,75264,692224,693760,561920,626688,549376,538624,562176,594688,593920,0,2147483648u,90,1065353216,1065353216,0,0,0,0,0,0,0,0,0,0,
76800,256,0,1180416,1196800,24309760,2048,24326144,4,0,1715200,1671168,8,768,11425792,2147483648u,2147483648u,11149312,24453120,27508736,5882368,5963776,312064,77312,694272,695808,561920,626688,627968,550912,562176,594688,593920,0,2147483648u,91,1065353216,0,0,0,0,0,0,0,0,0,0,0,
89088,768,0,222272,222272,29501440,65536,29632512,5,0,1728512,1703936,8,768,11071488,2147483648u,2147483648u,11239424,24182784,30081024,5947392,5767168,328448,71168,696320,697856,562944,627712,549504,552960,561152,595712,593920,0,2147483648u,88,0,0,0,0,0,0,0,0,0,0,0,0,
89088,768,0,222432,222432,29829120,65536,29960192,5,0,1729536,1736704,8,768,11073536,2147483648u,2147483648u,11329536,24272896,30097408,5947904,5832704,344832,73216,698368,699904,562944,627712,628480,548864,561152,595712,593920,0,2147483648u,89,0,0,0,0,0,0,0,0,0,0,0,0,
89088,768,0,164608,180992,24330240,2048,24346624,4,0,1730560,1769472,8,768,11431936,2147483648u,2147483648u,11419648,24363008,30113792,6012928,5898240,361216,75264,700416,701952,562944,627712,549504,555008,562176,595712,593920,0,2147483648u,90,1065353216,1065353216,0,0,0,0,0,0,0,0,0,0,
89088,768,0,1213184,1229568,24342528,2048,24358912,4,0,1731584,1802240,8,768,11433984,2147483648u,2147483648u,11509760,24453120,30130176,6013440,5963776,377600,77312,702464,704000,562944,627712,628480,550912,562176,595712,593920,0,2147483648u,91,1065353216,0,0,0,0,0,0,0,0,0,0,0,
101376,256,0,530496,530496,29515776,65536,29646848,5,0,2760704,2621440,8,768,13946880,2147483648u,2147483648u,13762560,24182784,27459584,6864896,6815744,787200,71168,753664,755200,570112,634880,550400,536576,569344,602880,602112,0,2147483648u,88,0,0,0,0,0,0,0,0,0,0,0,0,
101376,256,0,530656,530656,29843456,65536,29974528,5,0,2761728,2654208,8,768,13948928,2147483648u,2147483648u,13852672,24272896,27475968,6865408,6881280,803584,73216,755712,757248,570112,634880,636160,548864,569344,602880,602112,0,2147483648u,89,0,0,0,0,0,0,0,0,0,0,0,0,
101376,256,0,393984,410368,24186880,2048,24195072,4,0,2762752,2686976,8,768,14307328,2147483648u,2147483648u,13942784,24363008,27492352,6930432,6946816,819968,75264,757760,759296,570112,634880,550400,538624,570368,602880,602112,0,2147483648u,90,1065353216,1065353216,0,0,0,0,0,0,0,0,0,0,
101376,256,0,1377024,1393408,24522752,2048,24539136,4,2147483648u,2763776,2719744,8,768,14309376,2147483648u,2147483648u,14032896,24453120,27508736,6930944,7012352,836352,77312,759808,761344,570112,634880,636160,550912,570368,602880,602112,0,2147483648u,91,1065353216,0,0,0,0,0,0,0,0,0,0,0,
113664,768,0,574528,574528,29517824,65536,29648896,5,0,2777088,2752512,8,768,13955072,2147483648u,2147483648u,14123008,24182784,30081024,6995968,6815744,852736,71168,761856,763392,571136,635904,550528,552960,569344,603904,602112,0,2147483648u,88,0,0,0,0,0,0,0,0,0,0,0,0,
113664,768,0,574688,574688,29845504,65536,29976576,5,0,2778112,2785280,8,768,13957120,2147483648u,2147483648u,14213120,24272896,30097408,6996480,6881280,869120,73216,763904,765440,571136,635904,636672,548864,569344,603904,602112,0,2147483648u,89,0,0,0,0,0,0,0,0,0,0,0,0,
113664,768,0,426752,443136,24211456,2048,24227840,4,0,2779136,2818048,8,768,14315520,2147483648u,2147483648u,14303232,24363008,30113792,7061504,6946816,885504,75264,765952,767488,571136,635904,550528,555008,570368,603904,602112,0,2147483648u,90,1065353216,1065353216,0,0,0,0,0,0,0,0,0,0,
113664,768,0,1377024,1393408,24522752,2048,24539136,4,2147483648u,2780160,2850816,8,768,14317568,2147483648u,2147483648u,14393344,24453120,30130176,7062016,7012352,901888,77312,768000,769536,571136,635904,636672,550912,570368,603904,602112,0,2147483648u,91,1065353216,0,0,0,0,0,0,0,0,0,0,0,
77056,256,0,882752,882752,29532160,65536,29663232,5,0,3809280,3670016,8,768,16830464,2147483648u,2147483648u,16646144,24182784,27459584,7913472,7864320,1311488,71168,819200,820736,578304,643072,551424,536576,577536,611072,610304,0,2147483648u,88,0,0,0,0,0,0,0,0,0,0,0,0,
77056,256,0,882912,882912,29859840,65536,29990912,5,0,3810304,3702784,8,768,16832512,2147483648u,2147483648u,16736256,24272896,27475968,7913984,7929856,1327872,73216,821248,822784,578304,643072,644352,548864,577536,611072,610304,0,2147483648u,89,0,0,0,0,0,0,0,0,0,0,0,0,
77056,256,0,656128,672512,24440832,2048,24465408,4,0,3811328,3735552,8,768,17190912,2147483648u,2147483648u,16826368,24363008,27492352,7979008,7995392,1344256,75264,823296,824832,578304,643072,551424,538624,578560,611072,610304,0,2147483648u,90,1065353216,1065353216,0,0,0,0,0,0,0,0,0,0,
77056,256,0,1377024,1393408,24522752,2048,24539136,4,2147483648u,3812352,3768320,8,768,17192960,2147483648u,2147483648u,16916480,24453120,27508736,7979520,8060928,1360640,77312,825344,826880,578304,643072,644352,550912,578560,611072,610304,0,2147483648u,91,1065353216,0,0,0,0,0,0,0,0,0,0,0,
89344,768,0,926784,926784,29534208,65536,29665280,5,0,3825664,3801088,8,768,16838656,2147483648u,2147483648u,17006592,24182784,30081024,8044544,7864320,1377024,71168,827392,828928,579328,644096,551552,552960,577536,612096,610304,0,2147483648u,88,0,0,0,0,0,0,0,0,0,0,0,0,
89344,768,0,926944,926944,29861888,65536,29992960,5,0,3826688,3833856,8,768,16840704,2147483648u,2147483648u,17096704,24272896,30097408,8045056,7929856,1393408,73216,829440,830976,579328,644096,644864,548864,577536,612096,610304,0,2147483648u,89,0,0,0,0,0,0,0,0,0,0,0,0,
89344,768,0,688896,705280,24481792,2048,24498176,4,0,3827712,3866624,8,768,17199104,2147483648u,2147483648u,17186816,24363008,30113792,8110080,7995392,1409792,75264,831488,833024,579328,644096,551552,555008,578560,612096,610304,0,2147483648u,90,1065353216,1065353216,0,0,0,0,0,0,0,0,0,0,
89344,768,0,1377024,1393408,24522752,2048,24539136,4,2147483648u,3828736,3899392,8,768,17201152,2147483648u,2147483648u,17276928,24453120,30130176,8110592,8060928,1426176,77312,833536,835072,579328,644096,644864,550912,578560,612096,610304,0,2147483648u,91,1065353216,0,0,0,0,0,0,0,0,0,0,0,
101632,256,0,1235008,1235008,29548544,65536,29679616,5,0,4857856,4718592,8,768,19714048,2147483648u,2147483648u,19529728,24182784,27459584,8962048,8912896,1835776,71168,884736,886272,586496,651264,552448,536576,585728,619264,618496,0,2147483648u,88,0,0,0,0,0,0,0,0,0,0,0,0,
101632,256,0,1235168,1235168,29876224,65536,30007296,5,0,4858880,4751360,8,768,19716096,2147483648u,2147483648u,19619840,24272896,27475968,8962560,8978432,1852160,73216,886784,888320,586496,651264,652544,548864,585728,619264,618496,0,2147483648u,89,0,0,0,0,0,0,0,0,0,0,0,0,
101632,256,0,918272,934656,24379392,2048,24395776,4,0,4859904,4784128,8,768,20074496,2147483648u,2147483648u,19709952,24363008,27492352,9027584,9043968,1868544,75264,888832,890368,586496,651264,552448,538624,586752,619264,618496,0,2147483648u,90,1065353216,1065353216,0,0,0,0,0,0,0,0,0,0,
101632,256,0,1377024,1393408,24522752,2048,24539136,4,2147483648u,4860928,4816896,8,768,20076544,2147483648u,2147483648u,19800064,24453120,27508736,9028096,9109504,1884928,77312,890880,892416,586496,651264,652544,550912,586752,619264,618496,0,2147483648u,91,1065353216,0,0,0,0,0,0,0,0,0,0,0,
113920,768,0,1279040,1279040,29550592,65536,29681664,5,0,4874240,4849664,8,768,19722240,2147483648u,2147483648u,19890176,24182784,30081024,9093120,8912896,1901312,71168,892928,894464,587520,652288,552576,552960,585728,620288,618496,0,2147483648u,88,0,0,0,0,0,0,0,0,0,0,0,0,
113920,768,0,1279200,1279200,29878272,65536,30009344,5,0,4875264,4882432,8,768,19724288,2147483648u,2147483648u,19980288,24272896,30097408,9093632,8978432,1917696,73216,894976,896512,587520,652288,653056,548864,585728,620288,618496,0,2147483648u,89,0,0,0,0,0,0,0,0,0,0,0,0,
113920,768,0,951040,967424,24412160,2048,24428544,4,0,4876288,4915200,8,768,20082688,2147483648u,2147483648u,20070400,24363008,30113792,9158656,9043968,1934080,75264,897024,898560,587520,652288,552576,555008,586752,620288,618496,0,2147483648u,90,1065353216,1065353216,0,0,0,0,0,0,0,0,0,0,
113920,768,0,1377024,1393408,24522752,2048,24539136,4,2147483648u,4877312,4947968,8,768,20084736,2147483648u,2147483648u,20160512,24453120,30130176,9159168,9109504,1950464,77312,899072,900608,587520,652288,653056,550912,586752,620288,618496,0,2147483648u,91,1065353216,0,0,0,0,0,0,0,0,0,0,0,
77312,384,0,178560,178560,30154752,65536,30285824,5,0,1716224,1572864,8,896,11153408,2147483648u,2147483648u,10878976,24543232,27787264,5824512,5767168,263040,71424,688128,689920,562048,626688,553472,536576,561152,594816,593920,0,2147483648u,92,0,0,0,0,0,0,0,0,0,0,0,0,
77312,384,0,178720,178720,30482432,65536,30613504,5,0,1717248,1605632,8,896,11155456,2147483648u,2147483648u,10969088,24633344,27803648,5825024,5832704,279424,73472,690176,691968,562048,626688,628096,552960,561152,594816,593920,0,2147483648u,93,0,0,0,0,0,0,0,0,0,0,0,0,
77312,384,0,131968,148352,24657920,2048,24674304,4,0,1718272,1638400,8,896,11513856,2147483648u,2147483648u,11059200,24723456,27820032,5890048,5898240,295808,75520,692224,694016,562048,626688,553472,538624,562176,594816,593920,0,2147483648u,94,1065353216,1065353216,0,0,0,0,0,0,0,0,0,0,
77312,384,0,1180544,1196928,24670208,2048,24686592,4,0,1719296,1671168,8,896,11515904,2147483648u,2147483648u,11149312,24813568,27836416,5890560,5963776,312192,77568,694272,696064,562048,626688,628096,555008,562176,594816,593920,0,2147483648u,95,1065353216,0,0,0,0,0,0,0,0,0,0,0,
89600,896,0,222592,222592,30156800,65536,30287872,5,0,1732608,1703936,8,896,11161600,2147483648u,2147483648u,11239424,24543232,30408704,5955584,5767168,328576,71424,696320,698112,563072,627712,553600,552960,561152,595840,593920,0,2147483648u,92,0,0,0,0,0,0,0,0,0,0,0,0,
89600,896,0,222752,222752,30484480,65536,30615552,5,0,1733632,1736704,8,896,11163648,2147483648u,2147483648u,11329536,24633344,30425088,5956096,5832704,344960,73472,698368,700160,563072,627712,628608,552960,561152,595840,593920,0,2147483648u,93,0,0,0,0,0,0,0,0,0,0,0,0,
89600,896,0,164736,181120,24690688,2048,24707072,4,0,1734656,1769472,8,896,11522048,2147483648u,2147483648u,11419648,24723456,30441472,6021120,5898240,361344,75520,700416,702208,563072,627712,553600,555008,562176,595840,593920,0,2147483648u,94,1065353216,1065353216,0,0,0,0,0,0,0,0,0,0,
89600,896,0,1213312,1229696,24702976,2048,24719360,4,0,1735680,1802240,8,896,11524096,2147483648u,2147483648u,11509760,24813568,30457856,6021632,5963776,377728,77568,702464,704256,563072,627712,628608,555008,562176,595840,593920,0,2147483648u,95,1065353216,0,0,0,0,0,0,0,0,0,0,0,
101888,384,0,530816,530816,30171136,65536,30302208,5,0,2764800,2621440,8,896,14036992,2147483648u,2147483648u,13762560,24543232,27787264,6873088,6815744,787328,71424,753664,755456,570240,634880,554496,536576,569344,603008,602112,0,2147483648u,92,0,0,0,0,0,0,0,0,0,0,0,0,
101888,384,0,530976,530976,30498816,65536,30629888,5,0,2765824,2654208,8,896,14039040,2147483648u,2147483648u,13852672,24633344,27803648,6873600,6881280,803712,73472,755712,757504,570240,634880,636288,552960,569344,603008,602112,0,2147483648u,93,0,0,0,0,0,0,0,0,0,0,0,0,
101888,384,0,394112,410496,24547328,2048,24555520,4,0,2766848,2686976,8,896,14397440,2147483648u,2147483648u,13942784,24723456,27820032,6938624,6946816,820096,75520,757760,759552,570240,634880,554496,538624,570368,603008,602112,0,2147483648u,94,1065353216,1065353216,0,0,0,0,0,0,0,0,0,0,
101888,384,0,1377152,1393536,24883200,2048,24899584,4,2147483648u,2767872,2719744,8,896,14399488,2147483648u,2147483648u,14032896,24813568,27836416,6939136,7012352,836480,77568,759808,761600,570240,634880,636288,555008,570368,603008,602112,0,2147483648u,95,1065353216,0,0,0,0,0,0,0,0,0,0,0,
114176,896,0,574848,574848,30173184,65536,30304256,5,0,2781184,2752512,8,896,14045184,2147483648u,2147483648u,14123008,24543232,30408704,7004160,6815744,852864,71424,761856,763648,571264,635904,554624,552960,569344,604032,602112,0,2147483648u,92,0,0,0,0,0,0,0,0,0,0,0,0,
114176,896,0,575008,575008,30500864,65536,30631936,5,0,2782208,2785280,8,896,14047232,2147483648u,2147483648u,14213120,24633344,30425088,7004672,6881280,869248,73472,763904,765696,571264,635904,636800,552960,569344,604032,602112,0,2147483648u,93,0,0,0,0,0,0,0,0,0,0,0,0,
114176,896,0,426880,443264,24571904,2048,24588288,4,0,2783232,2818048,8,896,14405632,2147483648u,2147483648u,14303232,24723456,30441472,7069696,6946816,885632,75520,765952,767744,571264,635904,554624,555008,570368,604032,602112,0,2147483648u,94,1065353216,1065353216,0,0,0,0,0,0,0,0,0,0,
114176,896,0,1377152,1393536,24883200,2048,24899584,4,2147483648u,2784256,2850816,8,896,14407680,2147483648u,2147483648u,14393344,24813568,30457856,7070208,7012352,902016,77568,768000,769792,571264,635904,636800,555008,570368,604032,602112,0,2147483648u,95,1065353216,0,0,0,0,0,0,0,0,0,0,0,
77568,384,0,883072,883072,30187520,65536,30318592,5,0,3813376,3670016,8,896,16920576,2147483648u,2147483648u,16646144,24543232,27787264,7921664,7864320,1311616,71424,819200,820992,578432,643072,555520,536576,577536,611200,610304,0,2147483648u,92,0,0,0,0,0,0,0,0,0,0,0,0,
77568,384,0,883232,883232,30515200,65536,30646272,5,0,3814400,3702784,8,896,16922624,2147483648u,2147483648u,16736256,24633344,27803648,7922176,7929856,1328000,73472,821248,823040,578432,643072,644480,552960,577536,611200,610304,0,2147483648u,93,0,0,0,0,0,0,0,0,0,0,0,0,
77568,384,0,656256,672640,24801280,2048,24825856,4,0,3815424,3735552,8,896,17281024,2147483648u,2147483648u,16826368,24723456,27820032,7987200,7995392,1344384,75520,823296,825088,578432,643072,555520,538624,578560,611200,610304,0,2147483648u,94,1065353216,1065353216,0,0,0,0,0,0,0,0,0,0,
77568,384,0,1377152,1393536,24883200,2048,24899584,4,2147483648u,3816448,3768320,8,896,17283072,2147483648u,2147483648u,16916480,24813568,27836416,7987712,8060928,1360768,77568,825344,827136,578432,643072,644480,555008,578560,611200,610304,0,2147483648u,95,1065353216,0,0,0,0,0,0,0,0,0,0,0,
89856,896,0,927104,927104,30189568,65536,30320640,5,0,3829760,3801088,8,896,16928768,2147483648u,2147483648u,17006592,24543232,30408704,8052736,7864320,1377152,71424,827392,829184,579456,644096,555648,552960,577536,612224,610304,0,2147483648u,92,0,0,0,0,0,0,0,0,0,0,0,0,
89856,896,0,927264,927264,30517248,65536,30648320,5,0,3830784,3833856,8,896,16930816,2147483648u,2147483648u,17096704,24633344,30425088,8053248,7929856,1393536,73472,829440,831232,579456,644096,644992,552960,577536,612224,610304,0,2147483648u,93,0,0,0,0,0,0,0,0,0,0,0,0,
89856,896,0,689024,705408,24842240,2048,24858624,4,0,3831808,3866624,8,896,17289216,2147483648u,2147483648u,17186816,24723456,30441472,8118272,7995392,1409920,75520,831488,833280,579456,644096,555648,555008,578560,612224,610304,0,2147483648u,94,1065353216,1065353216,0,0,0,0,0,0,0,0,0,0,
89856,896,0,1377152,1393536,24883200,2048,24899584,4,2147483648u,3832832,3899392,8,896,17291264,2147483648u,2147483648u,17276928,24813568,30457856,8118784,8060928,1426304,77568,833536,835328,579456,644096,644992,555008,578560,612224,610304,0,2147483648u,95,1065353216,0,0,0,0,0,0,0,0,0,0,0,
102144,384,0,1235328,1235328,30203904,65536,30334976,5,0,4861952,4718592,8,896,19804160,2147483648u,2147483648u,19529728,24543232,27787264,8970240,8912896,1835904,71424,884736,886528,586624,651264,556544,536576,585728,619392,618496,0,2147483648u,92,0,0,0,0,0,0,0,0,0,0,0,0,
102144,384,0,1235488,1235488,30531584,65536,30662656,5,0,4862976,4751360,8,896,19806208,2147483648u,2147483648u,19619840,24633344,27803648,8970752,8978432,1852288,73472,886784,888576,586624,651264,652672,552960,585728,619392,618496,0,2147483648u,93,0,0,0,0,0,0,0,0,0,0,0,0,
102144,384,0,918400,934784,24739840,2048,24756224,4,0,4864000,4784128,8,896,20164608,2147483648u,2147483648u,19709952,24723456,27820032,9035776,9043968,1868672,75520,888832,890624,586624,651264,556544,538624,586752,619392,618496,0,2147483648u,94,1065353216,1065353216,0,0,0,0,0,0,0,0,0,0,
102144,384,0,1377152,1393536,24883200,2048,24899584,4,2147483648u,4865024,4816896,8,896,20166656,2147483648u,2147483648u,19800064,24813568,27836416,9036288,9109504,1885056,77568,890880,892672,586624,651264,652672,555008,586752,619392,618496,0,2147483648u,95,1065353216,0,0,0,0,0,0,0,0,0,0,0,
114432,896,0,1279360,1279360,30205952,65536,30337024,5,0,4878336,4849664,8,896,19812352,2147483648u,2147483648u,19890176,24543232,30408704,9101312,8912896,1901440,71424,892928,894720,587648,652288,556672,552960,585728,620416,618496,0,2147483648u,92,0,0,0,0,0,0,0,0,0,0,0,0,
114432,896,0,1279520,1279520,30533632,65536,30664704,5,0,4879360,4882432,8,896,19814400,2147483648u,2147483648u,19980288,24633344,30425088,9101824,8978432,1917824,73472,894976,896768,587648,652288,653184,552960,585728,620416,618496,0,2147483648u,93,0,0,0,0,0,0,0,0,0,0,0,0,
114432,896,0,951168,967552,24772608,2048,24788992,4,0,4880384,4915200,8,896,20172800,2147483648u,2147483648u,20070400,24723456,30441472,9166848,9043968,1934208,75520,897024,898816,587648,652288,556672,555008,586752,620416,618496,0,2147483648u,94,1065353216,1065353216,0,0,0,0,0,0,0,0,0,0,
114432,896,0,1377152,1393536,24883200,2048,24899584,4,2147483648u,4881408,4947968,8,896,20174848,2147483648u,2147483648u,20160512,24813568,30457856,9167360,9109504,1950592,77568,899072,900864,587648,652288,653184,555008,586752,620416,618496,0,2147483648u,95,1065353216,0,0,0,0,0,0,0,0,0,0,0,
77568,0,2147483648u,264192,264192,25178112,65536,25309184,6,0,1835008,1835008,12,0,11599872,0,0,11599872,22020096,25165824,6029312,6029312,393216,69632,704512,704512,563200,628736,525056,524288,563200,595968,595968,0,2147483648u,96,0,0,0,0,0,0,0,0,0,0,0,0,
77568,0,2147483648u,264384,264384,25571328,65536,25702400,6,0,1836032,1867776,12,0,11601920,0,0,11689984,22110208,25182208,6029824,6094848,409600,71680,706560,706560,563200,628736,628736,524288,563200,595968,595968,0,2147483648u,97,0,0,0,0,0,0,0,0,0,0,0,0,
77568,0,2147483648u,196608,212992,22200320,2048,22208512,4,0,1837056,1900544,12,0,11960320,0,0,11780096,22200320,25198592,6094848,6160384,425984,73728,708608,708608,563200,628736,525056,526336,564224,595968,595968,0,2147483648u,98,1065353216,1065353216,0,0,0,0,0,0,0,0,0,0,
77568,0,2147483648u,1245184,1261568,22220800,2048,22237184,4,0,1838080,1933312,12,0,11962368,0,0,11870208,22290432,25214976,6095360,6225920,442368,75776,710656,710656,563200,628736,628736,526336,564224,595968,595968,0,2147483648u,99,1065353216,0,0,0,0,0,0,0,0,0,0,0,
89856,512,2147483648u,308224,308224,25180160,65536,25311232,6,0,1851392,1966080,12,0,11608064,2147483648u,2147483648u,11960320,22020096,28114944,6160384,6029312,458752,69632,712704,712704,564224,629760,525184,540672,563200,596992,595968,0,2147483648u,96,0,0,0,0,0,0,0,0,0,0,0,0,
89856,512,2147483648u,308416,308416,25573376,65536,25704448,6,0,1852416,1998848,12,0,11610112,2147483648u,2147483648u,12050432,22110208,28131328,6160896,6094848,475136,71680,714752,714752,564224,629760,629248,524288,563200,596992,595968,0,2147483648u,97,0,0,0,0,0,0,0,0,0,0,0,0,
89856,512,2147483648u,229376,245760,22224896,2048,22241280,4,0,1853440,2031616,12,0,11968512,2147483648u,2147483648u,12140544,22200320,28147712,6225920,6160384,491520,73728,716800,716800,564224,629760,525184,542720,564224,596992,595968,0,2147483648u,98,1065353216,1065353216,0,0,0,0,0,0,0,0,0,0,
89856,512,2147483648u,1277952,1294336,22253568,2048,22269952,4,0,1854464,2064384,12,0,11970560,2147483648u,2147483648u,12230656,22290432,28164096,6226432,6225920,507904,75776,718848,718848,564224,629760,629248,526336,564224,596992,595968,0,2147483648u,99,1065353216,0,0,0,0,0,0,0,0,0,0,0,
102144,0,2147483648u,616448,616448,25194496,65536,25325568,6,0,2883584,2883584,12,0,14483456,0,0,14483456,22020096,25165824,7077888,7077888,917504,69632,770048,770048,571392,636928,526080,524288,571392,604160,604160,0,2147483648u,96,0,0,0,0,0,0,0,0,0,0,0,0,
102144,0,2147483648u,616640,616640,25587712,65536,25718784,6,0,2884608,2916352,12,0,14485504,0,0,14573568,22110208,25182208,7078400,7143424,933888,71680,772096,772096,571392,636928,636928,524288,571392,604160,604160,0,2147483648u,97,0,0,0,0,0,0,0,0,0,0,0,0,
102144,0,2147483648u,458752,475136,22081536,2048,22097920,4,0,2885632,2949120,12,0,14843904,0,0,14663680,22200320,25198592,7143424,7208960,950272,73728,774144,774144,571392,636928,526080,526336,572416,604160,604160,0,2147483648u,98,1065353216,1065353216,0,0,0,0,0,0,0,0,0,0,
102144,0,2147483648u,1376256,1392640,22360064,2048,22376448,4,2147483648u,2886656,2981888,12,0,14845952,0,0,14753792,22290432,25214976,7143936,7274496,966656,75776,776192,776192,571392,636928,636928,526336,572416,604160,604160,0,2147483648u,99,1065353216,0,0,0,0,0,0,0,0,0,0,0,
114432,512,2147483648u,660480,660480,25196544,65536,25327616,6,0,2899968,3014656,12,0,14491648,2147483648u,2147483648u,14843904,22020096,28114944,7208960,7077888,983040,69632,778240,778240,572416,637952,526208,540672,571392,605184,604160,0,2147483648u,96,0,0,0,0,0,0,0,0,0,0,0,0,
114432,512,2147483648u,660672,660672,25589760,65536,25720832,6,0,2900992,3047424,12,0,14493696,2147483648u,2147483648u,14934016,22110208,28131328,7209472,7143424,999424,71680,780288,780288,572416,637952,637440,524288,571392,605184,604160,0,2147483648u,97,0,0,0,0,0,0,0,0,0,0,0,0,
114432,512,2147483648u,491520,507904,22114304,2048,22122496,4,0,2902016,3080192,12,0,14852096,2147483648u,2147483648u,15024128,22200320,28147712,7274496,7208960,1015808,73728,782336,782336,572416,637952,526208,542720,572416,605184,604160,0,2147483648u,98,1065353216,1065353216,0,0,0,0,0,0,0,0,0,0,
114432,512,2147483648u,1376256,1392640,22360064,2048,22376448,4,2147483648u,2903040,3112960,12,0,14854144,2147483648u,2147483648u,15114240,22290432,28164096,7275008,7274496,1032192,75776,784384,784384,572416,637952,637440,526336,572416,605184,604160,0,2147483648u,99,1065353216,0,0,0,0,0,0,0,0,0,0,0,
77568,0,2147483648u,968704,968704,25210880,65536,25341952,6,0,3932160,3932160,12,0,17367040,0,0,17367040,22020096,25165824,8126464,8126464,1441792,69632,835584,835584,579584,645120,527104,524288,579584,612352,612352,0,2147483648u,96,0,0,0,0,0,0,0,0,0,0,0,0,
77568,0,2147483648u,968896,968896,25604096,65536,25735168,6,0,3933184,3964928,12,0,17369088,0,0,17457152,22110208,25182208,8126976,8192000,1458176,71680,837632,837632,579584,645120,645120,524288,579584,612352,612352,0,2147483648u,97,0,0,0,0,0,0,0,0,0,0,0,0,
77568,0,2147483648u,720896,737280,22351872,2048,22368256,4,0,3934208,3997696,12,0,17727488,0,0,17547264,22200320,25198592,8192000,8257536,1474560,73728,839680,839680,579584,645120,527104,526336,580608,612352,612352,0,2147483648u,98,1065353216,1065353216,0,0,0,0,0,0,0,0,0,0,
77568,0,2147483648u,1376256,1392640,22360064,2048,22376448,4,2147483648u,3935232,4030464,12,0,17729536,0,0,17637376,22290432,25214976,8192512,8323072,1490944,75776,841728,841728,579584,645120,645120,526336,580608,612352,612352,0,2147483648u,99,1065353216,0,0,0,0,0,0,0,0,0,0,0,
89856,512,2147483648u,1012736,1012736,25212928,65536,25344000,6,0,3948544,4063232,12,0,17375232,2147483648u,2147483648u,17727488,22020096,28114944,8257536,8126464,1507328,69632,843776,843776,580608,646144,527232,540672,579584,613376,612352,0,2147483648u,96,0,0,0,0,0,0,0,0,0,0,0,0,
89856,512,2147483648u,1012928,1012928,25606144,65536,25737216,6,0,3949568,4096000,12,0,17377280,2147483648u,2147483648u,17817600,22110208,28131328,8258048,8192000,1523712,71680,845824,845824,580608,646144,645632,524288,579584,613376,612352,0,2147483648u,97,0,0,0,0,0,0,0,0,0,0,0,0,
89856,512,2147483648u,753664,770048,22036480,2048,22052864,4,0,3950592,4128768,12,0,17735680,2147483648u,2147483648u,17907712,22200320,28147712,8323072,8257536,1540096,73728,847872,847872,580608,646144,527232,542720,580608,613376,612352,0,2147483648u,98,1065353216,1065353216,0,0,0,0,0,0,0,0,0,0,
89856,512,2147483648u,1376256,1392640,22360064,2048,22376448,4,2147483648u,3951616,4161536,12,0,17737728,2147483648u,2147483648u,17997824,22290432,28164096,8323584,8323072,1556480,75776,849920,849920,580608,646144,645632,526336,580608,613376,612352,0,2147483648u,99,1065353216,0,0,0,0,0,0,0,0,0,0,0,
102144,0,2147483648u,1320960,1320960,25227264,65536,25358336,6,0,4980736,4980736,12,0,20250624,0,0,20250624,22020096,25165824,9175040,9175040,1966080,69632,901120,901120,587776,653312,528128,524288,587776,620544,620544,0,2147483648u,96,0,0,0,0,0,0,0,0,0,0,0,0,
102144,0,2147483648u,1321152,1321152,25620480,65536,25751552,6,0,4981760,5013504,12,0,20252672,0,0,20340736,22110208,25182208,9175552,9240576,1982464,71680,903168,903168,587776,653312,653312,524288,587776,620544,620544,0,2147483648u,97,0,0,0,0,0,0,0,0,0,0,0,0,
102144,0,2147483648u,983040,999424,22282240,2048,22306816,4,0,4982784,5046272,12,0,20611072,0,0,20430848,22200320,25198592,9240576,9306112,1998848,73728,905216,905216,587776,653312,528128,526336,588800,620544,620544,0,2147483648u,98,1065353216,1065353216,0,0,0,0,0,0,0,0,0,0,
102144,0,2147483648u,1376256,1392640,22360064,2048,22376448,4,2147483648u,4983808,5079040,12,0,20613120,0,0,20520960,22290432,25214976,9241088,9371648,2015232,75776,907264,907264,587776,653312,653312,526336,588800,620544,620544,0,2147483648u,99,1065353216,0,0,0,0,0,0,0,0,0,0,0,
114432,512,2147483648u,1364992,1364992,25229312,65536,25360384,6,0,4997120,5111808,12,0,20258816,2147483648u,2147483648u,20611072,22020096,28114944,9306112,9175040,2031616,69632,909312,909312,588800,654336,528256,540672,587776,621568,620544,0,2147483648u,96,0,0,0,0,0,0,0,0,0,0,0,0,
114432,512,2147483648u,1365184,1365184,25622528,65536,25753600,6,0,4998144,5144576,12,0,20260864,2147483648u,2147483648u,20701184,22110208,28131328,9306624,9240576,2048000,71680,911360,911360,588800,654336,653824,524288,587776,621568,620544,0,2147483648u,97,0,0,0,0,0,0,0,0,0,0,0,0,
114432,512,2147483648u,1015808,1032192,22323200,2048,22339584,4,0,4999168,5177344,12,0,20619264,2147483648u,2147483648u,20791296,22200320,28147712,9371648,9306112,2064384,73728,913408,913408,588800,654336,528256,542720,588800,621568,620544,0,2147483648u,98,1065353216,1065353216,0,0,0,0,0,0,0,0,0,0,
114432,512,2147483648u,1376256,1392640,22360064,2048,22376448,4,2147483648u,5000192,5210112,12,0,20621312,2147483648u,2147483648u,20881408,22290432,28164096,9372160,9371648,2080768,75776,915456,915456,588800,654336,653824,526336,588800,621568,620544,0,2147483648u,99,1065353216,0,0,0,0,0,0,0,0,0,0,0,
77568,128,2147483648u,264576,264576,25964544,65536,26095616,6,0,1839104,1835008,12,128,11689984,0,0,11599872,22380544,25559040,6037504,6029312,393344,69888,704512,704768,563328,628736,529152,524288,563200,596096,595968,0,2147483648u,100,0,0,0,0,0,0,0,0,0,0,0,0,
77568,128,2147483648u,264768,264768,26357760,65536,26488832,6,0,1840128,1867776,12,128,11692032,0,0,11689984,22470656,25575424,6038016,6094848,409728,71936,706560,706816,563328,628736,628864,528384,563200,596096,595968,0,2147483648u,101,0,0,0,0,0,0,0,0,0,0,0,0,
77568,128,2147483648u,196736,213120,22560768,2048,22568960,4,0,1841152,1900544,12,128,12050432,0,0,11780096,22560768,25591808,6103040,6160384,426112,73984,708608,708864,563328,628736,529152,526336,564224,596096,595968,0,2147483648u,102,1065353216,1065353216,0,0,0,0,0,0,0,0,0,0,
77568,128,2147483648u,1245312,1261696,22581248,2048,22597632,4,0,1842176,1933312,12,128,12052480,0,0,11870208,22650880,25608192,6103552,6225920,442496,76032,710656,710912,563328,628736,628864,530432,564224,596096,595968,0,2147483648u,103,1065353216,0,0,0,0,0,0,0,0,0,0,0,
89856,640,2147483648u,308608,308608,25966592,65536,26097664,6,0,1855488,1966080,12,128,11698176,2147483648u,2147483648u,11960320,22380544,28442624,6168576,6029312,458880,69888,712704,712960,564352,629760,529280,540672,563200,597120,595968,0,2147483648u,100,0,0,0,0,0,0,0,0,0,0,0,0,
89856,640,2147483648u,308800,308800,26359808,65536,26490880,6,0,1856512,1998848,12,128,11700224,2147483648u,2147483648u,12050432,22470656,28459008,6169088,6094848,475264,71936,714752,715008,564352,629760,629376,528384,563200,597120,595968,0,2147483648u,101,0,0,0,0,0,0,0,0,0,0,0,0,
89856,640,2147483648u,229504,245888,22585344,2048,22601728,4,0,1857536,2031616,12,128,12058624,2147483648u,2147483648u,12140544,22560768,28475392,6234112,6160384,491648,73984,716800,717056,564352,629760,529280,542720,564224,597120,595968,0,2147483648u,102,1065353216,1065353216,0,0,0,0,0,0,0,0,0,0,
89856,640,2147483648u,1278080,1294464,22614016,2048,22630400,4,0,1858560,2064384,12,128,12060672,2147483648u,2147483648u,12230656,22650880,28491776,6234624,6225920,508032,76032,718848,719104,564352,629760,629376,530432,564224,597120,595968,0,2147483648u,103,1065353216,0,0,0,0,0,0,0,0,0,0,0,
102144,128,2147483648u,616832,616832,25980928,65536,26112000,6,0,2887680,2883584,12,128,14573568,0,0,14483456,22380544,25559040,7086080,7077888,917632,69888,770048,770304,571520,636928,530176,524288,571392,604288,604160,0,2147483648u,100,0,0,0,0,0,0,0,0,0,0,0,0,
102144,128,2147483648u,617024,617024,26374144,65536,26505216,6,0,2888704,2916352,12,128,14575616,0,0,14573568,22470656,25575424,7086592,7143424,934016,71936,772096,772352,571520,636928,637056,528384,571392,604288,604160,0,2147483648u,101,0,0,0,0,0,0,0,0,0,0,0,0,
102144,128,2147483648u,458880,475264,22441984,2048,22458368,4,0,2889728,2949120,12,128,14934016,0,0,14663680,22560768,25591808,7151616,7208960,950400,73984,774144,774400,571520,636928,530176,526336,572416,604288,604160,0,2147483648u,102,1065353216,1065353216,0,0,0,0,0,0,0,0,0,0,
102144,128,2147483648u,1376384,1392768,22720512,2048,22736896,4,2147483648u,2890752,2981888,12,128,14936064,0,0,14753792,22650880,25608192,7152128,7274496,966784,76032,776192,776448,571520,636928,637056,530432,572416,604288,604160,0,2147483648u,103,1065353216,0,0,0,0,0,0,0,0,0,0,0,
114432,640,2147483648u,660864,660864,25982976,65536,26114048,6,0,2904064,3014656,12,128,14581760,2147483648u,2147483648u,14843904,22380544,28442624,7217152,7077888,983168,69888,778240,778496,572544,637952,530304,540672,571392,605312,604160,0,2147483648u,100,0,0,0,0,0,0,0,0,0,0,0,0,
114432,640,2147483648u,661056,661056,26376192,65536,26507264,6,0,2905088,3047424,12,128,14583808,2147483648u,2147483648u,14934016,22470656,28459008,7217664,7143424,999552,71936,780288,780544,572544,637952,637568,528384,571392,605312,604160,0,2147483648u,101,0,0,0,0,0,0,0,0,0,0,0,0,
114432,640,2147483648u,491648,508032,22474752,2048,22482944,4,0,2906112,3080192,12,128,14942208,2147483648u,2147483648u,15024128,22560768,28475392,7282688,7208960,1015936,73984,782336,782592,572544,637952,530304,542720,572416,605312,604160,0,2147483648u,102,1065353216,1065353216,0,0,0,0,0,0,0,0,0,0,
114432,640,2147483648u,1376384,1392768,22720512,2048,22736896,4,2147483648u,2907136,3112960,12,128,14944256,2147483648u,2147483648u,15114240,22650880,28491776,7283200,7274496,1032320,76032,784384,784640,572544,637952,637568,530432,572416,605312,604160,0,2147483648u,103,1065353216,0,0,0,0,0,0,0,0,0,0,0,
77568,128,2147483648u,969088,969088,25997312,65536,26128384,6,0,3936256,3932160,12,128,17457152,0,0,17367040,22380544,25559040,8134656,8126464,1441920,69888,835584,835840,579712,645120,531200,524288,579584,612480,612352,0,2147483648u,100,0,0,0,0,0,0,0,0,0,0,0,0,
77568,128,2147483648u,969280,969280,26390528,65536,26521600,6,0,3937280,3964928,12,128,17459200,0,0,17457152,22470656,25575424,8135168,8192000,1458304,71936,837632,837888,579712,645120,645248,528384,579584,612480,612352,0,2147483648u,101,0,0,0,0,0,0,0,0,0,0,0,0,
77568,128,2147483648u,721024,737408,22712320,2048,22728704,4,0,3938304,3997696,12,128,17817600,0,0,17547264,22560768,25591808,8200192,8257536,1474688,73984,839680,839936,579712,645120,531200,526336,580608,612480,612352,0,2147483648u,102,1065353216,1065353216,0,0,0,0,0,0,0,0,0,0,
77568,128,2147483648u,1376384,1392768,22720512,2048,22736896,4,2147483648u,3939328,4030464,12,128,17819648,0,0,17637376,22650880,25608192,8200704,8323072,1491072,76032,841728,841984,579712,645120,645248,530432,580608,612480,612352,0,2147483648u,103,1065353216,0,0,0,0,0,0,0,0,0,0,0,
89856,640,2147483648u,1013120,1013120,25999360,65536,26130432,6,0,3952640,4063232,12,128,17465344,2147483648u,2147483648u,17727488,22380544,28442624,8265728,8126464,1507456,69888,843776,844032,580736,646144,531328,540672,579584,613504,612352,0,2147483648u,100,0,0,0,0,0,0,0,0,0,0,0,0,
89856,640,2147483648u,1013312,1013312,26392576,65536,26523648,6,0,3953664,4096000,12,128,17467392,2147483648u,2147483648u,17817600,22470656,28459008,8266240,8192000,1523840,71936,845824,846080,580736,646144,645760,528384,579584,613504,612352,0,2147483648u,101,0,0,0,0,0,0,0,0,0,0,0,0,
89856,640,2147483648u,753792,770176,22396928,2048,22413312,4,0,3954688,4128768,12,128,17825792,2147483648u,2147483648u,17907712,22560768,28475392,8331264,8257536,1540224,73984,847872,848128,580736,646144,531328,542720,580608,613504,612352,0,2147483648u,102,1065353216,1065353216,0,0,0,0,0,0,0,0,0,0,
89856,640,2147483648u,1376384,1392768,22720512,2048,22736896,4,2147483648u,3955712,4161536,12,128,17827840,2147483648u,2147483648u,17997824,22650880,28491776,8331776,8323072,1556608,76032,849920,850176,580736,646144,645760,530432,580608,613504,612352,0,2147483648u,103,1065353216,0,0,0,0,0,0,0,0,0,0,0,
102144,128,2147483648u,1321344,1321344,26013696,65536,26144768,6,0,4984832,4980736,12,128,20340736,0,0,20250624,22380544,25559040,9183232,9175040,1966208,69888,901120,901376,587904,653312,532224,524288,587776,620672,620544,0,2147483648u,100,0,0,0,0,0,0,0,0,0,0,0,0,
102144,128,2147483648u,1321536,1321536,26406912,65536,26537984,6,0,4985856,5013504,12,128,20342784,0,0,20340736,22470656,25575424,9183744,9240576,1982592,71936,903168,903424,587904,653312,653440,528384,587776,620672,620544,0,2147483648u,101,0,0,0,0,0,0,0,0,0,0,0,0,
102144,128,2147483648u,983168,999552,22642688,2048,22667264,4,0,4986880,5046272,12,128,20701184,0,0,20430848,22560768,25591808,9248768,9306112,1998976,73984,905216,905472,587904,653312,532224,526336,588800,620672,620544,0,2147483648u,102,1065353216,1065353216,0,0,0,0,0,0,0,0,0,0,
102144,128,2147483648u,1376384,1392768,22720512,2048,22736896,4,2147483648u,4987904,5079040,12,128,20703232,0,0,20520960,22650880,25608192,9249280,9371648,2015360,76032,907264,907520,587904,653312,653440,530432,588800,620672,620544,0,2147483648u,103,1065353216,0,0,0,0,0,0,0,0,0,0,0,
114432,640,2147483648u,1365376,1365376,26015744,65536,26146816,6,0,5001216,5111808,12,128,20348928,2147483648u,2147483648u,20611072,22380544,28442624,9314304,9175040,2031744,69888,909312,909568,588928,654336,532352,540672,587776,621696,620544,0,2147483648u,100,0,0,0,0,0,0,0,0,0,0,0,0,
114432,640,2147483648u,1365568,1365568,26408960,65536,26540032,6,0,5002240,5144576,12,128,20350976,2147483648u,2147483648u,20701184,22470656,28459008,9314816,9240576,2048128,71936,911360,911616,588928,654336,653952,528384,587776,621696,620544,0,2147483648u,101,0,0,0,0,0,0,0,0,0,0,0,0,
114432,640,2147483648u,1015936,1032320,22683648,2048,22700032,4,0,5003264,5177344,12,128,20709376,2147483648u,2147483648u,20791296,22560768,28475392,9379840,9306112,2064512,73984,913408,913664,588928,654336,532352,542720,588800,621696,620544,0,2147483648u,102,1065353216,1065353216,0,0,0,0,0,0,0,0,0,0,
114432,640,2147483648u,1376384,1392768,22720512,2048,22736896,4,2147483648u,5004288,5210112,12,128,20711424,2147483648u,2147483648u,20881408,22650880,28491776,9380352,9371648,2080896,76032,915456,915712,588928,654336,653952,530432,588800,621696,620544,0,2147483648u,103,1065353216,0,0,0,0,0,0,0,0,0,0,0,
77568,256,2147483648u,264960,264960,26750976,65536,26882048,6,0,1843200,1835008,12,256,11780096,0,0,11599872,22740992,25952256,6045696,6029312,393472,70144,704512,705024,563456,628736,533248,528384,563200,596224,595968,0,2147483648u,104,0,0,0,0,0,0,0,0,0,0,0,0,
77568,256,2147483648u,265152,265152,27144192,65536,27275264,6,0,1844224,1867776,12,256,11782144,0,0,11689984,22831104,25968640,6046208,6094848,409856,72192,706560,707072,563456,628736,628992,532480,563200,596224,595968,0,2147483648u,105,0,0,0,0,0,0,0,0,0,0,0,0,
77568,256,2147483648u,196864,213248,22921216,2048,22929408,4,0,1845248,1900544,12,256,12140544,0,0,11780096,22921216,25985024,6111232,6160384,426240,74240,708608,709120,563456,628736,533248,530432,564224,596224,595968,0,2147483648u,106,1065353216,1065353216,0,0,0,0,0,0,0,0,0,0,
77568,256,2147483648u,1245440,1261824,22941696,2048,22958080,4,0,1846272,1933312,12,256,12142592,0,0,11870208,23011328,26001408,6111744,6225920,442624,76288,710656,711168,563456,628736,628992,534528,564224,596224,595968,0,2147483648u,107,1065353216,0,0,0,0,0,0,0,0,0,0,0,
89856,768,2147483648u,308992,308992,26753024,65536,26884096,6,0,1859584,1966080,12,256,11788288,2147483648u,2147483648u,11960320,22740992,28770304,6176768,6029312,459008,70144,712704,713216,564480,629760,533376,544768,563200,597248,595968,0,2147483648u,104,0,0,0,0,0,0,0,0,0,0,0,0,
89856,768,2147483648u,309184,309184,27146240,65536,27277312,6,0,1860608,1998848,12,256,11790336,2147483648u,2147483648u,12050432,22831104,28786688,6177280,6094848,475392,72192,714752,715264,564480,629760,629504,532480,563200,597248,595968,0,2147483648u,105,0,0,0,0,0,0,0,0,0,0,0,0,
89856,768,2147483648u,229632,246016,22945792,2048,22962176,4,0,1861632,2031616,12,256,12148736,2147483648u,2147483648u,12140544,22921216,28803072,6242304,6160384,491776,74240,716800,717312,564480,629760,533376,546816,564224,597248,595968,0,2147483648u,106,1065353216,1065353216,0,0,0,0,0,0,0,0,0,0,
89856,768,2147483648u,1278208,1294592,22974464,2048,22990848,4,0,1862656,2064384,12,256,12150784,2147483648u,2147483648u,12230656,23011328,28819456,6242816,6225920,508160,76288,718848,719360,564480,629760,629504,534528,564224,597248,595968,0,2147483648u,107,1065353216,0,0,0,0,0,0,0,0,0,0,0,
102144,256,2147483648u,617216,617216,26767360,65536,26898432,6,0,2891776,2883584,12,256,14663680,0,0,14483456,22740992,25952256,7094272,7077888,917760,70144,770048,770560,571648,636928,534272,528384,571392,604416,604160,0,2147483648u,104,0,0,0,0,0,0,0,0,0,0,0,0,
102144,256,2147483648u,617408,617408,27160576,65536,27291648,6,0,2892800,2916352,12,256,14665728,0,0,14573568,22831104,25968640,7094784,7143424,934144,72192,772096,772608,571648,636928,637184,532480,571392,604416,604160,0,2147483648u,105,0,0,0,0,0,0,0,0,0,0,0,0,
102144,256,2147483648u,459008,475392,22802432,2048,22818816,4,0,2893824,2949120,12,256,15024128,0,0,14663680,22921216,25985024,7159808,7208960,950528,74240,774144,774656,571648,636928,534272,530432,572416,604416,604160,0,2147483648u,106,1065353216,1065353216,0,0,0,0,0,0,0,0,0,0,
102144,256,2147483648u,1376512,1392896,23080960,2048,23097344,4,2147483648u,2894848,2981888,12,256,15026176,0,0,14753792,23011328,26001408,7160320,7274496,966912,76288,776192,776704,571648,636928,637184,534528,572416,604416,604160,0,2147483648u,107,1065353216,0,0,0,0,0,0,0,0,0,0,0,
114432,768,2147483648u,661248,661248,26769408,65536,26900480,6,0,2908160,3014656,12,256,14671872,2147483648u,2147483648u,14843904,22740992,28770304,7225344,7077888,983296,70144,778240,778752,572672,637952,534400,544768,571392,605440,604160,0,2147483648u,104,0,0,0,0,0,0,0,0,0,0,0,0,
114432,768,2147483648u,661440,661440,27162624,65536,27293696,6,0,2909184,3047424,12,256,14673920,2147483648u,2147483648u,14934016,22831104,28786688,7225856,7143424,999680,72192,780288,780800,572672,637952,637696,532480,571392,605440,604160,0,2147483648u,105,0,0,0,0,0,0,0,0,0,0,0,0,
114432,768,2147483648u,491776,508160,22835200,2048,22843392,4,0,2910208,3080192,12,256,15032320,2147483648u,2147483648u,15024128,22921216,28803072,7290880,7208960,1016064,74240,782336,782848,572672,637952,534400,546816,572416,605440,604160,0,2147483648u,106,1065353216,1065353216,0,0,0,0,0,0,0,0,0,0,
114432,768,2147483648u,1376512,1392896,23080960,2048,23097344,4,2147483648u,2911232,3112960,12,256,15034368,2147483648u,2147483648u,15114240,23011328,28819456,7291392,7274496,1032448,76288,784384,784896,572672,637952,637696,534528,572416,605440,604160,0,2147483648u,107,1065353216,0,0,0,0,0,0,0,0,0,0,0,
77568,256,2147483648u,969472,969472,26783744,65536,26914816,6,0,3940352,3932160,12,256,17547264,0,0,17367040,22740992,25952256,8142848,8126464,1442048,70144,835584,836096,579840,645120,535296,528384,579584,612608,612352,0,2147483648u,104,0,0,0,0,0,0,0,0,0,0,0,0,
77568,256,2147483648u,969664,969664,27176960,65536,27308032,6,0,3941376,3964928,12,256,17549312,0,0,17457152,22831104,25968640,8143360,8192000,1458432,72192,837632,838144,579840,645120,645376,532480,579584,612608,612352,0,2147483648u,105,0,0,0,0,0,0,0,0,0,0,0,0,
77568,256,2147483648u,721152,737536,23072768,2048,23089152,4,0,3942400,3997696,12,256,17907712,0,0,17547264,22921216,25985024,8208384,8257536,1474816,74240,839680,840192,579840,645120,535296,530432,580608,612608,612352,0,2147483648u,106,1065353216,1065353216,0,0,0,0,0,0,0,0,0,0,
77568,256,2147483648u,1376512,1392896,23080960,2048,23097344,4,2147483648u,3943424,4030464,12,256,17909760,0,0,17637376,23011328,26001408,8208896,8323072,1491200,76288,841728,842240,579840,645120,645376,534528,580608,612608,612352,0,2147483648u,107,1065353216,0,0,0,0,0,0,0,0,0,0,0,
89856,768,2147483648u,1013504,1013504,26785792,65536,26916864,6,0,3956736,4063232,12,256,17555456,2147483648u,2147483648u,17727488,22740992,28770304,8273920,8126464,1507584,70144,843776,844288,580864,646144,535424,544768,579584,613632,612352,0,2147483648u,104,0,0,0,0,0,0,0,0,0,0,0,0,
89856,768,2147483648u,1013696,1013696,27179008,65536,27310080,6,0,3957760,4096000,12,256,17557504,2147483648u,2147483648u,17817600,22831104,28786688,8274432,8192000,1523968,72192,845824,846336,580864,646144,645888,532480,579584,613632,612352,0,2147483648u,105,0,0,0,0,0,0,0,0,0,0,0,0,
89856,768,2147483648u,753920,770304,22757376,2048,22773760,4,0,3958784,4128768,12,256,17915904,2147483648u,2147483648u,17907712,22921216,28803072,8339456,8257536,1540352,74240,847872,848384,580864,646144,535424,546816,580608,613632,612352,0,2147483648u,106,1065353216,1065353216,0,0,0,0,0,0,0,0,0,0,
89856,768,2147483648u,1376512,1392896,23080960,2048,23097344,4,2147483648u,3959808,4161536,12,256,17917952,2147483648u,2147483648u,17997824,23011328,28819456,8339968,8323072,1556736,76288,849920,850432,580864,646144,645888,534528,580608,613632,612352,0,2147483648u,107,1065353216,0,0,0,0,0,0,0,0,0,0,0,
102144,256,2147483648u,1321728,1321728,26800128,65536,26931200,6,0,4988928,4980736,12,256,20430848,0,0,20250624,22740992,25952256,9191424,9175040,1966336,70144,901120,901632,588032,653312,536320,528384,587776,620800,620544,0,2147483648u,104,0,0,0,0,0,0,0,0,0,0,0,0,
102144,256,2147483648u,1321920,1321920,27193344,65536,27324416,6,0,4989952,5013504,12,256,20432896,0,0,20340736,22831104,25968640,9191936,9240576,1982720,72192,903168,903680,588032,653312,653568,532480,587776,620800,620544,0,2147483648u,105,0,0,0,0,0,0,0,0,0,0,0,0,
102144,256,2147483648u,983296,999680,23003136,2048,23027712,4,0,4990976,5046272,12,256,20791296,0,0,20430848,22921216,25985024,9256960,9306112,1999104,74240,905216,905728,588032,653312,536320,530432,588800,620800,620544,0,2147483648u,106,1065353216,1065353216,0,0,0,0,0,0,0,0,0,0,
102144,256,2147483648u,1376512,1392896,23080960,2048,23097344,4,2147483648u,4992000,5079040,12,256,20793344,0,0,20520960,23011328,26001408,9257472,9371648,2015488,76288,907264,907776,588032,653312,653568,534528,588800,620800,620544,0,2147483648u,107,1065353216,0,0,0,0,0,0,0,0,0,0,0,
114432,768,2147483648u,1365760,1365760,26802176,65536,26933248,6,0,5005312,5111808,12,256,20439040,2147483648u,2147483648u,20611072,22740992,28770304,9322496,9175040,2031872,70144,909312,909824,589056,654336,536448,544768,587776,621824,620544,0,2147483648u,104,0,0,0,0,0,0,0,0,0,0,0,0,
114432,768,2147483648u,1365952,1365952,27195392,65536,27326464,6,0,5006336,5144576,12,256,20441088,2147483648u,2147483648u,20701184,22831104,28786688,9323008,9240576,2048256,72192,911360,911872,589056,654336,654080,532480,587776,621824,620544,0,2147483648u,105,0,0,0,0,0,0,0,0,0,0,0,0,
114432,768,2147483648u,1016064,1032448,23044096,2048,23060480,4,0,5007360,5177344,12,256,20799488,2147483648u,2147483648u,20791296,22921216,28803072,9388032,9306112,2064640,74240,913408,913920,589056,654336,536448,546816,588800,621824,620544,0,2147483648u,106,1065353216,1065353216,0,0,0,0,0,0,0,0,0,0,
114432,768,2147483648u,1376512,1392896,23080960,2048,23097344,4,2147483648u,5008384,5210112,12,256,20801536,2147483648u,2147483648u,20881408,23011328,28819456,9388544,9371648,2081024,76288,915456,915968,589056,654336,654080,534528,588800,621824,620544,0,2147483648u,107,1065353216,0,0,0,0,0,0,0,0,0,0,0,
77568,384,2147483648u,265344,265344,27537408,65536,27668480,5,0,1847296,1835008,12,384,11870208,0,0,11599872,23101440,26345472,6053888,6029312,393600,70400,704512,705280,563584,628736,537344,528384,563200,596352,595968,0,2147483648u,108,0,0,0,0,0,0,0,0,0,0,0,0,
77568,384,2147483648u,265504,265504,27865088,65536,27996160,5,0,1848320,1867776,12,384,11872256,0,0,11689984,23191552,26361856,6054400,6094848,409984,72448,706560,707328,563584,628736,629120,536576,563200,596352,595968,0,2147483648u,109,0,0,0,0,0,0,0,0,0,0,0,0,
77568,384,2147483648u,196992,213376,23281664,2048,23289856,4,0,1849344,1900544,12,384,12230656,0,0,11780096,23281664,26378240,6119424,6160384,426368,74496,708608,709376,563584,628736,537344,530432,564224,596352,595968,0,2147483648u,110,1065353216,1065353216,0,0,0,0,0,0,0,0,0,0,
77568,384,2147483648u,1245568,1261952,23302144,2048,23318528,4,0,1850368,1933312,12,384,12232704,0,0,11870208,23371776,26394624,6119936,6225920,442752,76544,710656,711424,563584,628736,629120,538624,564224,596352,595968,0,2147483648u,111,1065353216,0,0,0,0,0,0,0,0,0,0,0,
89856,896,2147483648u,309376,309376,27539456,65536,27670528,5,0,1863680,1966080,12,384,11878400,2147483648u,2147483648u,11960320,23101440,29097984,6184960,6029312,459136,70400,712704,713472,564608,629760,537472,544768,563200,597376,595968,0,2147483648u,108,0,0,0,0,0,0,0,0,0,0,0,0,
89856,896,2147483648u,309536,309536,27867136,65536,27998208,5,0,1864704,1998848,12,384,11880448,2147483648u,2147483648u,12050432,23191552,29114368,6185472,6094848,475520,72448,714752,715520,564608,629760,629632,536576,563200,597376,595968,0,2147483648u,109,0,0,0,0,0,0,0,0,0,0,0,0,
89856,896,2147483648u,229760,246144,23306240,2048,23322624,4,0,1865728,2031616,12,384,12238848,2147483648u,2147483648u,12140544,23281664,29130752,6250496,6160384,491904,74496,716800,717568,564608,629760,537472,546816,564224,597376,595968,0,2147483648u,110,1065353216,1065353216,0,0,0,0,0,0,0,0,0,0,
89856,896,2147483648u,1278336,1294720,23334912,2048,23351296,4,0,1866752,2064384,12,384,12240896,2147483648u,2147483648u,12230656,23371776,29147136,6251008,6225920,508288,76544,718848,719616,564608,629760,629632,538624,564224,597376,595968,0,2147483648u,111,1065353216,0,0,0,0,0,0,0,0,0,0,0,
102144,384,2147483648u,617600,617600,27553792,65536,27684864,5,0,2895872,2883584,12,384,14753792,0,0,14483456,23101440,26345472,7102464,7077888,917888,70400,770048,770816,571776,636928,538368,528384,571392,604544,604160,0,2147483648u,108,0,0,0,0,0,0,0,0,0,0,0,0,
102144,384,2147483648u,617760,617760,27881472,65536,28012544,5,0,2896896,2916352,12,384,14755840,0,0,14573568,23191552,26361856,7102976,7143424,934272,72448,772096,772864,571776,636928,637312,536576,571392,604544,604160,0,2147483648u,109,0,0,0,0,0,0,0,0,0,0,0,0,
102144,384,2147483648u,459136,475520,23162880,2048,23179264,4,0,2897920,2949120,12,384,15114240,0,0,14663680,23281664,26378240,7168000,7208960,950656,74496,774144,774912,571776,636928,538368,530432,572416,604544,604160,0,2147483648u,110,1065353216,1065353216,0,0,0,0,0,0,0,0,0,0,
102144,384,2147483648u,1376640,1393024,23441408,2048,23457792,4,2147483648u,2898944,2981888,12,384,15116288,0,0,14753792,23371776,26394624,7168512,7274496,967040,76544,776192,776960,571776,636928,637312,538624,572416,604544,604160,0,2147483648u,111,1065353216,0,0,0,0,0,0,0,0,0,0,0,
114432,896,2147483648u,661632,661632,27555840,65536,27686912,5,0,2912256,3014656,12,384,14761984,2147483648u,2147483648u,14843904,23101440,29097984,7233536,7077888,983424,70400,778240,779008,572800,637952,538496,544768,571392,605568,604160,0,2147483648u,108,0,0,0,0,0,0,0,0,0,0,0,0,
114432,896,2147483648u,661792,661792,27883520,65536,28014592,5,0,2913280,3047424,12,384,14764032,2147483648u,2147483648u,14934016,23191552,29114368,7234048,7143424,999808,72448,780288,781056,572800,637952,637824,536576,571392,605568,604160,0,2147483648u,109,0,0,0,0,0,0,0,0,0,0,0,0,
114432,896,2147483648u,491904,508288,23195648,2048,23203840,4,0,2914304,3080192,12,384,15122432,2147483648u,2147483648u,15024128,23281664,29130752,7299072,7208960,1016192,74496,782336,783104,572800,637952,538496,546816,572416,605568,604160,0,2147483648u,110,1065353216,1065353216,0,0,0,0,0,0,0,0,0,0,
114432,896,2147483648u,1376640,1393024,23441408,2048,23457792,4,2147483648u,2915328,3112960,12,384,15124480,2147483648u,2147483648u,15114240,23371776,29147136,7299584,7274496,1032576,76544,784384,785152,572800,637952,637824,538624,572416,605568,604160,0,2147483648u,111,1065353216,0,0,0,0,0,0,0,0,0,0,0,
77568,384,2147483648u,969856,969856,27570176,65536,27701248,5,0,3944448,3932160,12,384,17637376,0,0,17367040,23101440,26345472,8151040,8126464,1442176,70400,835584,836352,579968,645120,539392,528384,579584,612736,612352,0,2147483648u,108,0,0,0,0,0,0,0,0,0,0,0,0,
77568,384,2147483648u,970016,970016,27897856,65536,28028928,5,0,3945472,3964928,12,384,17639424,0,0,17457152,23191552,26361856,8151552,8192000,1458560,72448,837632,838400,579968,645120,645504,536576,579584,612736,612352,0,2147483648u,109,0,0,0,0,0,0,0,0,0,0,0,0,
77568,384,2147483648u,721280,737664,23433216,2048,23449600,4,0,3946496,3997696,12,384,17997824,0,0,17547264,23281664,26378240,8216576,8257536,1474944,74496,839680,840448,579968,645120,539392,530432,580608,612736,612352,0,2147483648u,110,1065353216,1065353216,0,0,0,0,0,0,0,0,0,0,
77568,384,2147483648u,1376640,1393024,23441408,2048,23457792,4,2147483648u,3947520,4030464,12,384,17999872,0,0,17637376,23371776,26394624,8217088,8323072,1491328,76544,841728,842496,579968,645120,645504,538624,580608,612736,612352,0,2147483648u,111,1065353216,0,0,0,0,0,0,0,0,0,0,0,
89856,896,2147483648u,1013888,1013888,27572224,65536,27703296,5,0,3960832,4063232,12,384,17645568,2147483648u,2147483648u,17727488,23101440,29097984,8282112,8126464,1507712,70400,843776,844544,580992,646144,539520,544768,579584,613760,612352,0,2147483648u,108,0,0,0,0,0,0,0,0,0,0,0,0,
89856,896,2147483648u,1014048,1014048,27899904,65536,28030976,5,0,3961856,4096000,12,384,17647616,2147483648u,2147483648u,17817600,23191552,29114368,8282624,8192000,1524096,72448,845824,846592,580992,646144,646016,536576,579584,613760,612352,0,2147483648u,109,0,0,0,0,0,0,0,0,0,0,0,0,
89856,896,2147483648u,754048,770432,23117824,2048,23134208,4,0,3962880,4128768,12,384,18006016,2147483648u,2147483648u,17907712,23281664,29130752,8347648,8257536,1540480,74496,847872,848640,580992,646144,539520,546816,580608,613760,612352,0,2147483648u,110,1065353216,1065353216,0,0,0,0,0,0,0,0,0,0,
89856,896,2147483648u,1376640,1393024,23441408,2048,23457792,4,2147483648u,3963904,4161536,12,384,18008064,2147483648u,2147483648u,17997824,23371776,29147136,8348160,8323072,1556864,76544,849920,850688,580992,646144,646016,538624,580608,613760,612352,0,2147483648u,111,1065353216,0,0,0,0,0,0,0,0,0,0,0,
102144,384,2147483648u,1322112,1322112,27586560,65536,27717632,5,0,4993024,4980736,12,384,20520960,0,0,20250624,23101440,26345472,9199616,9175040,1966464,70400,901120,901888,588160,653312,540416,528384,587776,620928,620544,0,2147483648u,108,0,0,0,0,0,0,0,0,0,0,0,0,
102144,384,2147483648u,1322272,1322272,27914240,65536,28045312,5,0,4994048,5013504,12,384,20523008,0,0,20340736,23191552,26361856,9200128,9240576,1982848,72448,903168,903936,588160,653312,653696,536576,587776,620928,620544,0,2147483648u,109,0,0,0,0,0,0,0,0,0,0,0,0,
102144,384,2147483648u,983424,999808,23363584,2048,23388160,4,0,4995072,5046272,12,384,20881408,0,0,20430848,23281664,26378240,9265152,9306112,1999232,74496,905216,905984,588160,653312,540416,530432,588800,620928,620544,0,2147483648u,110,1065353216,1065353216,0,0,0,0,0,0,0,0,0,0,
102144,384,2147483648u,1376640,1393024,23441408,2048,23457792,4,2147483648u,4996096,5079040,12,384,20883456,0,0,20520960,23371776,26394624,9265664,9371648,2015616,76544,907264,908032,588160,653312,653696,538624,588800,620928,620544,0,2147483648u,111,1065353216,0,0,0,0,0,0,0,0,0,0,0,
114432,896,2147483648u,1366144,1366144,27588608,65536,27719680,5,0,5009408,5111808,12,384,20529152,2147483648u,2147483648u,20611072,23101440,29097984,9330688,9175040,2032000,70400,909312,910080,589184,654336,540544,544768,587776,621952,620544,0,2147483648u,108,0,0,0,0,0,0,0,0,0,0,0,0,
114432,896,2147483648u,1366304,1366304,27916288,65536,28047360,5,0,5010432,5144576,12,384,20531200,2147483648u,2147483648u,20701184,23191552,29114368,9331200,9240576,2048384,72448,911360,912128,589184,654336,654208,536576,587776,621952,620544,0,2147483648u,109,0,0,0,0,0,0,0,0,0,0,0,0,
114432,896,2147483648u,1016192,1032576,23404544,2048,23420928,4,0,5011456,5177344,12,384,20889600,2147483648u,2147483648u,20791296,23281664,29130752,9396224,9306112,2064768,74496,913408,914176,589184,654336,540544,546816,588800,621952,620544,0,2147483648u,110,1065353216,1065353216,0,0,0,0,0,0,0,0,0,0,
114432,896,2147483648u,1376640,1393024,23441408,2048,23457792,4,2147483648u,5012480,5210112,12,384,20891648,2147483648u,2147483648u,20881408,23371776,29147136,9396736,9371648,2081152,76544,915456,916224,589184,654336,654208,538624,588800,621952,620544,0,2147483648u,111,1065353216,0,0,0,0,0,0,0,0,0,0,0,
77568,0,2147483648u,265664,265664,28192768,65536,28323840,5,0,1966080,1835008,12,512,11603968,0,0,11599872,23461888,26738688,6062080,6029312,393728,70656,704512,705536,563712,628736,541440,532480,563200,596480,595968,0,2147483648u,112,0,0,0,0,0,0,0,0,0,0,0,0,
77568,0,2147483648u,265824,265824,28520448,65536,28651520,5,0,1967104,1867776,12,512,11606016,0,0,11689984,23552000,26755072,6062592,6094848,410112,72704,706560,707584,563712,628736,629760,540672,563200,596480,595968,0,2147483648u,113,0,0,0,0,0,0,0,0,0,0,0,0,
77568,0,2147483648u,197120,213504,23642112,2048,23650304,4,0,1968128,1900544,12,512,11964416,0,0,11780096,23642112,26771456,6127616,6160384,426496,74752,708608,709632,563712,628736,541440,534528,564224,596480,595968,0,2147483648u,114,1065353216,1065353216,0,0,0,0,0,0,0,0,0,0,
77568,0,2147483648u,1245696,1262080,23662592,2048,23678976,4,0,1969152,1933312,12,512,11966464,0,0,11870208,23732224,26787840,6128128,6225920,442880,76800,710656,711680,563712,628736,629760,542720,564224,596480,595968,0,2147483648u,115,1065353216,0,0,0,0,0,0,0,0,0,0,0,
89856,512,2147483648u,309696,309696,28194816,65536,28325888,5,0,1982464,1966080,12,512,11612160,2147483648u,2147483648u,11960320,23461888,29425664,6193152,6029312,459264,70656,712704,713728,564736,629760,541568,548864,563200,597504,595968,0,2147483648u,112,0,0,0,0,0,0,0,0,0,0,0,0,
89856,512,2147483648u,309856,309856,28522496,65536,28653568,5,0,1983488,1998848,12,512,11614208,2147483648u,2147483648u,12050432,23552000,29442048,6193664,6094848,475648,72704,714752,715776,564736,629760,630272,540672,563200,597504,595968,0,2147483648u,113,0,0,0,0,0,0,0,0,0,0,0,0,
89856,512,2147483648u,229888,246272,23666688,2048,23683072,4,0,1984512,2031616,12,512,11972608,2147483648u,2147483648u,12140544,23642112,29458432,6258688,6160384,492032,74752,716800,717824,564736,629760,541568,550912,564224,597504,595968,0,2147483648u,114,1065353216,1065353216,0,0,0,0,0,0,0,0,0,0,
89856,512,2147483648u,1278464,1294848,23695360,2048,23711744,4,0,1985536,2064384,12,512,11974656,2147483648u,2147483648u,12230656,23732224,29474816,6259200,6225920,508416,76800,718848,719872,564736,629760,630272,542720,564224,597504,595968,0,2147483648u,115,1065353216,0,0,0,0,0,0,0,0,0,0,0,
102144,0,2147483648u,617920,617920,28209152,65536,28340224,5,0,3014656,2883584,12,512,14487552,0,0,14483456,23461888,26738688,7110656,7077888,918016,70656,770048,771072,571904,636928,542464,532480,571392,604672,604160,0,2147483648u,112,0,0,0,0,0,0,0,0,0,0,0,0,
102144,0,2147483648u,618080,618080,28536832,65536,28667904,5,0,3015680,2916352,12,512,14489600,0,0,14573568,23552000,26755072,7111168,7143424,934400,72704,772096,773120,571904,636928,637952,540672,571392,604672,604160,0,2147483648u,113,0,0,0,0,0,0,0,0,0,0,0,0,
102144,0,2147483648u,459264,475648,23523328,2048,23539712,4,0,3016704,2949120,12,512,14848000,0,0,14663680,23642112,26771456,7176192,7208960,950784,74752,774144,775168,571904,636928,542464,534528,572416,604672,604160,0,2147483648u,114,1065353216,1065353216,0,0,0,0,0,0,0,0,0,0,
102144,0,2147483648u,1376768,1393152,23801856,2048,23818240,4,2147483648u,3017728,2981888,12,512,14850048,0,0,14753792,23732224,26787840,7176704,7274496,967168,76800,776192,777216,571904,636928,637952,542720,572416,604672,604160,0,2147483648u,115,1065353216,0,0,0,0,0,0,0,0,0,0,0,
114432,512,2147483648u,661952,661952,28211200,65536,28342272,5,0,3031040,3014656,12,512,14495744,2147483648u,2147483648u,14843904,23461888,29425664,7241728,7077888,983552,70656,778240,779264,572928,637952,542592,548864,571392,605696,604160,0,2147483648u,112,0,0,0,0,0,0,0,0,0,0,0,0,
114432,512,2147483648u,662112,662112,28538880,65536,28669952,5,0,3032064,3047424,12,512,14497792,2147483648u,2147483648u,14934016,23552000,29442048,7242240,7143424,999936,72704,780288,781312,572928,637952,638464,540672,571392,605696,604160,0,2147483648u,113,0,0,0,0,0,0,0,0,0,0,0,0,
114432,512,2147483648u,492032,508416,23556096,2048,23564288,4,0,3033088,3080192,12,512,14856192,2147483648u,2147483648u,15024128,23642112,29458432,7307264,7208960,1016320,74752,782336,783360,572928,637952,542592,550912,572416,605696,604160,0,2147483648u,114,1065353216,1065353216,0,0,0,0,0,0,0,0,0,0,
114432,512,2147483648u,1376768,1393152,23801856,2048,23818240,4,2147483648u,3034112,3112960,12,512,14858240,2147483648u,2147483648u,15114240,23732224,29474816,7307776,7274496,1032704,76800,784384,785408,572928,637952,638464,542720,572416,605696,604160,0,2147483648u,115,1065353216,0,0,0,0,0,0,0,0,0,0,0,
77568,0,2147483648u,970176,970176,28225536,65536,28356608,5,0,4063232,3932160,12,512,17371136,0,0,17367040,23461888,26738688,8159232,8126464,1442304,70656,835584,836608,580096,645120,543488,532480,579584,612864,612352,0,2147483648u,112,0,0,0,0,0,0,0,0,0,0,0,0,
77568,0,2147483648u,970336,970336,28553216,65536,28684288,5,0,4064256,3964928,12,512,17373184,0,0,17457152,23552000,26755072,8159744,8192000,1458688,72704,837632,838656,580096,645120,646144,540672,579584,612864,612352,0,2147483648u,113,0,0,0,0,0,0,0,0,0,0,0,0,
77568,0,2147483648u,721408,737792,23793664,2048,23810048,4,0,4065280,3997696,12,512,17731584,0,0,17547264,23642112,26771456,8224768,8257536,1475072,74752,839680,840704,580096,645120,543488,534528,580608,612864,612352,0,2147483648u,114,1065353216,1065353216,0,0,0,0,0,0,0,0,0,0,
77568,0,2147483648u,1376768,1393152,23801856,2048,23818240,4,2147483648u,4066304,4030464,12,512,17733632,0,0,17637376,23732224,26787840,8225280,8323072,1491456,76800,841728,842752,580096,645120,646144,542720,580608,612864,612352,0,2147483648u,115,1065353216,0,0,0,0,0,0,0,0,0,0,0,
89856,512,2147483648u,1014208,1014208,28227584,65536,28358656,5,0,4079616,4063232,12,512,17379328,2147483648u,2147483648u,17727488,23461888,29425664,8290304,8126464,1507840,70656,843776,844800,581120,646144,543616,548864,579584,613888,612352,0,2147483648u,112,0,0,0,0,0,0,0,0,0,0,0,0,
89856,512,2147483648u,1014368,1014368,28555264,65536,28686336,5,0,4080640,4096000,12,512,17381376,2147483648u,2147483648u,17817600,23552000,29442048,8290816,8192000,1524224,72704,845824,846848,581120,646144,646656,540672,579584,613888,612352,0,2147483648u,113,0,0,0,0,0,0,0,0,0,0,0,0,
89856,512,2147483648u,754176,770560,23478272,2048,23494656,4,0,4081664,4128768,12,512,17739776,2147483648u,2147483648u,17907712,23642112,29458432,8355840,8257536,1540608,74752,847872,848896,581120,646144,543616,550912,580608,613888,612352,0,2147483648u,114,1065353216,1065353216,0,0,0,0,0,0,0,0,0,0,
89856,512,2147483648u,1376768,1393152,23801856,2048,23818240,4,2147483648u,4082688,4161536,12,512,17741824,2147483648u,2147483648u,17997824,23732224,29474816,8356352,8323072,1556992,76800,849920,850944,581120,646144,646656,542720,580608,613888,612352,0,2147483648u,115,1065353216,0,0,0,0,0,0,0,0,0,0,0,
102144,0,2147483648u,1322432,1322432,28241920,65536,28372992,5,0,5111808,4980736,12,512,20254720,0,0,20250624,23461888,26738688,9207808,9175040,1966592,70656,901120,902144,588288,653312,544512,532480,587776,621056,620544,0,2147483648u,112,0,0,0,0,0,0,0,0,0,0,0,0,
102144,0,2147483648u,1322592,1322592,28569600,65536,28700672,5,0,5112832,5013504,12,512,20256768,0,0,20340736,23552000,26755072,9208320,9240576,1982976,72704,903168,904192,588288,653312,654336,540672,587776,621056,620544,0,2147483648u,113,0,0,0,0,0,0,0,0,0,0,0,0,
102144,0,2147483648u,983552,999936,23724032,2048,23748608,4,0,5113856,5046272,12,512,20615168,0,0,20430848,23642112,26771456,9273344,9306112,1999360,74752,905216,906240,588288,653312,544512,534528,588800,621056,620544,0,2147483648u,114,1065353216,1065353216,0,0,0,0,0,0,0,0,0,0,
102144,0,2147483648u,1376768,1393152,23801856,2048,23818240,4,2147483648u,5114880,5079040,12,512,20617216,0,0,20520960,23732224,26787840,9273856,9371648,2015744,76800,907264,908288,588288,653312,654336,542720,588800,621056,620544,0,2147483648u,115,1065353216,0,0,0,0,0,0,0,0,0,0,0,
114432,512,2147483648u,1366464,1366464,28243968,65536,28375040,5,0,5128192,5111808,12,512,20262912,2147483648u,2147483648u,20611072,23461888,29425664,9338880,9175040,2032128,70656,909312,910336,589312,654336,544640,548864,587776,622080,620544,0,2147483648u,112,0,0,0,0,0,0,0,0,0,0,0,0,
114432,512,2147483648u,1366624,1366624,28571648,65536,28702720,5,0,5129216,5144576,12,512,20264960,2147483648u,2147483648u,20701184,23552000,29442048,9339392,9240576,2048512,72704,911360,912384,589312,654336,654848,540672,587776,622080,620544,0,2147483648u,113,0,0,0,0,0,0,0,0,0,0,0,0,
114432,512,2147483648u,1016320,1032704,23764992,2048,23781376,4,0,5130240,5177344,12,512,20623360,2147483648u,2147483648u,20791296,23642112,29458432,9404416,9306112,2064896,74752,913408,914432,589312,654336,544640,550912,588800,622080,620544,0,2147483648u,114,1065353216,1065353216,0,0,0,0,0,0,0,0,0,0,
114432,512,2147483648u,1376768,1393152,23801856,2048,23818240,4,2147483648u,5131264,5210112,12,512,20625408,2147483648u,2147483648u,20881408,23732224,29474816,9404928,9371648,2081280,76800,915456,916480,589312,654336,654848,542720,588800,622080,620544,0,2147483648u,115,1065353216,0,0,0,0,0,0,0,0,0,0,0,
77568,128,2147483648u,265984,265984,28848128,65536,28979200,5,0,1970176,1835008,12,640,11694080,0,0,11599872,23822336,27131904,6070272,6029312,393856,70912,704512,705792,563840,628736,545536,532480,563200,596608,595968,0,2147483648u,116,0,0,0,0,0,0,0,0,0,0,0,0,
77568,128,2147483648u,266144,266144,29175808,65536,29306880,5,0,1971200,1867776,12,640,11696128,0,0,11689984,23912448,27148288,6070784,6094848,410240,72960,706560,707840,563840,628736,629888,544768,563200,596608,595968,0,2147483648u,117,0,0,0,0,0,0,0,0,0,0,0,0,
77568,128,2147483648u,197248,213632,24002560,2048,24010752,4,0,1972224,1900544,12,640,12054528,0,0,11780096,24002560,27164672,6135808,6160384,426624,75008,708608,709888,563840,628736,545536,534528,564224,596608,595968,0,2147483648u,118,1065353216,1065353216,0,0,0,0,0,0,0,0,0,0,
77568,128,2147483648u,1245824,1262208,24023040,2048,24039424,4,0,1973248,1933312,12,640,12056576,0,0,11870208,24092672,27181056,6136320,6225920,443008,77056,710656,711936,563840,628736,629888,546816,564224,596608,595968,0,2147483648u,119,1065353216,0,0,0,0,0,0,0,0,0,0,0,
89856,640,2147483648u,310016,310016,28850176,65536,28981248,5,0,1986560,1966080,12,640,11702272,2147483648u,2147483648u,11960320,23822336,29753344,6201344,6029312,459392,70912,712704,713984,564864,629760,545664,548864,563200,597632,595968,0,2147483648u,116,0,0,0,0,0,0,0,0,0,0,0,0,
89856,640,2147483648u,310176,310176,29177856,65536,29308928,5,0,1987584,1998848,12,640,11704320,2147483648u,2147483648u,12050432,23912448,29769728,6201856,6094848,475776,72960,714752,716032,564864,629760,630400,544768,563200,597632,595968,0,2147483648u,117,0,0,0,0,0,0,0,0,0,0,0,0,
89856,640,2147483648u,230016,246400,24027136,2048,24043520,4,0,1988608,2031616,12,640,12062720,2147483648u,2147483648u,12140544,24002560,29786112,6266880,6160384,492160,75008,716800,718080,564864,629760,545664,550912,564224,597632,595968,0,2147483648u,118,1065353216,1065353216,0,0,0,0,0,0,0,0,0,0,
89856,640,2147483648u,1278592,1294976,24055808,2048,24072192,4,0,1989632,2064384,12,640,12064768,2147483648u,2147483648u,12230656,24092672,29802496,6267392,6225920,508544,77056,718848,720128,564864,629760,630400,546816,564224,597632,595968,0,2147483648u,119,1065353216,0,0,0,0,0,0,0,0,0,0,0,
102144,128,2147483648u,618240,618240,28864512,65536,28995584,5,0,3018752,2883584,12,640,14577664,0,0,14483456,23822336,27131904,7118848,7077888,918144,70912,770048,771328,572032,636928,546560,532480,571392,604800,604160,0,2147483648u,116,0,0,0,0,0,0,0,0,0,0,0,0,
102144,128,2147483648u,618400,618400,29192192,65536,29323264,5,0,3019776,2916352,12,640,14579712,0,0,14573568,23912448,27148288,7119360,7143424,934528,72960,772096,773376,572032,636928,638080,544768,571392,604800,604160,0,2147483648u,117,0,0,0,0,0,0,0,0,0,0,0,0,
102144,128,2147483648u,459392,475776,23883776,2048,23900160,4,0,3020800,2949120,12,640,14938112,0,0,14663680,24002560,27164672,7184384,7208960,950912,75008,774144,775424,572032,636928,546560,534528,572416,604800,604160,0,2147483648u,118,1065353216,1065353216,0,0,0,0,0,0,0,0,0,0,
102144,128,2147483648u,1376896,1393280,24162304,2048,24178688,4,2147483648u,3021824,2981888,12,640,14940160,0,0,14753792,24092672,27181056,7184896,7274496,967296,77056,776192,777472,572032,636928,638080,546816,572416,604800,604160,0,2147483648u,119,1065353216,0,0,0,0,0,0,0,0,0,0,0,
114432,640,2147483648u,662272,662272,28866560,65536,28997632,5,0,3035136,3014656,12,640,14585856,2147483648u,2147483648u,14843904,23822336,29753344,7249920,7077888,983680,70912,778240,779520,573056,637952,546688,548864,571392,605824,604160,0,2147483648u,116,0,0,0,0,0,0,0,0,0,0,0,0,
114432,640,2147483648u,662432,662432,29194240,65536,29325312,5,0,3036160,3047424,12,640,14587904,2147483648u,2147483648u,14934016,23912448,29769728,7250432,7143424,1000064,72960,780288,781568,573056,637952,638592,544768,571392,605824,604160,0,2147483648u,117,0,0,0,0,0,0,0,0,0,0,0,0,
114432,640,2147483648u,492160,508544,23916544,2048,23924736,4,0,3037184,3080192,12,640,14946304,2147483648u,2147483648u,15024128,24002560,29786112,7315456,7208960,1016448,75008,782336,783616,573056,637952,546688,550912,572416,605824,604160,0,2147483648u,118,1065353216,1065353216,0,0,0,0,0,0,0,0,0,0,
114432,640,2147483648u,1376896,1393280,24162304,2048,24178688,4,2147483648u,3038208,3112960,12,640,14948352,2147483648u,2147483648u,15114240,24092672,29802496,7315968,7274496,1032832,77056,784384,785664,573056,637952,638592,546816,572416,605824,604160,0,2147483648u,119,1065353216,0,0,0,0,0,0,0,0,0,0,0,
77568,128,2147483648u,970496,970496,28880896,65536,29011968,5,0,4067328,3932160,12,640,17461248,0,0,17367040,23822336,27131904,8167424,8126464,1442432,70912,835584,836864,580224,645120,547584,532480,579584,612992,612352,0,2147483648u,116,0,0,0,0,0,0,0,0,0,0,0,0,
77568,128,2147483648u,970656,970656,29208576,65536,29339648,5,0,4068352,3964928,12,640,17463296,0,0,17457152,23912448,27148288,8167936,8192000,1458816,72960,837632,838912,580224,645120,646272,544768,579584,612992,612352,0,2147483648u,117,0,0,0,0,0,0,0,0,0,0,0,0,
77568,128,2147483648u,721536,737920,24154112,2048,24170496,4,0,4069376,3997696,12,640,17821696,0,0,17547264,24002560,27164672,8232960,8257536,1475200,75008,839680,840960,580224,645120,547584,534528,580608,612992,612352,0,2147483648u,118,1065353216,1065353216,0,0,0,0,0,0,0,0,0,0,
77568,128,2147483648u,1376896,1393280,24162304,2048,24178688,4,2147483648u,4070400,4030464,12,640,17823744,0,0,17637376,24092672,27181056,8233472,8323072,1491584,77056,841728,843008,580224,645120,646272,546816,580608,612992,612352,0,2147483648u,119,1065353216,0,0,0,0,0,0,0,0,0,0,0,
89856,640,2147483648u,1014528,1014528,28882944,65536,29014016,5,0,4083712,4063232,12,640,17469440,2147483648u,2147483648u,17727488,23822336,29753344,8298496,8126464,1507968,70912,843776,845056,581248,646144,547712,548864,579584,614016,612352,0,2147483648u,116,0,0,0,0,0,0,0,0,0,0,0,0,
89856,640,2147483648u,1014688,1014688,29210624,65536,29341696,5,0,4084736,4096000,12,640,17471488,2147483648u,2147483648u,17817600,23912448,29769728,8299008,8192000,1524352,72960,845824,847104,581248,646144,646784,544768,579584,614016,612352,0,2147483648u,117,0,0,0,0,0,0,0,0,0,0,0,0,
89856,640,2147483648u,754304,770688,23838720,2048,23855104,4,0,4085760,4128768,12,640,17829888,2147483648u,2147483648u,17907712,24002560,29786112,8364032,8257536,1540736,75008,847872,849152,581248,646144,547712,550912,580608,614016,612352,0,2147483648u,118,1065353216,1065353216,0,0,0,0,0,0,0,0,0,0,
89856,640,2147483648u,1376896,1393280,24162304,2048,24178688,4,2147483648u,4086784,4161536,12,640,17831936,2147483648u,2147483648u,17997824,24092672,29802496,8364544,8323072,1557120,77056,849920,851200,581248,646144,646784,546816,580608,614016,612352,0,2147483648u,119,1065353216,0,0,0,0,0,0,0,0,0,0,0,
102144,128,2147483648u,1322752,1322752,28897280,65536,29028352,5,0,5115904,4980736,12,640,20344832,0,0,20250624,23822336,27131904,9216000,9175040,1966720,70912,901120,902400,588416,653312,548608,532480,587776,621184,620544,0,2147483648u,116,0,0,0,0,0,0,0,0,0,0,0,0,
102144,128,2147483648u,1322912,1322912,29224960,65536,29356032,5,0,5116928,5013504,12,640,20346880,0,0,20340736,23912448,27148288,9216512,9240576,1983104,72960,903168,904448,588416,653312,654464,544768,587776,621184,620544,0,2147483648u,117,0,0,0,0,0,0,0,0,0,0,0,0,
102144,128,2147483648u,983680,1000064,24084480,2048,24109056,4,0,5117952,5046272,12,640,20705280,0,0,20430848,24002560,27164672,9281536,9306112,1999488,75008,905216,906496,588416,653312,548608,534528,588800,621184,620544,0,2147483648u,118,1065353216,1065353216,0,0,0,0,0,0,0,0,0,0,
102144,128,2147483648u,1376896,1393280,24162304,2048,24178688,4,2147483648u,5118976,5079040,12,640,20707328,0,0,20520960,24092672,27181056,9282048,9371648,2015872,77056,907264,908544,588416,653312,654464,546816,588800,621184,620544,0,2147483648u,119,1065353216,0,0,0,0,0,0,0,0,0,0,0,
114432,640,2147483648u,1366784,1366784,28899328,65536,29030400,5,0,5132288,5111808,12,640,20353024,2147483648u,2147483648u,20611072,23822336,29753344,9347072,9175040,2032256,70912,909312,910592,589440,654336,548736,548864,587776,622208,620544,0,2147483648u,116,0,0,0,0,0,0,0,0,0,0,0,0,
114432,640,2147483648u,1366944,1366944,29227008,65536,29358080,5,0,5133312,5144576,12,640,20355072,2147483648u,2147483648u,20701184,23912448,29769728,9347584,9240576,2048640,72960,911360,912640,589440,654336,654976,544768,587776,622208,620544,0,2147483648u,117,0,0,0,0,0,0,0,0,0,0,0,0,
114432,640,2147483648u,1016448,1032832,24125440,2048,24141824,4,0,5134336,5177344,12,640,20713472,2147483648u,2147483648u,20791296,24002560,29786112,9412608,9306112,2065024,75008,913408,914688,589440,654336,548736,550912,588800,622208,620544,0,2147483648u,118,1065353216,1065353216,0,0,0,0,0,0,0,0,0,0,
114432,640,2147483648u,1376896,1393280,24162304,2048,24178688,4,2147483648u,5135360,5210112,12,640,20715520,2147483648u,2147483648u,20881408,24092672,29802496,9413120,9371648,2081408,77056,915456,916736,589440,654336,654976,546816,588800,622208,620544,0,2147483648u,119,1065353216,0,0,0,0,0,0,0,0,0,0,0,
77568,256,2147483648u,266304,266304,29503488,65536,29634560,5,0,1974272,1835008,12,768,11784192,2147483648u,2147483648u,11599872,24182784,27459584,6078464,6029312,393984,71168,704512,706048,563968,628736,549632,536576,563200,596736,595968,0,2147483648u,120,0,0,0,0,0,0,0,0,0,0,0,0,
77568,256,2147483648u,266464,266464,29831168,65536,29962240,5,0,1975296,1867776,12,768,11786240,2147483648u,2147483648u,11689984,24272896,27475968,6078976,6094848,410368,73216,706560,708096,563968,628736,630016,548864,563200,596736,595968,0,2147483648u,121,0,0,0,0,0,0,0,0,0,0,0,0,
77568,256,2147483648u,197376,213760,24363008,2048,24371200,4,0,1976320,1900544,12,768,12144640,2147483648u,2147483648u,11780096,24363008,27492352,6144000,6160384,426752,75264,708608,710144,563968,628736,549632,538624,564224,596736,595968,0,2147483648u,122,1065353216,1065353216,0,0,0,0,0,0,0,0,0,0,
77568,256,2147483648u,1245952,1262336,24383488,2048,24399872,4,0,1977344,1933312,12,768,12146688,2147483648u,2147483648u,11870208,24453120,27508736,6144512,6225920,443136,77312,710656,712192,563968,628736,630016,550912,564224,596736,595968,0,2147483648u,123,1065353216,0,0,0,0,0,0,0,0,0,0,0,
89856,768,2147483648u,310336,310336,29505536,65536,29636608,5,0,1990656,1966080,12,768,11792384,2147483648u,2147483648u,11960320,24182784,30081024,6209536,6029312,459520,71168,712704,714240,564992,629760,549760,552960,563200,597760,595968,0,2147483648u,120,0,0,0,0,0,0,0,0,0,0,0,0,
89856,768,2147483648u,310496,310496,29833216,65536,29964288,5,0,1991680,1998848,12,768,11794432,2147483648u,2147483648u,12050432,24272896,30097408,6210048,6094848,475904,73216,714752,716288,564992,629760,630528,548864,563200,597760,595968,0,2147483648u,121,0,0,0,0,0,0,0,0,0,0,0,0,
89856,768,2147483648u,230144,246528,24387584,2048,24403968,4,0,1992704,2031616,12,768,12152832,2147483648u,2147483648u,12140544,24363008,30113792,6275072,6160384,492288,75264,716800,718336,564992,629760,549760,555008,564224,597760,595968,0,2147483648u,122,1065353216,1065353216,0,0,0,0,0,0,0,0,0,0,
89856,768,2147483648u,1278720,1295104,24416256,2048,24432640,4,0,1993728,2064384,12,768,12154880,2147483648u,2147483648u,12230656,24453120,30130176,6275584,6225920,508672,77312,718848,720384,564992,629760,630528,550912,564224,597760,595968,0,2147483648u,123,1065353216,0,0,0,0,0,0,0,0,0,0,0,
102144,256,2147483648u,618560,618560,29519872,65536,29650944,5,0,3022848,2883584,12,768,14667776,2147483648u,2147483648u,14483456,24182784,27459584,7127040,7077888,918272,71168,770048,771584,572160,636928,550656,536576,571392,604928,604160,0,2147483648u,120,0,0,0,0,0,0,0,0,0,0,0,0,
102144,256,2147483648u,618720,618720,29847552,65536,29978624,5,0,3023872,2916352,12,768,14669824,2147483648u,2147483648u,14573568,24272896,27475968,7127552,7143424,934656,73216,772096,773632,572160,636928,638208,548864,571392,604928,604160,0,2147483648u,121,0,0,0,0,0,0,0,0,0,0,0,0,
102144,256,2147483648u,459520,475904,24244224,2048,24260608,4,0,3024896,2949120,12,768,15028224,2147483648u,2147483648u,14663680,24363008,27492352,7192576,7208960,951040,75264,774144,775680,572160,636928,550656,538624,572416,604928,604160,0,2147483648u,122,1065353216,1065353216,0,0,0,0,0,0,0,0,0,0,
102144,256,2147483648u,1377024,1393408,24522752,2048,24539136,4,2147483648u,3025920,2981888,12,768,15030272,2147483648u,2147483648u,14753792,24453120,27508736,7193088,7274496,967424,77312,776192,777728,572160,636928,638208,550912,572416,604928,604160,0,2147483648u,123,1065353216,0,0,0,0,0,0,0,0,0,0,0,
114432,768,2147483648u,662592,662592,29521920,65536,29652992,5,0,3039232,3014656,12,768,14675968,2147483648u,2147483648u,14843904,24182784,30081024,7258112,7077888,983808,71168,778240,779776,573184,637952,550784,552960,571392,605952,604160,0,2147483648u,120,0,0,0,0,0,0,0,0,0,0,0,0,
114432,768,2147483648u,662752,662752,29849600,65536,29980672,5,0,3040256,3047424,12,768,14678016,2147483648u,2147483648u,14934016,24272896,30097408,7258624,7143424,1000192,73216,780288,781824,573184,637952,638720,548864,571392,605952,604160,0,2147483648u,121,0,0,0,0,0,0,0,0,0,0,0,0,
114432,768,2147483648u,492288,508672,24276992,2048,24285184,4,0,3041280,3080192,12,768,15036416,2147483648u,2147483648u,15024128,24363008,30113792,7323648,7208960,1016576,75264,782336,783872,573184,637952,550784,555008,572416,605952,604160,0,2147483648u,122,1065353216,1065353216,0,0,0,0,0,0,0,0,0,0,
114432,768,2147483648u,1377024,1393408,24522752,2048,24539136,4,2147483648u,3042304,3112960,12,768,15038464,2147483648u,2147483648u,15114240,24453120,30130176,7324160,7274496,1032960,77312,784384,785920,573184,637952,638720,550912,572416,605952,604160,0,2147483648u,123,1065353216,0,0,0,0,0,0,0,0,0,0,0,
77568,256,2147483648u,970816,970816,29536256,65536,29667328,5,0,4071424,3932160,12,768,17551360,2147483648u,2147483648u,17367040,24182784,27459584,8175616,8126464,1442560,71168,835584,837120,580352,645120,551680,536576,579584,613120,612352,0,2147483648u,120,0,0,0,0,0,0,0,0,0,0,0,0,
77568,256,2147483648u,970976,970976,29863936,65536,29995008,5,0,4072448,3964928,12,768,17553408,2147483648u,2147483648u,17457152,24272896,27475968,8176128,8192000,1458944,73216,837632,839168,580352,645120,646400,548864,579584,613120,612352,0,2147483648u,121,0,0,0,0,0,0,0,0,0,0,0,0,
77568,256,2147483648u,721664,738048,24514560,2048,24530944,4,0,4073472,3997696,12,768,17911808,2147483648u,2147483648u,17547264,24363008,27492352,8241152,8257536,1475328,75264,839680,841216,580352,645120,551680,538624,580608,613120,612352,0,2147483648u,122,1065353216,1065353216,0,0,0,0,0,0,0,0,0,0,
77568,256,2147483648u,1377024,1393408,24522752,2048,24539136,4,2147483648u,4074496,4030464,12,768,17913856,2147483648u,2147483648u,17637376,24453120,27508736,8241664,8323072,1491712,77312,841728,843264,580352,645120,646400,550912,580608,613120,612352,0,2147483648u,123,1065353216,0,0,0,0,0,0,0,0,0,0,0,
89856,768,2147483648u,1014848,1014848,29538304,65536,29669376,5,0,4087808,4063232,12,768,17559552,2147483648u,2147483648u,17727488,24182784,30081024,8306688,8126464,1508096,71168,843776,845312,581376,646144,551808,552960,579584,614144,612352,0,2147483648u,120,0,0,0,0,0,0,0,0,0,0,0,0,
89856,768,2147483648u,1015008,1015008,29865984,65536,29997056,5,0,4088832,4096000,12,768,17561600,2147483648u,2147483648u,17817600,24272896,30097408,8307200,8192000,1524480,73216,845824,847360,581376,646144,646912,548864,579584,614144,612352,0,2147483648u,121,0,0,0,0,0,0,0,0,0,0,0,0,
89856,768,2147483648u,754432,770816,24199168,2048,24215552,4,0,4089856,4128768,12,768,17920000,2147483648u,2147483648u,17907712,24363008,30113792,8372224,8257536,1540864,75264,847872,849408,581376,646144,551808,555008,580608,614144,612352,0,2147483648u,122,1065353216,1065353216,0,0,0,0,0,0,0,0,0,0,
89856,768,2147483648u,1377024,1393408,24522752,2048,24539136,4,2147483648u,4090880,4161536,12,768,17922048,2147483648u,2147483648u,17997824,24453120,30130176,8372736,8323072,1557248,77312,849920,851456,581376,646144,646912,550912,580608,614144,612352,0,2147483648u,123,1065353216,0,0,0,0,0,0,0,0,0,0,0,
102144,256,2147483648u,1323072,1323072,29552640,65536,29683712,5,0,5120000,4980736,12,768,20434944,2147483648u,2147483648u,20250624,24182784,27459584,9224192,9175040,1966848,71168,901120,902656,588544,653312,552704,536576,587776,621312,620544,0,2147483648u,120,0,0,0,0,0,0,0,0,0,0,0,0,
102144,256,2147483648u,1323232,1323232,29880320,65536,30011392,5,0,5121024,5013504,12,768,20436992,2147483648u,2147483648u,20340736,24272896,27475968,9224704,9240576,1983232,73216,903168,904704,588544,653312,654592,548864,587776,621312,620544,0,2147483648u,121,0,0,0,0,0,0,0,0,0,0,0,0,
102144,256,2147483648u,983808,1000192,24444928,2048,24469504,4,0,5122048,5046272,12,768,20795392,2147483648u,2147483648u,20430848,24363008,27492352,9289728,9306112,1999616,75264,905216,906752,588544,653312,552704,538624,588800,621312,620544,0,2147483648u,122,1065353216,1065353216,0,0,0,0,0,0,0,0,0,0,
102144,256,2147483648u,1377024,1393408,24522752,2048,24539136,4,2147483648u,5123072,5079040,12,768,20797440,2147483648u,2147483648u,20520960,24453120,27508736,9290240,9371648,2016000,77312,907264,908800,588544,653312,654592,550912,588800,621312,620544,0,2147483648u,123,1065353216,0,0,0,0,0,0,0,0,0,0,0,
114432,768,2147483648u,1367104,1367104,29554688,65536,29685760,5,0,5136384,5111808,12,768,20443136,2147483648u,2147483648u,20611072,24182784,30081024,9355264,9175040,2032384,71168,909312,910848,589568,654336,552832,552960,587776,622336,620544,0,2147483648u,120,0,0,0,0,0,0,0,0,0,0,0,0,
114432,768,2147483648u,1367264,1367264,29882368,65536,30013440,5,0,5137408,5144576,12,768,20445184,2147483648u,2147483648u,20701184,24272896,30097408,9355776,9240576,2048768,73216,911360,912896,589568,654336,655104,548864,587776,622336,620544,0,2147483648u,121,0,0,0,0,0,0,0,0,0,0,0,0,
114432,768,2147483648u,1016576,1032960,24485888,2048,24502272,4,0,5138432,5177344,12,768,20803584,2147483648u,2147483648u,20791296,24363008,30113792,9420800,9306112,2065152,75264,913408,914944,589568,654336,552832,555008,588800,622336,620544,0,2147483648u,122,1065353216,1065353216,0,0,0,0,0,0,0,0,0,0,
114432,768,2147483648u,1377024,1393408,24522752,2048,24539136,4,2147483648u,5139456,5210112,12,768,20805632,2147483648u,2147483648u,20881408,24453120,30130176,9421312,9371648,2081536,77312,915456,916992,589568,654336,655104,550912,588800,622336,620544,0,2147483648u,123,1065353216,0,0,0,0,0,0,0,0,0,0,0,
77568,384,2147483648u,266624,266624,30158848,65536,30289920,5,0,1978368,1835008,12,896,11874304,2147483648u,2147483648u,11599872,24543232,27787264,6086656,6029312,394112,71424,704512,706304,564096,628736,553728,536576,563200,596864,595968,0,2147483648u,124,0,0,0,0,0,0,0,0,0,0,0,0,
77568,384,2147483648u,266784,266784,30486528,65536,30617600,5,0,1979392,1867776,12,896,11876352,2147483648u,2147483648u,11689984,24633344,27803648,6087168,6094848,410496,73472,706560,708352,564096,628736,630144,552960,563200,596864,595968,0,2147483648u,125,0,0,0,0,0,0,0,0,0,0,0,0,
77568,384,2147483648u,197504,213888,24723456,2048,24731648,4,0,1980416,1900544,12,896,12234752,2147483648u,2147483648u,11780096,24723456,27820032,6152192,6160384,426880,75520,708608,710400,564096,628736,553728,538624,564224,596864,595968,0,2147483648u,126,1065353216,1065353216,0,0,0,0,0,0,0,0,0,0,
77568,384,2147483648u,1246080,1262464,24743936,2048,24760320,4,0,1981440,1933312,12,896,12236800,2147483648u,2147483648u,11870208,24813568,27836416,6152704,6225920,443264,77568,710656,712448,564096,628736,630144,555008,564224,596864,595968,0,2147483648u,127,1065353216,0,0,0,0,0,0,0,0,0,0,0,
89856,896,2147483648u,310656,310656,30160896,65536,30291968,5,0,1994752,1966080,12,896,11882496,2147483648u,2147483648u,11960320,24543232,30408704,6217728,6029312,459648,71424,712704,714496,565120,629760,553856,552960,563200,597888,595968,0,2147483648u,124,0,0,0,0,0,0,0,0,0,0,0,0,
89856,896,2147483648u,310816,310816,30488576,65536,30619648,5,0,1995776,1998848,12,896,11884544,2147483648u,2147483648u,12050432,24633344,30425088,6218240,6094848,476032,73472,714752,716544,565120,629760,630656,552960,563200,597888,595968,0,2147483648u,125,0,0,0,0,0,0,0,0,0,0,0,0,
89856,896,2147483648u,230272,246656,24748032,2048,24764416,4,0,1996800,2031616,12,896,12242944,2147483648u,2147483648u,12140544,24723456,30441472,6283264,6160384,492416,75520,716800,718592,565120,629760,553856,555008,564224,597888,595968,0,2147483648u,126,1065353216,1065353216,0,0,0,0,0,0,0,0,0,0,
89856,896,2147483648u,1278848,1295232,24776704,2048,24793088,4,0,1997824,2064384,12,896,12244992,2147483648u,2147483648u,12230656,24813568,30457856,6283776,6225920,508800,77568,718848,720640,565120,629760,630656,555008,564224,597888,595968,0,2147483648u,127,1065353216,0,0,0,0,0,0,0,0,0,0,0,
102144,384,2147483648u,618880,618880,30175232,65536,30306304,5,0,3026944,2883584,12,896,14757888,2147483648u,2147483648u,14483456,24543232,27787264,7135232,7077888,918400,71424,770048,771840,572288,636928,554752,536576,571392,605056,604160,0,2147483648u,124,0,0,0,0,0,0,0,0,0,0,0,0,
102144,384,2147483648u,619040,619040,30502912,65536,30633984,5,0,3027968,2916352,12,896,14759936,2147483648u,2147483648u,14573568,24633344,27803648,7135744,7143424,934784,73472,772096,773888,572288,636928,638336,552960,571392,605056,604160,0,2147483648u,125,0,0,0,0,0,0,0,0,0,0,0,0,
102144,384,2147483648u,459648,476032,24604672,2048,24621056,4,0,3028992,2949120,12,896,15118336,2147483648u,2147483648u,14663680,24723456,27820032,7200768,7208960,951168,75520,774144,775936,572288,636928,554752,538624,572416,605056,604160,0,2147483648u,126,1065353216,1065353216,0,0,0,0,0,0,0,0,0,0,
102144,384,2147483648u,1377152,1393536,24883200,2048,24899584,4,2147483648u,3030016,2981888,12,896,15120384,2147483648u,2147483648u,14753792,24813568,27836416,7201280,7274496,967552,77568,776192,777984,572288,636928,638336,555008,572416,605056,604160,0,2147483648u,127,1065353216,0,0,0,0,0,0,0,0,0,0,0,
114432,896,2147483648u,662912,662912,30177280,65536,30308352,5,0,3043328,3014656,12,896,14766080,2147483648u,2147483648u,14843904,24543232,30408704,7266304,7077888,983936,71424,778240,780032,573312,637952,554880,552960,571392,606080,604160,0,2147483648u,124,0,0,0,0,0,0,0,0,0,0,0,0,
114432,896,2147483648u,663072,663072,30504960,65536,30636032,5,0,3044352,3047424,12,896,14768128,2147483648u,2147483648u,14934016,24633344,30425088,7266816,7143424,1000320,73472,780288,782080,573312,637952,638848,552960,571392,606080,604160,0,2147483648u,125,0,0,0,0,0,0,0,0,0,0,0,0,
114432,896,2147483648u,492416,508800,24637440,2048,24645632,4,0,3045376,3080192,12,896,15126528,2147483648u,2147483648u,15024128,24723456,30441472,7331840,7208960,1016704,75520,782336,784128,573312,637952,554880,555008,572416,606080,604160,0,2147483648u,126,1065353216,1065353216,0,0,0,0,0,0,0,0,0,0,
114432,896,2147483648u,1377152,1393536,24883200,2048,24899584,4,2147483648u,3046400,3112960,12,896,15128576,2147483648u,2147483648u,15114240,24813568,30457856,7332352,7274496,1033088,77568,784384,786176,573312,637952,638848,555008,572416,606080,604160,0,2147483648u,127,1065353216,0,0,0,0,0,0,0,0,0,0,0,
77568,384,2147483648u,971136,971136,30191616,65536,30322688,5,0,4075520,3932160,12,896,17641472,2147483648u,2147483648u,17367040,24543232,27787264,8183808,8126464,1442688,71424,835584,837376,580480,645120,555776,536576,579584,613248,612352,0,2147483648u,124,0,0,0,0,0,0,0,0,0,0,0,0,
77568,384,2147483648u,971296,971296,30519296,65536,30650368,5,0,4076544,3964928,12,896,17643520,2147483648u,2147483648u,17457152,24633344,27803648,8184320,8192000,1459072,73472,837632,839424,580480,645120,646528,552960,579584,613248,612352,0,2147483648u,125,0,0,0,0,0,0,0,0,0,0,0,0,
77568,384,2147483648u,721792,738176,24875008,2048,24891392,4,0,4077568,3997696,12,896,18001920,2147483648u,2147483648u,17547264,24723456,27820032,8249344,8257536,1475456,75520,839680,841472,580480,645120,555776,538624,580608,613248,612352,0,2147483648u,126,1065353216,1065353216,0,0,0,0,0,0,0,0,0,0,
77568,384,2147483648u,1377152,1393536,24883200,2048,24899584,4,2147483648u,4078592,4030464,12,896,18003968,2147483648u,2147483648u,17637376,24813568,27836416,8249856,8323072,1491840,77568,841728,843520,580480,645120,646528,555008,580608,613248,612352,0,2147483648u,127,1065353216,0,0,0,0,0,0,0,0,0,0,0,
89856,896,2147483648u,1015168,1015168,30193664,65536,30324736,5,0,4091904,4063232,12,896,17649664,2147483648u,2147483648u,17727488,24543232,30408704,8314880,8126464,1508224,71424,843776,845568,581504,646144,555904,552960,579584,614272,612352,0,2147483648u,124,0,0,0,0,0,0,0,0,0,0,0,0,
89856,896,2147483648u,1015328,1015328,30521344,65536,30652416,5,0,4092928,4096000,12,896,17651712,2147483648u,2147483648u,17817600,24633344,30425088,8315392,8192000,1524608,73472,845824,847616,581504,646144,647040,552960,579584,614272,612352,0,2147483648u,125,0,0,0,0,0,0,0,0,0,0,0,0,
89856,896,2147483648u,754560,770944,24559616,2048,24576000,4,0,4093952,4128768,12,896,18010112,2147483648u,2147483648u,17907712,24723456,30441472,8380416,8257536,1540992,75520,847872,849664,581504,646144,555904,555008,580608,614272,612352,0,2147483648u,126,1065353216,1065353216,0,0,0,0,0,0,0,0,0,0,
89856,896,2147483648u,1377152,1393536,24883200,2048,24899584,4,2147483648u,4094976,4161536,12,896,18012160,2147483648u,2147483648u,17997824,24813568,30457856,8380928,8323072,1557376,77568,849920,851712,581504,646144,647040,555008,580608,614272,612352,0,2147483648u,127,1065353216,0,0,0,0,0,0,0,0,0,0,0,
102144,384,2147483648u,1323392,1323392,30208000,65536,30339072,5,0,5124096,4980736,12,896,20525056,2147483648u,2147483648u,20250624,24543232,27787264,9232384,9175040,1966976,71424,901120,902912,588672,653312,556800,536576,587776,621440,620544,0,2147483648u,124,0,0,0,0,0,0,0,0,0,0,0,0,
102144,384,2147483648u,1323552,1323552,30535680,65536,30666752,5,0,5125120,5013504,12,896,20527104,2147483648u,2147483648u,20340736,24633344,27803648,9232896,9240576,1983360,73472,903168,904960,588672,653312,654720,552960,587776,621440,620544,0,2147483648u,125,0,0,0,0,0,0,0,0,0,0,0,0,
102144,384,2147483648u,983936,1000320,24805376,2048,24829952,4,0,5126144,5046272,12,896,20885504,2147483648u,2147483648u,20430848,24723456,27820032,9297920,9306112,1999744,75520,905216,907008,588672,653312,556800,538624,588800,621440,620544,0,2147483648u,126,1065353216,1065353216,0,0,0,0,0,0,0,0,0,0,
102144,384,2147483648u,1377152,1393536,24883200,2048,24899584,4,2147483648u,5127168,5079040,12,896,20887552,2147483648u,2147483648u,20520960,24813568,27836416,9298432,9371648,2016128,77568,907264,909056,588672,653312,654720,555008,588800,621440,620544,0,2147483648u,127,1065353216,0,0,0,0,0,0,0,0,0,0,0,
114432,896,2147483648u,1367424,1367424,30210048,65536,30341120,5,0,5140480,5111808,12,896,20533248,2147483648u,2147483648u,20611072,24543232,30408704,9363456,9175040,2032512,71424,909312,911104,589696,654336,556928,552960,587776,622464,620544,0,2147483648u,124,0,0,0,0,0,0,0,0,0,0,0,0,
114432,896,2147483648u,1367584,1367584,30537728,65536,30668800,5,0,5141504,5144576,12,896,20535296,2147483648u,2147483648u,20701184,24633344,30425088,9363968,9240576,2048896,73472,911360,913152,589696,654336,655232,552960,587776,622464,620544,0,2147483648u,125,0,0,0,0,0,0,0,0,0,0,0,0,
114432,896,2147483648u,1016704,1033088,24846336,2048,24862720,4,0,5142528,5177344,12,896,20893696,2147483648u,2147483648u,20791296,24723456,30441472,9428992,9306112,2065280,75520,913408,915200,589696,654336,556928,555008,588800,622464,620544,0,2147483648u,126,1065353216,1065353216,0,0,0,0,0,0,0,0,0,0,
114432,896,2147483648u,1377152,1393536,24883200,2048,24899584,4,2147483648u,5143552,5210112,12,896,20895744,2147483648u,2147483648u,20881408,24813568,30457856,9429504,9371648,2081664,77568,915456,917248,589696,654336,655232,555008,588800,622464,620544,0,2147483648u,127,1065353216,0,0,0,0,0,0,0,0,0,0,0};

__global__ void __launch_bounds__(256, 1) fused_block_kernel(const void* p0, unsigned n0, unsigned f0, const void* p1, unsigned n1, unsigned f1, const void* p2, unsigned n2, unsigned f2, const void* p3, unsigned n3, unsigned f3, const void* p4, unsigned n4, unsigned f4, const void* p5, unsigned n5, unsigned f5, const void* p6, unsigned n6, unsigned f6, const void* p7, unsigned n7, unsigned f7, const void* p8, unsigned n8, unsigned f8, const void* p9, unsigned n9, unsigned f9) {
  __shared__ __attribute__((aligned(16))) char lds[163840];
#if defined(__HIP_DEVICE_COMPILE__)
  asm volatile(
      "s_load_dwordx16 s[4:19], s[0:1], 0\n"
      "s_load_dwordx16 s[20:35], s[0:1], 64\n"
      "s_load_dwordx8 s[36:43], s[0:1], 128\n"
      "s_getpc_b64 s[92:93]\n"
      ".Lpc:\n"
      "v_lshrrev_b32 v32, 6, v0\n"
      "v_and_b32 v1, 63, v0\n"
      "s_bfe_u32 s94, s2, 0x30003\n"
      "s_lshl_b32 s94, s94, 13\n"
      "v_and_b32 v48, 15, v0\n"
      "v_lshl_add_u32 v33, v32, 4, v48\n"
      "v_lshl_add_u32 v33, v33, 7, s94\n"
      "v_min_u32 v33, .Lend-.Lpc-4, v33\n"
      "global_load_dword v34, v33, s[92:93]\n"
      "s_getpc_b64 s[92:93]\n"
      "s_add_u32 s92, s92, role_table@rel32@lo+4\n"
      "s_addc_u32 s93, s93, role_table@rel32@hi+12\n"
      "s_mul_i32 s94, s2, 768\n"
      "v_min_u32 v33, 5, v0\n"
      "v_lshl_add_u32 v33, v33, 7, s94\n"
      "global_load_dword v34, v33, s[92:93]\n"
      "v_readfirstlane_b32 s84, v32\n"
      "s_lshr_b32 s85, s2, 5\n"
      "s_bfe_u32 s94, s2, 0x20001\n"
      "s_lshl3_add_u32 s94, s94, s85\n"
      "s_lshl_b32 s88, s94, 16\n"
      "s_sub_u32 s87, s88, 16384\n"
      "s_max_i32 s87, s87, 0\n"
      "s_add_u32 s89, s88, 16384\n"
      "s_add_u32 s90, s88, 32768\n"
      "s_add_u32 s91, s88, 49152\n"
      "v_lshrrev_b32 v12, 4, v1\n"
      "s_lshl_b32 s94, s85, 3\n"
      "v_add_u32 v49, s94, v48\n"
      "v_lshl_add_u32 v50, v32, 2, v12\n"
      "v_lshlrev_b32 v47, 10, v50\n"
      "v_add_u32 v51, 0, v49\n"
      "v_and_b32 v51, 63, v51\n"
      "v_lshl_add_u32 v20, v51, 4, v47\n"
      "v_add_u32 v51, 16, v49\n"
      "v_and_b32 v51, 63, v51\n"
      "v_lshl_add_u32 v21, v51, 4, v47\n"
      "v_add_u32 v51, 32, v49\n"
      "v_and_b32 v51, 63, v51\n"
      "v_lshl_add_u32 v22, v51, 4, v47\n"
      "v_add_u32 v51, 48, v49\n"
      "v_and_b32 v51, 63, v51\n"
      "v_lshl_add_u32 v23, v51, 4, v47\n"
      "s_lshr_b32 s3, s2, 6\n"
      "v_lshrrev_b32 v35, 3, v1\n"
      "v_and_b32 v36, 2, v35\n"
      "v_and_b32 v46, 1, v32\n"
      "v_lshl_or_b32 v36, v46, 2, v36\n"
      "v_and_b32 v37, 7, v1\n"
      "v_xor_b32 v36, v36, v37\n"
      "v_lshlrev_b32 v36, 4, v36\n"
      "v_lshl_add_u32 v38, v32, 3, v35\n"
      "v_mul_u32_u24 v39, 6144, v38\n"
      "v_add_u32 v39, v39, v36\n"
      "v_and_b32 v40, 127, v0\n"
      "v_lshlrev_b32 v40, 1, v40\n"
      "s_and_b32 s94, s2, 3\n"
      "s_lshl_b32 s82, s94, 8\n"
      "s_mul_i32 s94, s94, 786432\n"
      "s_lshr_b32 s95, s2, 2\n"
      "s_min_u32 s95, s95, 47\n"
      "s_lshl_b32 s95, s95, 7\n"
      "s_add_u32 s83, s94, s95\n"
      "s_lshl_b32 s86, s84, 10\n"
      "s_lshl2_add_u32 s94, s2, s84\n"
      "s_mul_i32 s94, s94, 192\n"
      "s_waitcnt lgkmcnt(0)\n"
      "s_load_dwordx16 s[44:59], s[92:93], s94\n"
      "s_load_dwordx16 s[60:75], s[92:93], s94 offset:64\n"
      "s_load_dwordx4 s[76:79], s[92:93], s94 offset:128\n"
      "s_load_dwordx2 s[80:81], s[92:93], s94 offset:144\n"
      "s_mov_b32 s0, s83\n"
      "s_mov_b32 s1, s82\n"
      "buffer_load_dword v41, off, s[4:7], 0 sc1\n"
      "s_cmp_eq_u32 s3, 0\n"
      "s_cbranch_scc0 .Lno_1\n"
      "s_mov_b32 m0, s86\n"
      "buffer_load_ushort v42, v40, s[8:11], s1 offen\n"
      "buffer_load_dwordx4 v39, s[12:15], s83 offen lds\n"
      "s_add_u32 m0, s86, 4096\n"
      "s_add_u32 s83, s83, 196608\n"
      "buffer_load_dwordx4 v39, s[12:15], s83 offen lds\n"
      "s_add_u32 m0, s86, 8192\n"
      "s_add_u32 s83, s83, 196608\n"
      "buffer_load_dwordx4 v39, s[12:15], s83 offen lds\n"
      "s_add_u32 m0, s86, 12288\n"
      "s_add_u32 s83, s83, 196608\n"
      "buffer_load_dwordx4 v39, s[12:15], s83 offen lds\n"
      "s_barrier\n"
      ".Lno_1:\n"
      "buffer_load_dwordx4 v[112:115], v20, s[16:19], s87 offen\n"
      "buffer_load_dwordx4 v[116:119], v21, s[16:19], s87 offen\n"
      "buffer_load_dwordx4 v[120:123], v22, s[16:19], s87 offen\n"
      "buffer_load_dwordx4 v[124:127], v23, s[16:19], s87 offen\n"
      "buffer_load_dwordx4 v[144:147], v20, s[16:19], s88 offen\n"
      "s_cmp_lg_u32 s3, 0\n"
      "s_cbranch_scc0 .Lno_2\n"
      "buffer_load_dwordx4 v[148:151], v21, s[16:19], s88 offen\n"
      "buffer_load_dwordx4 v[152:155], v22, s[16:19], s88 offen\n"
      "buffer_load_dwordx4 v[156:159], v23, s[16:19], s88 offen\n"
      "buffer_load_dwordx4 v[176:179], v20, s[16:19], s89 offen\n"
      ".Lno_2:\n"
      "s_mov_b32 s82, 0x3f803f80\n"
      "s_mov_b32 s83, 0xffff0000\n"
      "v_bfe_u32 v52, v1, 2, 2\n"
      "v_and_b32 v53, 3, v1\n"
      "v_bfe_u32 v54, v1, 3, 2\n"
      "v_lshlrev_b32 v55, 7, v52\n"
      "v_lshl_add_u32 v55, v53, 3, v55\n"
      "v_lshlrev_b32 v27, 1, v46\n"
      "v_xor_b32 v27, v27, v54\n"
      "v_lshl_add_u32 v27, v27, 5, v55\n"
      "v_lshl_add_u32 v27, v12, 10, v27\n"
      "v_xor_b32 v28, 32, v27\n"
      "v_lshlrev_b32 v29, 4, v12\n"
      "v_lshlrev_b32 v30, 3, v1\n"
      "v_lshl_add_u32 v30, v46, 7, v30\n"
      "v_and_b32 v44, 0xb0, v0\n"
      "v_cmp_eq_u32 vcc, 0, v44\n"
      "v_mov_b32 v51, 2147483648\n"
      "v_lshlrev_b32 v56, 4, v37\n"
      "v_cndmask_b32 v30, v51, v30, vcc\n"
      "v_lshl_add_u32 v2, v35, 8, v56\n"
      "v_mul_u32_u24 v6, 3072, v38\n"
      "v_add_u32 v6, v6, v36\n"
      "v_xor_b32 v61, v32, v54\n"
      "v_lshl_add_u32 v61, v61, 5, v55\n"
      "v_mul_u32_u24 v62, 3072, v12\n"
      "v_add_u32 v61, v61, v62\n"
      "v_add_u32 v7, 81920, v61\n"
      "v_add_u32 v8, 131072, v61\n"
      "v_lshlrev_b32 v13, 4, v0\n"
      "v_bfe_u32 v44, v0, 3, 4\n"
      "v_bfe_u32 v45, v0, 2, 1\n"
      "v_lshl_add_u32 v44, v44, 2, v45\n"
      "v_bfe_u32 v45, v0, 1, 1\n"
      "v_lshlrev_b32 v45, 10, v45\n"
      "v_and_b32 v57, 1, v0\n"
      "v_lshl_add_u32 v45, v57, 3, v45\n"
      "v_lshrrev_b32 v58, 1, v32\n"
      "v_lshl_add_u32 v45, v58, 11, v45\n"
      "s_lshl_b32 s94, s85, 3\n"
      "v_subrev_u32 v44, s94, v44\n"
      "v_and_b32 v44, 63, v44\n"
      "v_lshl_add_u32 v80, v44, 4, v45\n"
      "v_add_u32 v80, 155648, v80\n"
      "v_mul_u32_u24 v49, 5120, v52\n"
      "v_lshl_add_u32 v49, v53, 8, v49\n"
      "v_xor_b32 v50, v50, v37\n"
      "v_lshl_add_u32 v81, v50, 4, v49\n"
      "v_lshlrev_b32 v59, 4, v48\n"
      "v_add_u32 v83, 155648, v59\n"
      "v_xor_b32 v49, v48, v12\n"
      "v_lshlrev_b32 v49, 4, v49\n"
      "v_lshl_add_u32 v9, v12, 8, v49\n"
      "v_xor_b32 v10, 64, v9\n"
      "s_lshl_b32 s85, s85, 1\n"
      "v_mov_b32 v16, -1\n"
      "v_mov_b32 v17, -1\n"
      "v_mov_b32 v18, -1\n"
      "v_mov_b32 v19, -1\n"
      "v_and_b32 v44, 1, v35\n"
      "v_lshl_add_u32 v44, v44, 8, v56\n"
      "v_lshl_add_u32 v94, v12, 10, v44\n"
      "v_lshrrev_b32 v60, 5, v1\n"
      "v_lshl_add_u32 v44, v60, 9, v44\n"
      "v_and_b32 v62, 1, v12\n"
      "v_lshl_add_u32 v89, v62, 15, v44\n"
      "v_lshl_add_u32 v5, v48, 10, v29\n"
      "v_lshlrev_b32 v11, 7, v37\n"
      "v_lshlrev_b32 v44, 2, v1\n"
      "v_and_b32 v45, 0xa0, v0\n"
      "v_cmp_eq_u32 vcc, 0, v45\n"
      "v_lshlrev_b32 v24, 7, v48\n"
      "v_lshl_or_b32 v4, v58, 31, v2\n"
      "v_cndmask_b32 v25, v51, v44, vcc\n"
      "v_cmp_gt_u32 vcc, 32, v0\n"
      "v_lshlrev_b32 v45, 6, v62\n"
      "v_xor_b32 v45, v45, v56\n"
      "v_cndmask_b32 v26, v51, v44, vcc\n"
      "v_lshl_add_u32 v88, v38, 10, v45\n"
      "v_mul_u32_u24 v44, 12288, v12\n"
      "v_add_u32 v44, v44, v59\n"
      "v_accvgpr_write_b32 a255, v44\n"
      "v_lshrrev_b32 v44, 1, v52\n"
      "v_lshlrev_b32 v44, 6, v44\n"
      "v_lshl_add_u32 v44, v53, 4, v44\n"
      "v_lshl_add_u32 v44, v52, 7, v44\n"
      "v_xor_b32 v45, 64, v44\n"
      "v_mul_u32_u24 v61, 1032, v12\n"
      "v_add_u32 v61, 81920, v61\n"
      "v_add_u32 v44, v44, v61\n"
      "v_add_u32 v45, v45, v61\n"
      "v_accvgpr_write_b32 a254, v44\n"
      "v_accvgpr_write_b32 a253, v45\n"
      "v_lshlrev_b32 v44, 3, v57\n"
      "v_lshl_add_u32 v44, v60, 2, v44\n"
      "v_bfe_u32 v45, v1, 1, 1\n"
      "v_lshl_add_u32 v44, v45, 10, v44\n"
      "v_bfe_u32 v45, v1, 2, 1\n"
      "v_lshl_add_u32 v44, v45, 4, v44\n"
      "v_lshl_add_u32 v44, v62, 5, v44\n"
      "v_bfe_u32 v45, v1, 3, 1\n"
      "v_lshl_add_u32 v44, v45, 6, v44\n"
      "v_lshl_add_u32 v44, v32, 7, v44\n"
      "v_add_u32 v44, 155648, v44\n"
      "v_accvgpr_write_b32 a252, v44\n"
      "v_and_b32 v44, 31, v1\n"
      "v_xor_b32 v45, v60, v48\n"
      "v_lshlrev_b32 v45, 4, v45\n"
      "v_lshl_add_u32 v45, v44, 8, v45\n"
      "v_lshl_add_u32 v45, v32, 14, v45\n"
      "v_add_u32 v45, 81920, v45\n"
      "v_accvgpr_write_b32 a251, v45\n"
      "v_lshlrev_b32 v44, 1, v37\n"
      "v_xor_b32 v44, v44, v35\n"
      "v_lshlrev_b32 v44, 4, v44\n"
      "v_lshl_add_u32 v44, v35, 8, v44\n"
      "v_lshl_add_u32 v44, v32, 12, v44\n"
      "v_add_u32 v44, 81920, v44\n"
      "v_accvgpr_write_b32 a250, v44\n"
      "v_lshlrev_b32 v44, 3, v48\n"
      "v_lshl_add_u32 v44, v60, 7, v44\n"
      "v_lshl_add_u32 v45, v62, 10, v44\n"
      "v_lshl_or_b32 v44, v62, 31, v44\n"
      "v_accvgpr_write_b32 a249, v44\n"
      "v_accvgpr_write_b32 a248, v45\n"
      "v_lshl_add_u32 v64, v60, 3, v52\n"
      "s_waitcnt lgkmcnt(0)\n"
      "s_cmp_lt_u32 s84, 2\n"
      "s_cbranch_scc0 .Lno_3\n"
      "s_lshl_b32 s94, s52, 1\n"
      "v_add_u32 v44, 0, v1\n"
      "v_mul_u32_u24 v45, 171, v44\n"
      "v_lshrrev_b32 v45, 11, v45\n"
      "v_mul_u32_u24 v61, 12, v45\n"
      "v_sub_u32 v61, v44, v61\n"
      "v_cmp_gt_u32 vcc, s94, v61\n"
      "v_mul_u32_u24 v45, 2752, v45\n"
      "v_lshl_add_u32 v61, v61, 4, v45\n"
      "v_cndmask_b32 v90, v51, v61, vcc\n"
      "v_add_u32 v44, 64, v1\n"
      "v_mul_u32_u24 v45, 171, v44\n"
      "v_lshrrev_b32 v45, 11, v45\n"
      "v_mul_u32_u24 v61, 12, v45\n"
      "v_sub_u32 v61, v44, v61\n"
      "v_cmp_gt_u32 vcc, s94, v61\n"
      "v_mul_u32_u24 v45, 2752, v45\n"
      "v_lshl_add_u32 v61, v61, 4, v45\n"
      "v_cndmask_b32 v91, v51, v61, vcc\n"
      "v_add_u32 v44, 128, v1\n"
      "v_mul_u32_u24 v45, 171, v44\n"
      "v_lshrrev_b32 v45, 11, v45\n"
      "v_mul_u32_u24 v61, 12, v45\n"
      "v_sub_u32 v61, v44, v61\n"
      "v_cmp_gt_u32 vcc, s94, v61\n"
      "v_mul_u32_u24 v45, 2752, v45\n"
      "v_lshl_add_u32 v61, v61, 4, v45\n"
      "v_cndmask_b32 v92, v51, v61, vcc\n"
      "v_mul_u32_u24 v63, 192, v64\n"
      "v_mul_u32_u24 v61, 12288, v62\n"
      "v_lshl_add_u32 v63, v57, 4, v63\n"
      "v_lshrrev_b32 v44, 1, v53\n"
      "v_lshl_add_u32 v63, v44, 3, v63\n"
      "v_add_u32 v93, v63, v61\n"
      ".Lno_3:\n"
      "s_cmp_ge_u32 s84, 2\n"
      "s_cbranch_scc0 .Lno_4\n"
      "s_mov_b64 s[24:25], s[36:37]\n"
      "s_mov_b64 s[28:29], s[36:37]\n"
      "v_lshl_add_u32 v44, v35, 10, v56\n"
      "v_or_b32 v90, s53, v44\n"
      "v_add_u32 v91, 8192, v90\n"
      "v_mov_b32 v92, v51\n"
      "v_lshlrev_b32 v63, 7, v64\n"
      "v_lshl_add_u32 v63, v62, 5, v63\n"
      "v_lshl_add_u32 v93, v53, 3, v63\n"
      ".Lno_4:\n"
      "v_mul_u32_u24 v44, 3072, v32\n"
      "v_add_u32 v44, 131072, v44\n"
      "v_add_u32 v93, v93, v44\n"
      "v_or_b32 v95, s53, v2\n"
      "v_or_b32 v3, s60, v2\n"
      "v_add_u32 v6, s45, v6\n"
      "v_add_u32 v88, s57, v88\n"
      "v_or_b32 v31, s46, v39\n"
      "s_lshr_b32 s94, s20, 7\n"
      "s_sub_u32 s94, 3, s94\n"
      "s_and_b32 s94, s94, 7\n"
      "s_lshl_b32 s94, s94, 7\n"
      "s_xor_b32 s95, s94, s45\n"
      "s_lshl_b32 s95, s95, 22\n"
      "s_and_b32 s95, s95, 2147483648\n"
      "s_or_b32 s94, s94, s95\n"
      "s_mul_i32 s95, s79, 12\n"
      "v_min_u32 v44, 11, v1\n"
      "v_add_u32 v44, s95, v44\n"
      "v_lshl_or_b32 v44, v44, 10, s94\n"
      "buffer_load_dword v45, v44, s[20:23], 0 offen\n"
      "v_lshl_add_u32 v44, v54, 5, v37\n"
      "v_lshl_add_u32 v44, v32, 3, v44\n"
      "v_mul_u32_u24 v44, 6144, v44\n"
      "v_or_b32 v44, s46, v44\n"
      "v_and_b32 v61, 31, v1\n"
      "s_lshl_b32 s95, s79, 12\n"
      "v_lshl_add_u32 v61, v61, 7, s95\n"
      "s_mul_i32 s79, s84, 3072\n"
      "s_lshl2_add_u32 s95, s2, s84\n"
      "s_mul_i32 s95, s95, 384\n"
      "s_add_u32 s95, s95, 524288\n"
      "v_min_u32 v63, 2, v1\n"
      "v_lshl_add_u32 v63, v63, 7, s95\n"
      "v_accvgpr_write_b32 a247, v63\n"
      "v_accvgpr_write_b32 a246, v44\n"
      "v_accvgpr_write_b32 a245, v61\n"
      "v_accvgpr_write_b32 a244, v40\n"
      "s_cmp_eq_u32 s3, 0\n"
      "s_cbranch_scc0 .Lno_5\n"
      "s_waitcnt vmcnt(10)\n"
      "v_lshlrev_b32 v42, 16, v42\n"
      "v_mul_f32 v43, 0xbfb8aa3b, v42\n"
      "v_exp_f32 v43, v43\n"
      "s_nop 0\n"
      "v_add_f32 v43, 1.0, v43\n"
      "v_rcp_f32 v43, v43\n"
      "s_nop 0\n"
      "v_mul_f32 v43, v43, v42\n"
      "v_cvt_pk_bf16_f32 v43, v43, v43\n"
      "ds_write_b16 v40, v43 offset:16384\n"
      "s_waitcnt vmcnt(6)\n"
      "v_readfirstlane_b32 s86, v41\n"
      "s_mul_i32 s86, s86, 0x9e3779b1\n"
      "s_add_u32 s86, s86, 0x7f4a7c15\n"
      "s_waitcnt lgkmcnt(0)\n"
      "s_barrier\n"
      "ds_read_b128 a[0:3], v29 offset:16384\n"
      "ds_read_b64_tr_b16 a[4:5], v27 offset:0\n"
      "ds_read_b64_tr_b16 a[6:7], v27 offset:512\n"
      "ds_read_b64_tr_b16 a[8:9], v28 offset:0\n"
      "ds_read_b64_tr_b16 a[10:11], v28 offset:512\n"
      "ds_read_b128 a[16:19], v29 offset:16448\n"
      "ds_read_b64_tr_b16 a[20:21], v27 offset:4096\n"
      "ds_read_b64_tr_b16 a[22:23], v27 offset:4608\n"
      "ds_read_b64_tr_b16 a[24:25], v28 offset:4096\n"
      "ds_read_b64_tr_b16 a[26:27], v28 offset:4608\n"
      "buffer_load_dwordx4 v[148:151], v21, s[16:19], s88 offen\n"
      "ds_read_b128 a[32:35], v29 offset:16512\n"
      "ds_read_b64_tr_b16 a[36:37], v27 offset:8192\n"
      "ds_read_b64_tr_b16 a[38:39], v27 offset:8704\n"
      "ds_read_b64_tr_b16 a[40:41], v28 offset:8192\n"
      "ds_read_b64_tr_b16 a[42:43], v28 offset:8704\n"
      "ds_read_b128 a[48:51], v29 offset:16576\n"
      "ds_read_b64_tr_b16 a[52:53], v27 offset:12288\n"
      "ds_read_b64_tr_b16 a[54:55], v27 offset:12800\n"
      "ds_read_b64_tr_b16 a[56:57], v28 offset:12288\n"
      "ds_read_b64_tr_b16 a[58:59], v28 offset:12800\n"
      "buffer_load_dwordx4 v[152:155], v22, s[16:19], s88 offen\n"
      "s_waitcnt lgkmcnt(15)\n"
      "v_mfma_f32_16x16x32_bf16 v[68:71], a[0:3], a[4:7], 0\n"
      "v_mfma_f32_16x16x32_bf16 v[72:75], a[0:3], a[8:11], 0\n"
      "s_waitcnt lgkmcnt(11)\n"
      "v_mfma_f32_16x16x32_bf16 v[68:71], a[16:19], a[20:23], v[68:71]\n"
      "s_waitcnt lgkmcnt(9)\n"
      "v_mfma_f32_16x16x32_bf16 v[72:75], a[16:19], a[24:27], v[72:75]\n"
      "s_waitcnt lgkmcnt(6)\n"
      "buffer_load_dwordx4 v[156:159], v23, s[16:19], s88 offen\n"
      "v_mfma_f32_16x16x32_bf16 v[68:71], a[32:35], a[36:39], v[68:71]\n"
      "s_waitcnt lgkmcnt(4)\n"
      "v_mfma_f32_16x16x32_bf16 v[72:75], a[32:35], a[40:43], v[72:75]\n"
      "s_waitcnt lgkmcnt(2)\n"
      "v_mfma_f32_16x16x32_bf16 v[68:71], a[48:51], a[52:55], v[68:71]\n"
      "s_waitcnt lgkmcnt(0)\n"
      "v_mfma_f32_16x16x32_bf16 v[72:75], a[48:51], a[56:59], v[72:75]\n"
      "buffer_load_dwordx4 v[176:179], v20, s[16:19], s89 offen\n"
      "v_mov_b32 v67, s86\n"
      "s_nop 5\n"
      "v_cvt_pk_bf16_f32 v66, v68, v72\n"
      "buffer_store_dwordx2 v[66:67], v30, s[4:7], s44 offen sc1\n"
      ".Lno_5:\n"
      "s_waitcnt vmcnt(9)\n"
      "v_dot2_f32_bf16 v84, v112, s82, 0\n"
      "buffer_load_dwordx4 v[180:183], v21, s[16:19], s89 offen\n"
      "v_dot2_f32_bf16 v85, v112, v112, 0\n"
      "s_nop 0\n"
      "v_dot2c_f32_bf16 v84, s82, v113\n"
      "s_nop 0\n"
      "v_dot2c_f32_bf16 v85, v113, v113\n"
      "v_dot2c_f32_bf16 v84, s82, v114\n"
      "v_dot2c_f32_bf16 v85, v114, v114\n"
      "v_dot2c_f32_bf16 v84, s82, v115\n"
      "v_dot2c_f32_bf16 v85, v115, v115\n"
      "s_waitcnt vmcnt(9)\n"
      "v_dot2c_f32_bf16 v84, s82, v116\n"
      "v_dot2c_f32_bf16 v85, v116, v116\n"
      "v_dot2c_f32_bf16 v84, s82, v117\n"
      "v_dot2c_f32_bf16 v85, v117, v117\n"
      "v_dot2c_f32_bf16 v84, s82, v118\n"
      "v_dot2c_f32_bf16 v85, v118, v118\n"
      "buffer_load_dwordx4 v[184:187], v22, s[16:19], s89 offen\n"
      "v_dot2c_f32_bf16 v84, s82, v119\n"
      "v_dot2c_f32_bf16 v85, v119, v119\n"
      "s_waitcnt vmcnt(9)\n"
      "v_dot2c_f32_bf16 v84, s82, v120\n"
      "v_dot2c_f32_bf16 v85, v120, v120\n"
      "v_dot2c_f32_bf16 v84, s82, v121\n"
      "v_dot2c_f32_bf16 v85, v121, v121\n"
      "v_dot2c_f32_bf16 v84, s82, v122\n"
      "v_dot2c_f32_bf16 v85, v122, v122\n"
      "v_dot2c_f32_bf16 v84, s82, v123\n"
      "v_dot2c_f32_bf16 v85, v123, v123\n"
      "s_waitcnt vmcnt(8)\n"
      "v_dot2c_f32_bf16 v84, s82, v124\n"
      "v_dot2c_f32_bf16 v85, v124, v124\n"
      "v_dot2c_f32_bf16 v84, s82, v125\n"
      "v_dot2c_f32_bf16 v85, v125, v125\n"
      "v_dot2c_f32_bf16 v84, s82, v126\n"
      "v_dot2c_f32_bf16 v85, v126, v126\n"
      "v_dot2c_f32_bf16 v84, s82, v127\n"
      "v_dot2c_f32_bf16 v85, v127, v127\n"
      "v_lshlrev_b32 v96, 16, v112\n"
      "v_and_b32 v97, s83, v112\n"
      "v_add_f32_dpp v84, v84, v84 quad_perm:[1,0,3,2] row_mask:0xf bank_mask:0xf\n"
      "v_add_f32_dpp v85, v85, v85 quad_perm:[1,0,3,2] row_mask:0xf bank_mask:0xf\n"
      "v_lshlrev_b32 v98, 16, v113\n"
      "v_add_f32_dpp v84, v84, v84 quad_perm:[2,3,0,1] row_mask:0xf bank_mask:0xf\n"
      "v_add_f32_dpp v85, v85, v85 quad_perm:[2,3,0,1] row_mask:0xf bank_mask:0xf\n"
      "v_and_b32 v99, s83, v113\n"
      "v_add_f32_dpp v84, v84, v84 row_half_mirror row_mask:0xf bank_mask:0xf\n"
      "buffer_load_dwordx4 v[188:191], v23, s[16:19], s89 offen\n"
      "v_add_f32_dpp v85, v85, v85 row_half_mirror row_mask:0xf bank_mask:0xf\n"
      "v_lshlrev_b32 v100, 16, v114\n"
      "v_add_f32_dpp v84, v84, v84 row_mirror row_mask:0xf bank_mask:0xf\n"
      "v_add_f32_dpp v85, v85, v85 row_mirror row_mask:0xf bank_mask:0xf\n"
      "v_and_b32 v101, s83, v114\n"
      "v_mul_f32 v84, 0x3b000000, v84\n"
      "v_mul_f32 v85, 0x3b000000, v85\n"
      "v_fma_f32 v85, -v84, v84, v85\n"
      "v_max_f32 v85, 0, v85\n"
      "v_add_f32 v85, 0x3727c5ac, v85\n"
      "v_rsq_f32 v86, v85\n"
      "v_lshlrev_b32 v102, 16, v115\n"
      "v_mul_f32_e64 v87, -v84, v86\n"
      "v_and_b32 v103, s83, v115\n"
      "v_lshlrev_b32 v104, 16, v116\n"
      "v_and_b32 v105, s83, v116\n"
      "v_lshlrev_b32 v106, 16, v117\n"
      "v_and_b32 v107, s83, v117\n"
      "v_lshlrev_b32 v108, 16, v118\n"
      "v_and_b32 v109, s83, v118\n"
      "v_lshlrev_b32 v110, 16, v119\n"
      "v_and_b32 v111, s83, v119\n"
      "v_pk_fma_f32 v[96:97], v[96:97], v[86:87], v[86:87] op_sel:[0,0,1] op_sel_hi:[1,0,1]\n"
      "v_pk_fma_f32 v[98:99], v[98:99], v[86:87], v[86:87] op_sel:[0,0,1] op_sel_hi:[1,0,1]\n"
      "v_pk_fma_f32 v[100:101], v[100:101], v[86:87], v[86:87] op_sel:[0,0,1] op_sel_hi:[1,0,1]\n"
      "v_pk_fma_f32 v[102:103], v[102:103], v[86:87], v[86:87] op_sel:[0,0,1] op_sel_hi:[1,0,1]\n"
      "v_pk_fma_f32 v[104:105], v[104:105], v[86:87], v[86:87] op_sel:[0,0,1] op_sel_hi:[1,0,1]\n"
      "v_pk_fma_f32 v[106:107], v[106:107], v[86:87], v[86:87] op_sel:[0,0,1] op_sel_hi:[1,0,1]\n"
      "buffer_load_dwordx4 v[208:211], v20, s[16:19], s90 offen\n"
      "v_pk_fma_f32 v[108:109], v[108:109], v[86:87], v[86:87] op_sel:[0,0,1] op_sel_hi:[1,0,1]\n"
      "v_pk_fma_f32 v[110:111], v[110:111], v[86:87], v[86:87] op_sel:[0,0,1] op_sel_hi:[1,0,1]\n"
      "v_lshlrev_b32 v112, 16, v120\n"
      "v_and_b32 v113, s83, v120\n"
      "v_lshlrev_b32 v114, 16, v121\n"
      "v_and_b32 v115, s83, v121\n"
      "v_lshlrev_b32 v116, 16, v122\n"
      "v_and_b32 v117, s83, v122\n"
      "v_lshlrev_b32 v118, 16, v123\n"
      "v_and_b32 v119, s83, v123\n"
      "v_pk_fma_f32 v[112:113], v[112:113], v[86:87], v[86:87] op_sel:[0,0,1] op_sel_hi:[1,0,1]\n"
      "v_pk_fma_f32 v[114:115], v[114:115], v[86:87], v[86:87] op_sel:[0,0,1] op_sel_hi:[1,0,1]\n"
      "v_pk_fma_f32 v[116:117], v[116:117], v[86:87], v[86:87] op_sel:[0,0,1] op_sel_hi:[1,0,1]\n"
      "v_pk_fma_f32 v[118:119], v[118:119], v[86:87], v[86:87] op_sel:[0,0,1] op_sel_hi:[1,0,1]\n"
      "v_lshlrev_b32 v120, 16, v124\n"
      "v_and_b32 v121, s83, v124\n"
      "v_lshlrev_b32 v122, 16, v125\n"
      "v_and_b32 v123, s83, v125\n"
      "v_lshlrev_b32 v124, 16, v126\n"
      "v_and_b32 v125, s83, v126\n"
      "v_lshlrev_b32 v126, 16, v127\n"
      "v_and_b32 v127, s83, v127\n"
      "v_pk_fma_f32 v[120:121], v[120:121], v[86:87], v[86:87] op_sel:[0,0,1] op_sel_hi:[1,0,1]\n"
      "v_pk_fma_f32 v[122:123], v[122:123], v[86:87], v[86:87] op_sel:[0,0,1] op_sel_hi:[1,0,1]\n"
      "v_pk_fma_f32 v[124:125], v[124:125], v[86:87], v[86:87] op_sel:[0,0,1] op_sel_hi:[1,0,1]\n"
      "v_pk_fma_f32 v[126:127], v[126:127], v[86:87], v[86:87] op_sel:[0,0,1] op_sel_hi:[1,0,1]\n"
      "v_readfirstlane_b32 s86, v41\n"
      "s_mul_i32 s86, s86, 0x9e3779b1\n"
      "s_add_u32 s86, s86, 0x7f4a7c15\n"
      "s_waitcnt vmcnt(9)\n"
      "v_dot2_f32_bf16 v84, v144, s82, 0\n"
      "buffer_load_dwordx4 v[212:215], v21, s[16:19], s90 offen\n"
      "v_dot2_f32_bf16 v85, v144, v144, 0\n"
      "s_nop 0\n"
      "v_dot2c_f32_bf16 v84, s82, v145\n"
      "s_nop 0\n"
      "v_dot2c_f32_bf16 v85, v145, v145\n"
      "v_dot2c_f32_bf16 v84, s82, v146\n"
      "v_dot2c_f32_bf16 v85, v146, v146\n"
      "v_dot2c_f32_bf16 v84, s82, v147\n"
      "v_dot2c_f32_bf16 v85, v147, v147\n"
      "s_waitcnt vmcnt(9)\n"
      "v_dot2c_f32_bf16 v84, s82, v148\n"
      "v_dot2c_f32_bf16 v85, v148, v148\n"
      "v_dot2c_f32_bf16 v84, s82, v149\n"
      "v_dot2c_f32_bf16 v85, v149, v149\n"
      "v_dot2c_f32_bf16 v84, s82, v150\n"
      "v_dot2c_f32_bf16 v85, v150, v150\n"
      "v_dot2c_f32_bf16 v84, s82, v151\n"
      "v_dot2c_f32_bf16 v85, v151, v151\n"
      "s_waitcnt vmcnt(8)\n"
      "v_dot2c_f32_bf16 v84, s82, v152\n"
      "v_dot2c_f32_bf16 v85, v152, v152\n"
      "v_dot2c_f32_bf16 v84, s82, v153\n"
      "v_dot2c_f32_bf16 v85, v153, v153\n"
      "v_dot2c_f32_bf16 v84, s82, v154\n"
      "v_dot2c_f32_bf16 v85, v154, v154\n"
      "v_dot2c_f32_bf16 v84, s82, v155\n"
      "v_dot2c_f32_bf16 v85, v155, v155\n"
      "s_waitcnt vmcnt(7)\n"
      "v_dot2c_f32_bf16 v84, s82, v156\n"
      "v_dot2c_f32_bf16 v85, v156, v156\n"
      "v_dot2c_f32_bf16 v84, s82, v157\n"
      "v_dot2c_f32_bf16 v85, v157, v157\n"
      "v_dot2c_f32_bf16 v84, s82, v158\n"
      "v_dot2c_f32_bf16 v85, v158, v158\n"
      "buffer_load_dwordx4 v[216:219], v22, s[16:19], s90 offen\n"
      "v_dot2c_f32_bf16 v84, s82, v159\n"
      "v_dot2c_f32_bf16 v85, v159, v159\n"
      "v_lshlrev_b32 v128, 16, v144\n"
      "v_and_b32 v129, s83, v144\n"
      "v_add_f32_dpp v84, v84, v84 quad_perm:[1,0,3,2] row_mask:0xf bank_mask:0xf\n"
      "v_add_f32_dpp v85, v85, v85 quad_perm:[1,0,3,2] row_mask:0xf bank_mask:0xf\n"
      "v_lshlrev_b32 v130, 16, v145\n"
      "v_add_f32_dpp v84, v84, v84 quad_perm:[2,3,0,1] row_mask:0xf bank_mask:0xf\n"
      "v_add_f32_dpp v85, v85, v85 quad_perm:[2,3,0,1] row_mask:0xf bank_mask:0xf\n"
      "v_and_b32 v131, s83, v145\n"
      "v_add_f32_dpp v84, v84, v84 row_half_mirror row_mask:0xf bank_mask:0xf\n"
      "v_add_f32_dpp v85, v85, v85 row_half_mirror row_mask:0xf bank_mask:0xf\n"
      "v_lshlrev_b32 v132, 16, v146\n"
      "v_add_f32_dpp v84, v84, v84 row_mirror row_mask:0xf bank_mask:0xf\n"
      "v_add_f32_dpp v85, v85, v85 row_mirror row_mask:0xf bank_mask:0xf\n"
      "v_and_b32 v133, s83, v146\n"
      "v_mul_f32 v84, 0x3b000000, v84\n"
      "v_mul_f32 v85, 0x3b000000, v85\n"
      "v_fma_f32 v85, -v84, v84, v85\n"
      "v_max_f32 v85, 0, v85\n"
      "v_add_f32 v85, 0x3727c5ac, v85\n"
      "v_rsq_f32 v86, v85\n"
      "v_lshlrev_b32 v134, 16, v147\n"
      "v_mul_f32_e64 v87, -v84, v86\n"
      "v_and_b32 v135, s83, v147\n"
      "v_lshlrev_b32 v136, 16, v148\n"
      "v_and_b32 v137, s83, v148\n"
      "v_lshlrev_b32 v138, 16, v149\n"
      "v_and_b32 v139, s83, v149\n"
      "buffer_load_dwordx4 v[220:223], v23, s[16:19], s90 offen\n"
      "v_lshlrev_b32 v140, 16, v150\n"
      "v_and_b32 v141, s83, v150\n"
      "v_lshlrev_b32 v142, 16, v151\n"
      "v_and_b32 v143, s83, v151\n"
      "v_pk_fma_f32 v[128:129], v[128:129], v[86:87], v[86:87] op_sel:[0,0,1] op_sel_hi:[1,0,1]\n"
      "v_pk_fma_f32 v[130:131], v[130:131], v[86:87], v[86:87] op_sel:[0,0,1] op_sel_hi:[1,0,1]\n"
      "v_pk_fma_f32 v[132:133], v[132:133], v[86:87], v[86:87] op_sel:[0,0,1] op_sel_hi:[1,0,1]\n"
      "v_pk_fma_f32 v[134:135], v[134:135], v[86:87], v[86:87] op_sel:[0,0,1] op_sel_hi:[1,0,1]\n"
      "v_pk_fma_f32 v[136:137], v[136:137], v[86:87], v[86:87] op_sel:[0,0,1] op_sel_hi:[1,0,1]\n"
      "v_pk_fma_f32 v[138:139], v[138:139], v[86:87], v[86:87] op_sel:[0,0,1] op_sel_hi:[1,0,1]\n"
      "v_pk_fma_f32 v[140:141], v[140:141], v[86:87], v[86:87] op_sel:[0,0,1] op_sel_hi:[1,0,1]\n"
      "v_pk_fma_f32 v[142:143], v[142:143], v[86:87], v[86:87] op_sel:[0,0,1] op_sel_hi:[1,0,1]\n"
      "v_lshlrev_b32 v144, 16, v152\n"
      "v_and_b32 v145, s83, v152\n"
      "v_lshlrev_b32 v146, 16, v153\n"
      "v_and_b32 v147, s83, v153\n"
      "v_lshlrev_b32 v148, 16, v154\n"
      "v_and_b32 v149, s83, v154\n"
      "v_lshlrev_b32 v150, 16, v155\n"
      "v_and_b32 v151, s83, v155\n"
      "v_pk_fma_f32 v[144:145], v[144:145], v[86:87], v[86:87] op_sel:[0,0,1] op_sel_hi:[1,0,1]\n"
      "v_pk_fma_f32 v[146:147], v[146:147], v[86:87], v[86:87] op_sel:[0,0,1] op_sel_hi:[1,0,1]\n"
      "v_pk_fma_f32 v[148:149], v[148:149], v[86:87], v[86:87] op_sel:[0,0,1] op_sel_hi:[1,0,1]\n"
      "v_pk_fma_f32 v[150:151], v[150:151], v[86:87], v[86:87] op_sel:[0,0,1] op_sel_hi:[1,0,1]\n"
      "v_lshlrev_b32 v152, 16, v156\n"
      "v_and_b32 v153, s83, v156\n"
      "v_lshlrev_b32 v154, 16, v157\n"
      "v_and_b32 v155, s83, v157\n"
      "v_lshlrev_b32 v156, 16, v158\n"
      "buffer_load_dwordx4 v[240:243], v20, s[16:19], s91 offen\n"
      "v_and_b32 v157, s83, v158\n"
      "v_lshlrev_b32 v158, 16, v159\n"
      "v_and_b32 v159, s83, v159\n"
      "v_pk_fma_f32 v[152:153], v[152:153], v[86:87], v[86:87] op_sel:[0,0,1] op_sel_hi:[1,0,1]\n"
      "v_pk_fma_f32 v[154:155], v[154:155], v[86:87], v[86:87] op_sel:[0,0,1] op_sel_hi:[1,0,1]\n"
      "v_pk_fma_f32 v[156:157], v[156:157], v[86:87], v[86:87] op_sel:[0,0,1] op_sel_hi:[1,0,1]\n"
      "v_pk_fma_f32 v[158:159], v[158:159], v[86:87], v[86:87] op_sel:[0,0,1] op_sel_hi:[1,0,1]\n"
      "s_waitcnt vmcnt(9)\n"
      "v_dot2_f32_bf16 v84, v176, s82, 0\n"
      "v_dot2_f32_bf16 v85, v176, v176, 0\n"
      "s_nop 1\n"
      "v_dot2c_f32_bf16 v84, s82, v177\n"
      "v_dot2c_f32_bf16 v85, v177, v177\n"
      "v_dot2c_f32_bf16 v84, s82, v178\n"
      "v_dot2c_f32_bf16 v85, v178, v178\n"
      "v_dot2c_f32_bf16 v84, s82, v179\n"
      "v_dot2c_f32_bf16 v85, v179, v179\n"
      "buffer_load_dwordx4 v[244:247], v21, s[16:19], s91 offen\n"
      "buffer_load_dwordx4 v[248:251], v22, s[16:19], s91 offen\n"
      "s_waitcnt vmcnt(9)\n"
      "v_dot2c_f32_bf16 v84, s82, v180\n"
      "v_dot2c_f32_bf16 v85, v180, v180\n"
      "v_dot2c_f32_bf16 v84, s82, v181\n"
      "v_dot2c_f32_bf16 v85, v181, v181\n"
      "v_dot2c_f32_bf16 v84, s82, v182\n"
      "v_dot2c_f32_bf16 v85, v182, v182\n"
      "v_dot2c_f32_bf16 v84, s82, v183\n"
      "v_dot2c_f32_bf16 v85, v183, v183\n"
      "buffer_load_dwordx4 v[252:255], v23, s[16:19], s91 offen\n"
      "s_waitcnt vmcnt(9)\n"
      "v_dot2c_f32_bf16 v84, s82, v184\n"
      "v_dot2c_f32_bf16 v85, v184, v184\n"
      "v_dot2c_f32_bf16 v84, s82, v185\n"
      "v_dot2c_f32_bf16 v85, v185, v185\n"
      "v_dot2c_f32_bf16 v84, s82, v186\n"
      "v_dot2c_f32_bf16 v85, v186, v186\n"
      "v_dot2c_f32_bf16 v84, s82, v187\n"
      "v_dot2c_f32_bf16 v85, v187, v187\n"
      "s_add_u32 m0, s79, 131072\n"
      "s_nop 0\n"
      "buffer_load_dwordx4 v90, s[24:27], s47 offen lds\n"
      "s_waitcnt vmcnt(9)\n"
      "v_dot2c_f32_bf16 v84, s82, v188\n"
      "v_dot2c_f32_bf16 v85, v188, v188\n"
      "v_dot2c_f32_bf16 v84, s82, v189\n"
      "v_dot2c_f32_bf16 v85, v189, v189\n"
      "v_dot2c_f32_bf16 v84, s82, v190\n"
      "v_dot2c_f32_bf16 v85, v190, v190\n"
      "v_dot2c_f32_bf16 v84, s82, v191\n"
      "v_dot2c_f32_bf16 v85, v191, v191\n"
      "v_lshlrev_b32 v160, 16, v176\n"
      "v_and_b32 v161, s83, v176\n"
      "v_add_f32_dpp v84, v84, v84 quad_perm:[1,0,3,2] row_mask:0xf bank_mask:0xf\n"
      "v_add_f32_dpp v85, v85, v85 quad_perm:[1,0,3,2] row_mask:0xf bank_mask:0xf\n"
      "v_lshlrev_b32 v162, 16, v177\n"
      "v_add_f32_dpp v84, v84, v84 quad_perm:[2,3,0,1] row_mask:0xf bank_mask:0xf\n"
      "v_add_f32_dpp v85, v85, v85 quad_perm:[2,3,0,1] row_mask:0xf bank_mask:0xf\n"
      "v_and_b32 v163, s83, v177\n"
      "v_add_f32_dpp v84, v84, v84 row_half_mirror row_mask:0xf bank_mask:0xf\n"
      "v_add_f32_dpp v85, v85, v85 row_half_mirror row_mask:0xf bank_mask:0xf\n"
      "s_add_u32 m0, s79, 132096\n"
      "s_nop 0\n"
      "buffer_load_dwordx4 v91, s[24:27], s47 offen lds\n"
      "v_lshlrev_b32 v164, 16, v178\n"
      "v_add_f32_dpp v84, v84, v84 row_mirror row_mask:0xf bank_mask:0xf\n"
      "v_add_f32_dpp v85, v85, v85 row_mirror row_mask:0xf bank_mask:0xf\n"
      "v_and_b32 v165, s83, v178\n"
      "v_mul_f32 v84, 0x3b000000, v84\n"
      "v_mul_f32 v85, 0x3b000000, v85\n"
      "v_fma_f32 v85, -v84, v84, v85\n"
      "v_max_f32 v85, 0, v85\n"
      "v_add_f32 v85, 0x3727c5ac, v85\n"
      "v_rsq_f32 v86, v85\n"
      "v_lshlrev_b32 v166, 16, v179\n"
      "v_mul_f32_e64 v87, -v84, v86\n"
      "v_and_b32 v167, s83, v179\n"
      "v_lshlrev_b32 v168, 16, v180\n"
      "v_and_b32 v169, s83, v180\n"
      "v_lshlrev_b32 v170, 16, v181\n"
      "v_and_b32 v171, s83, v181\n"
      "v_lshlrev_b32 v172, 16, v182\n"
      "v_and_b32 v173, s83, v182\n"
      "v_lshlrev_b32 v174, 16, v183\n"
      "v_and_b32 v175, s83, v183\n"
      "v_pk_fma_f32 v[160:161], v[160:161], v[86:87], v[86:87] op_sel:[0,0,1] op_sel_hi:[1,0,1]\n"
      "v_pk_fma_f32 v[162:163], v[162:163], v[86:87], v[86:87] op_sel:[0,0,1] op_sel_hi:[1,0,1]\n"
      "v_pk_fma_f32 v[164:165], v[164:165], v[86:87], v[86:87] op_sel:[0,0,1] op_sel_hi:[1,0,1]\n"
      "v_pk_fma_f32 v[166:167], v[166:167], v[86:87], v[86:87] op_sel:[0,0,1] op_sel_hi:[1,0,1]\n"
      "v_pk_fma_f32 v[168:169], v[168:169], v[86:87], v[86:87] op_sel:[0,0,1] op_sel_hi:[1,0,1]\n"
      "v_pk_fma_f32 v[170:171], v[170:171], v[86:87], v[86:87] op_sel:[0,0,1] op_sel_hi:[1,0,1]\n"
      "v_pk_fma_f32 v[172:173], v[172:173], v[86:87], v[86:87] op_sel:[0,0,1] op_sel_hi:[1,0,1]\n"
      "s_add_u32 m0, s79, 133120\n"
      "s_nop 0\n"
      "buffer_load_dwordx4 v92, s[24:27], s47 offen lds\n"
      "v_pk_fma_f32 v[174:175], v[174:175], v[86:87], v[86:87] op_sel:[0,0,1] op_sel_hi:[1,0,1]\n"
      "v_lshlrev_b32 v176, 16, v184\n"
      "v_and_b32 v177, s83, v184\n"
      "v_lshlrev_b32 v178, 16, v185\n"
      "v_and_b32 v179, s83, v185\n"
      "v_lshlrev_b32 v180, 16, v186\n"
      "v_and_b32 v181, s83, v186\n"
      "v_lshlrev_b32 v182, 16, v187\n"
      "v_and_b32 v183, s83, v187\n"
      "v_pk_fma_f32 v[176:177], v[176:177], v[86:87], v[86:87] op_sel:[0,0,1] op_sel_hi:[1,0,1]\n"
      "v_pk_fma_f32 v[178:179], v[178:179], v[86:87], v[86:87] op_sel:[0,0,1] op_sel_hi:[1,0,1]\n"
      "v_pk_fma_f32 v[180:181], v[180:181], v[86:87], v[86:87] op_sel:[0,0,1] op_sel_hi:[1,0,1]\n"
      "v_pk_fma_f32 v[182:183], v[182:183], v[86:87], v[86:87] op_sel:[0,0,1] op_sel_hi:[1,0,1]\n"
      "v_lshlrev_b32 v184, 16, v188\n"
      "v_and_b32 v185, s83, v188\n"
      "v_lshlrev_b32 v186, 16, v189\n"
      "v_and_b32 v187, s83, v189\n"
      "v_lshlrev_b32 v188, 16, v190\n"
      "v_and_b32 v189, s83, v190\n"
      "v_lshlrev_b32 v190, 16, v191\n"
      "v_and_b32 v191, s83, v191\n"
      "v_pk_fma_f32 v[184:185], v[184:185], v[86:87], v[86:87] op_sel:[0,0,1] op_sel_hi:[1,0,1]\n"
      "v_pk_fma_f32 v[186:187], v[186:187], v[86:87], v[86:87] op_sel:[0,0,1] op_sel_hi:[1,0,1]\n"
      "v_pk_fma_f32 v[188:189], v[188:189], v[86:87], v[86:87] op_sel:[0,0,1] op_sel_hi:[1,0,1]\n"
      "v_pk_fma_f32 v[190:191], v[190:191], v[86:87], v[86:87] op_sel:[0,0,1] op_sel_hi:[1,0,1]\n"
      "s_waitcnt vmcnt(10)\n"
      "v_dot2_f32_bf16 v84, v208, s82, 0\n"
      "v_dot2_f32_bf16 v85, v208, v208, 0\n"
      "s_nop 1\n"
      "v_dot2c_f32_bf16 v84, s82, v209\n"
      "s_add_u32 m0, s79, 143360\n"
      "s_nop 0\n"
      "buffer_load_dwordx4 v90, s[28:31], s48 offen lds\n"
      "v_dot2c_f32_bf16 v85, v209, v209\n"
      "v_dot2c_f32_bf16 v84, s82, v210\n"
      "v_dot2c_f32_bf16 v85, v210, v210\n"
      "v_dot2c_f32_bf16 v84, s82, v211\n"
      "v_dot2c_f32_bf16 v85, v211, v211\n"
      "s_waitcnt vmcnt(10)\n"
      "v_dot2c_f32_bf16 v84, s82, v212\n"
      "v_dot2c_f32_bf16 v85, v212, v212\n"
      "v_dot2c_f32_bf16 v84, s82, v213\n"
      "v_dot2c_f32_bf16 v85, v213, v213\n"
      "v_dot2c_f32_bf16 v84, s82, v214\n"
      "v_dot2c_f32_bf16 v85, v214, v214\n"
      "v_dot2c_f32_bf16 v84, s82, v215\n"
      "v_dot2c_f32_bf16 v85, v215, v215\n"
      "s_waitcnt vmcnt(9)\n"
      "v_dot2c_f32_bf16 v84, s82, v216\n"
      "v_dot2c_f32_bf16 v85, v216, v216\n"
      "v_dot2c_f32_bf16 v84, s82, v217\n"
      "v_dot2c_f32_bf16 v85, v217, v217\n"
      "v_dot2c_f32_bf16 v84, s82, v218\n"
      "v_dot2c_f32_bf16 v85, v218, v218\n"
      "v_dot2c_f32_bf16 v84, s82, v219\n"
      "v_dot2c_f32_bf16 v85, v219, v219\n"
      "s_add_u32 m0, s79, 144384\n"
      "s_nop 0\n"
      "buffer_load_dwordx4 v91, s[28:31], s48 offen lds\n"
      "s_waitcnt vmcnt(9)\n"
      "v_dot2c_f32_bf16 v84, s82, v220\n"
      "v_dot2c_f32_bf16 v85, v220, v220\n"
      "v_dot2c_f32_bf16 v84, s82, v221\n"
      "v_dot2c_f32_bf16 v85, v221, v221\n"
      "v_dot2c_f32_bf16 v84, s82, v222\n"
      "v_dot2c_f32_bf16 v85, v222, v222\n"
      "v_dot2c_f32_bf16 v84, s82, v223\n"
      "v_dot2c_f32_bf16 v85, v223, v223\n"
      "v_lshlrev_b32 v192, 16, v208\n"
      "v_and_b32 v193, s83, v208\n"
      "v_add_f32_dpp v84, v84, v84 quad_perm:[1,0,3,2] row_mask:0xf bank_mask:0xf\n"
      "v_add_f32_dpp v85, v85, v85 quad_perm:[1,0,3,2] row_mask:0xf bank_mask:0xf\n"
      "v_lshlrev_b32 v194, 16, v209\n"
      "v_add_f32_dpp v84, v84, v84 quad_perm:[2,3,0,1] row_mask:0xf bank_mask:0xf\n"
      "v_add_f32_dpp v85, v85, v85 quad_perm:[2,3,0,1] row_mask:0xf bank_mask:0xf\n"
      "v_and_b32 v195, s83, v209\n"
      "v_add_f32_dpp v84, v84, v84 row_half_mirror row_mask:0xf bank_mask:0xf\n"
      "v_add_f32_dpp v85, v85, v85 row_half_mirror row_mask:0xf bank_mask:0xf\n"
      "s_add_u32 m0, s79, 145408\n"
      "s_nop 0\n"
      "buffer_load_dwordx4 v92, s[28:31], s48 offen lds\n"
      "v_lshlrev_b32 v196, 16, v210\n"
      "v_add_f32_dpp v84, v84, v84 row_mirror row_mask:0xf bank_mask:0xf\n"
      "v_add_f32_dpp v85, v85, v85 row_mirror row_mask:0xf bank_mask:0xf\n"
      "v_and_b32 v197, s83, v210\n"
      "v_mul_f32 v84, 0x3b000000, v84\n"
      "v_mul_f32 v85, 0x3b000000, v85\n"
      "v_fma_f32 v85, -v84, v84, v85\n"
      "v_max_f32 v85, 0, v85\n"
      "v_add_f32 v85, 0x3727c5ac, v85\n"
      "v_rsq_f32 v86, v85\n"
      "v_lshlrev_b32 v198, 16, v211\n"
      "v_mul_f32_e64 v87, -v84, v86\n"
      "v_and_b32 v199, s83, v211\n"
      "v_lshlrev_b32 v200, 16, v212\n"
      "v_and_b32 v201, s83, v212\n"
      "v_lshlrev_b32 v202, 16, v213\n"
      "v_and_b32 v203, s83, v213\n"
      "v_lshlrev_b32 v204, 16, v214\n"
      "v_and_b32 v205, s83, v214\n"
      "v_lshlrev_b32 v206, 16, v215\n"
      "v_and_b32 v207, s83, v215\n"
      "v_pk_fma_f32 v[192:193], v[192:193], v[86:87], v[86:87] op_sel:[0,0,1] op_sel_hi:[1,0,1]\n"
      "v_pk_fma_f32 v[194:195], v[194:195], v[86:87], v[86:87] op_sel:[0,0,1] op_sel_hi:[1,0,1]\n"
      "v_pk_fma_f32 v[196:197], v[196:197], v[86:87], v[86:87] op_sel:[0,0,1] op_sel_hi:[1,0,1]\n"
      "v_pk_fma_f32 v[198:199], v[198:199], v[86:87], v[86:87] op_sel:[0,0,1] op_sel_hi:[1,0,1]\n"
      "v_pk_fma_f32 v[200:201], v[200:201], v[86:87], v[86:87] op_sel:[0,0,1] op_sel_hi:[1,0,1]\n"
      "v_pk_fma_f32 v[202:203], v[202:203], v[86:87], v[86:87] op_sel:[0,0,1] op_sel_hi:[1,0,1]\n"
      "v_pk_fma_f32 v[204:205], v[204:205], v[86:87], v[86:87] op_sel:[0,0,1] op_sel_hi:[1,0,1]\n"
      "s_add_u32 s95, s85, 0\n"
      "s_and_b32 s95, s95, 15\n"
      "s_add_u32 m0, s79, 81920\n"
      "s_mul_i32 s95, s95, 98304\n"
      "buffer_load_dwordx4 v6, s[20:23], s95 offen offset:0 lds\n"
      "v_pk_fma_f32 v[206:207], v[206:207], v[86:87], v[86:87] op_sel:[0,0,1] op_sel_hi:[1,0,1]\n"
      "v_lshlrev_b32 v208, 16, v216\n"
      "v_and_b32 v209, s83, v216\n"
      "v_lshlrev_b32 v210, 16, v217\n"
      "v_and_b32 v211, s83, v217\n"
      "v_lshlrev_b32 v212, 16, v218\n"
      "v_and_b32 v213, s83, v218\n"
      "v_lshlrev_b32 v214, 16, v219\n"
      "v_and_b32 v215, s83, v219\n"
      "v_pk_fma_f32 v[208:209], v[208:209], v[86:87], v[86:87] op_sel:[0,0,1] op_sel_hi:[1,0,1]\n"
      "v_pk_fma_f32 v[210:211], v[210:211], v[86:87], v[86:87] op_sel:[0,0,1] op_sel_hi:[1,0,1]\n"
      "v_pk_fma_f32 v[212:213], v[212:213], v[86:87], v[86:87] op_sel:[0,0,1] op_sel_hi:[1,0,1]\n"
      "v_pk_fma_f32 v[214:215], v[214:215], v[86:87], v[86:87] op_sel:[0,0,1] op_sel_hi:[1,0,1]\n"
      "v_lshlrev_b32 v216, 16, v220\n"
      "v_and_b32 v217, s83, v220\n"
      "v_lshlrev_b32 v218, 16, v221\n"
      "v_and_b32 v219, s83, v221\n"
      "v_lshlrev_b32 v220, 16, v222\n"
      "v_and_b32 v221, s83, v222\n"
      "v_lshlrev_b32 v222, 16, v223\n"
      "v_and_b32 v223, s83, v223\n"
      "v_pk_fma_f32 v[216:217], v[216:217], v[86:87], v[86:87] op_sel:[0,0,1] op_sel_hi:[1,0,1]\n"
      "v_pk_fma_f32 v[218:219], v[218:219], v[86:87], v[86:87] op_sel:[0,0,1] op_sel_hi:[1,0,1]\n"
      "v_pk_fma_f32 v[220:221], v[220:221], v[86:87], v[86:87] op_sel:[0,0,1] op_sel_hi:[1,0,1]\n"
      "v_pk_fma_f32 v[222:223], v[222:223], v[86:87], v[86:87] op_sel:[0,0,1] op_sel_hi:[1,0,1]\n"
      "buffer_load_dwordx4 v6, s[20:23], s95 offen offset:1024 lds\n"
      "s_mov_b32 s92, 65536\n"
      "buffer_load_dwordx4 v[48:51], v13, s[4:7], s92 offen sc1\n"
      "s_mov_b32 s92, 77824\n"
      "buffer_load_dwordx4 v[52:55], v13, s[4:7], s92 offen sc1\n"
      "s_mov_b32 s92, 90112\n"
      "buffer_load_dwordx4 v[56:59], v13, s[4:7], s92 offen sc1\n"
      "s_mov_b32 s92, 102400\n"
      "buffer_load_dwordx4 v[60:63], v13, s[4:7], s92 offen sc1\n"
      "s_waitcnt vmcnt(15)\n"
      "v_dot2_f32_bf16 v84, v240, s82, 0\n"
      "v_dot2_f32_bf16 v85, v240, v240, 0\n"
      "s_nop 1\n"
      "v_dot2c_f32_bf16 v84, s82, v241\n"
      "v_dot2c_f32_bf16 v85, v241, v241\n"
      "v_dot2c_f32_bf16 v84, s82, v242\n"
      "v_dot2c_f32_bf16 v85, v242, v242\n"
      "v_dot2c_f32_bf16 v84, s82, v243\n"
      "v_dot2c_f32_bf16 v85, v243, v243\n"
      "s_waitcnt vmcnt(14)\n"
      "v_dot2c_f32_bf16 v84, s82, v244\n"
      "v_dot2c_f32_bf16 v85, v244, v244\n"
      "v_dot2c_f32_bf16 v84, s82, v245\n"
      "v_dot2c_f32_bf16 v85, v245, v245\n"
      "v_dot2c_f32_bf16 v84, s82, v246\n"
      "v_dot2c_f32_bf16 v85, v246, v246\n"
      "v_dot2c_f32_bf16 v84, s82, v247\n"
      "v_dot2c_f32_bf16 v85, v247, v247\n"
      "s_waitcnt vmcnt(13)\n"
      "v_dot2c_f32_bf16 v84, s82, v248\n"
      "v_dot2c_f32_bf16 v85, v248, v248\n"
      "v_dot2c_f32_bf16 v84, s82, v249\n"
      "v_dot2c_f32_bf16 v85, v249, v249\n"
      "v_dot2c_f32_bf16 v84, s82, v250\n"
      "v_dot2c_f32_bf16 v85, v250, v250\n"
      "v_dot2c_f32_bf16 v84, s82, v251\n"
      "v_dot2c_f32_bf16 v85, v251, v251\n"
      "s_waitcnt vmcnt(12)\n"
      "v_dot2c_f32_bf16 v84, s82, v252\n"
      "v_dot2c_f32_bf16 v85, v252, v252\n"
      "v_dot2c_f32_bf16 v84, s82, v253\n"
      "v_dot2c_f32_bf16 v85, v253, v253\n"
      "v_dot2c_f32_bf16 v84, s82, v254\n"
      "v_dot2c_f32_bf16 v85, v254, v254\n"
      "v_dot2c_f32_bf16 v84, s82, v255\n"
      "v_dot2c_f32_bf16 v85, v255, v255\n"
      "v_lshlrev_b32 v224, 16, v240\n"
      "v_and_b32 v225, s83, v240\n"
      "v_add_f32_dpp v84, v84, v84 quad_perm:[1,0,3,2] row_mask:0xf bank_mask:0xf\n"
      "v_add_f32_dpp v85, v85, v85 quad_perm:[1,0,3,2] row_mask:0xf bank_mask:0xf\n"
      "v_lshlrev_b32 v226, 16, v241\n"
      "v_add_f32_dpp v84, v84, v84 quad_perm:[2,3,0,1] row_mask:0xf bank_mask:0xf\n"
      "v_add_f32_dpp v85, v85, v85 quad_perm:[2,3,0,1] row_mask:0xf bank_mask:0xf\n"
      "v_and_b32 v227, s83, v241\n"
      "v_add_f32_dpp v84, v84, v84 row_half_mirror row_mask:0xf bank_mask:0xf\n"
      "v_add_f32_dpp v85, v85, v85 row_half_mirror row_mask:0xf bank_mask:0xf\n"
      "v_lshlrev_b32 v228, 16, v242\n"
      "v_add_f32_dpp v84, v84, v84 row_mirror row_mask:0xf bank_mask:0xf\n"
      "v_add_f32_dpp v85, v85, v85 row_mirror row_mask:0xf bank_mask:0xf\n"
      "v_and_b32 v229, s83, v242\n"
      "v_mul_f32 v84, 0x3b000000, v84\n"
      "v_mul_f32 v85, 0x3b000000, v85\n"
      "v_fma_f32 v85, -v84, v84, v85\n"
      "v_max_f32 v85, 0, v85\n"
      "v_add_f32 v85, 0x3727c5ac, v85\n"
      "v_rsq_f32 v86, v85\n"
      "v_lshlrev_b32 v230, 16, v243\n"
      "v_mul_f32_e64 v87, -v84, v86\n"
      "v_and_b32 v231, s83, v243\n"
      "v_lshlrev_b32 v232, 16, v244\n"
      "v_and_b32 v233, s83, v244\n"
      "v_lshlrev_b32 v234, 16, v245\n"
      "v_and_b32 v235, s83, v245\n"
      "v_lshlrev_b32 v236, 16, v246\n"
      "v_and_b32 v237, s83, v246\n"
      "v_lshlrev_b32 v238, 16, v247\n"
      "v_and_b32 v239, s83, v247\n"
      "v_pk_fma_f32 v[224:225], v[224:225], v[86:87], v[86:87] op_sel:[0,0,1] op_sel_hi:[1,0,1]\n"
      "v_pk_fma_f32 v[226:227], v[226:227], v[86:87], v[86:87] op_sel:[0,0,1] op_sel_hi:[1,0,1]\n"
      "v_pk_fma_f32 v[228:229], v[228:229], v[86:87], v[86:87] op_sel:[0,0,1] op_sel_hi:[1,0,1]\n"
      "v_pk_fma_f32 v[230:231], v[230:231], v[86:87], v[86:87] op_sel:[0,0,1] op_sel_hi:[1,0,1]\n"
      "v_pk_fma_f32 v[232:233], v[232:233], v[86:87], v[86:87] op_sel:[0,0,1] op_sel_hi:[1,0,1]\n"
      "v_pk_fma_f32 v[234:235], v[234:235], v[86:87], v[86:87] op_sel:[0,0,1] op_sel_hi:[1,0,1]\n"
      "v_pk_fma_f32 v[236:237], v[236:237], v[86:87], v[86:87] op_sel:[0,0,1] op_sel_hi:[1,0,1]\n"
      "v_pk_fma_f32 v[238:239], v[238:239], v[86:87], v[86:87] op_sel:[0,0,1] op_sel_hi:[1,0,1]\n"
      "v_lshlrev_b32 v240, 16, v248\n"
      "v_and_b32 v241, s83, v248\n"
      "v_lshlrev_b32 v242, 16, v249\n"
      "v_and_b32 v243, s83, v249\n"
      "v_lshlrev_b32 v244, 16, v250\n"
      "v_and_b32 v245, s83, v250\n"
      "v_lshlrev_b32 v246, 16, v251\n"
      "v_and_b32 v247, s83, v251\n"
      "v_pk_fma_f32 v[240:241], v[240:241], v[86:87], v[86:87] op_sel:[0,0,1] op_sel_hi:[1,0,1]\n"
      "v_pk_fma_f32 v[242:243], v[242:243], v[86:87], v[86:87] op_sel:[0,0,1] op_sel_hi:[1,0,1]\n"
      "v_pk_fma_f32 v[244:245], v[244:245], v[86:87], v[86:87] op_sel:[0,0,1] op_sel_hi:[1,0,1]\n"
      "v_pk_fma_f32 v[246:247], v[246:247], v[86:87], v[86:87] op_sel:[0,0,1] op_sel_hi:[1,0,1]\n"
      "v_lshlrev_b32 v248, 16, v252\n"
      "v_and_b32 v249, s83, v252\n"
      "v_lshlrev_b32 v250, 16, v253\n"
      "v_and_b32 v251, s83, v253\n"
      "v_lshlrev_b32 v252, 16, v254\n"
      "v_and_b32 v253, s83, v254\n"
      "v_lshlrev_b32 v254, 16, v255\n"
      "v_and_b32 v255, s83, v255\n"
      "v_pk_fma_f32 v[248:249], v[248:249], v[86:87], v[86:87] op_sel:[0,0,1] op_sel_hi:[1,0,1]\n"
      "v_pk_fma_f32 v[250:251], v[250:251], v[86:87], v[86:87] op_sel:[0,0,1] op_sel_hi:[1,0,1]\n"
      "v_pk_fma_f32 v[252:253], v[252:253], v[86:87], v[86:87] op_sel:[0,0,1] op_sel_hi:[1,0,1]\n"
      "v_pk_fma_f32 v[254:255], v[254:255], v[86:87], v[86:87] op_sel:[0,0,1] op_sel_hi:[1,0,1]\n"
      "buffer_load_dwordx4 v6, s[20:23], s95 offen offset:2048 lds\n"
      "s_add_u32 s95, s85, 1\n"
      "s_and_b32 s95, s95, 15\n"
      "s_add_u32 m0, s79, 94208\n"
      "s_mul_i32 s95, s95, 98304\n"
      "buffer_load_dwordx4 v6, s[20:23], s95 offen offset:0 lds\n"
      "s_waitcnt vmcnt(5)\n"
      "v_xor_b32 v64, s86, v49\n"
      "buffer_load_dwordx4 v6, s[20:23], s95 offen offset:1024 lds\n"
      "v_bitop3_b32 v64, v64, v51, s86 bitop3:0xf6\n"
      "s_waitcnt vmcnt(5)\n"
      "v_bitop3_b32 v64, v64, v53, s86 bitop3:0xf6\n"
      "buffer_load_dwordx4 v6, s[20:23], s95 offen offset:2048 lds\n"
      "v_bitop3_b32 v64, v64, v55, s86 bitop3:0xf6\n"
      "s_waitcnt vmcnt(5)\n"
      "v_bitop3_b32 v64, v64, v57, s86 bitop3:0xf6\n"
      "s_add_u32 s95, s85, 2\n"
      "s_and_b32 s95, s95, 15\n"
      "s_add_u32 m0, s79, 106496\n"
      "s_mul_i32 s95, s95, 98304\n"
      "buffer_load_dwordx4 v6, s[20:23], s95 offen offset:0 lds\n"
      "v_bitop3_b32 v64, v64, v59, s86 bitop3:0xf6\n"
      "s_waitcnt vmcnt(5)\n"
      "v_bitop3_b32 v64, v64, v61, s86 bitop3:0xf6\n"
      "buffer_load_dwordx4 v6, s[20:23], s95 offen offset:1024 lds\n"
      "v_bitop3_b32 v64, v64, v63, s86 bitop3:0xf6\n"
      "v_cmp_ne_u32 vcc, 0, v64\n"
      "s_cbranch_vccnz .Lretry_9\n"
      ".Lback_10:\n"
      "v_lshlrev_b32 v64, 16, v48\n"
      "v_and_b32 v66, s83, v48\n"
      "v_lshlrev_b32 v65, 16, v50\n"
      "v_and_b32 v67, s83, v50\n"
      "v_lshlrev_b32 v68, 16, v52\n"
      "v_and_b32 v70, s83, v52\n"
      "v_lshlrev_b32 v69, 16, v54\n"
      "v_and_b32 v71, s83, v54\n"
      "v_pk_add_f32 v[64:65], v[64:65], v[68:69]\n"
      "v_pk_add_f32 v[66:67], v[66:67], v[70:71]\n"
      "buffer_load_dwordx4 v6, s[20:23], s95 offen offset:2048 lds\n"
      "v_lshlrev_b32 v68, 16, v56\n"
      "v_and_b32 v70, s83, v56\n"
      "v_lshlrev_b32 v69, 16, v58\n"
      "v_and_b32 v71, s83, v58\n"
      "v_pk_add_f32 v[64:65], v[64:65], v[68:69]\n"
      "v_pk_add_f32 v[66:67], v[66:67], v[70:71]\n"
      "v_lshlrev_b32 v68, 16, v60\n"
      "v_and_b32 v70, s83, v60\n"
      "v_lshlrev_b32 v69, 16, v62\n"
      "v_and_b32 v71, s83, v62\n"
      "v_pk_add_f32 v[64:65], v[64:65], v[68:69]\n"
      "v_pk_add_f32 v[66:67], v[66:67], v[70:71]\n"
      "v_cvt_pk_bf16_f32 v68, v64, v65\n"
      "s_add_u32 s95, s85, 3\n"
      "s_and_b32 s95, s95, 15\n"
      "s_add_u32 m0, s79, 118784\n"
      "s_mul_i32 s95, s95, 98304\n"
      "buffer_load_dwordx4 v6, s[20:23], s95 offen offset:0 lds\n"
      "v_cvt_pk_bf16_f32 v69, v66, v67\n"
      "v_lshlrev_b32 v64, 16, v68\n"
      "v_and_b32 v65, s83, v68\n"
      "v_lshlrev_b32 v66, 16, v69\n"
      "v_and_b32 v67, s83, v69\n"
      "v_add_f32 v64, s80, v64\n"
      "v_add_f32 v65, s80, v65\n"
      "v_add_f32 v66, s80, v66\n"
      "v_add_f32 v67, s80, v67\n"
      "v_cvt_pk_bf16_f32 v68, v64, v65\n"
      "v_cvt_pk_bf16_f32 v69, v66, v67\n"
      "v_lshlrev_b32 v64, 16, v68\n"
      "v_and_b32 v65, s83, v68\n"
      "v_lshlrev_b32 v66, 16, v69\n"
      "v_and_b32 v67, s83, v69\n"
      "buffer_load_dwordx4 v6, s[20:23], s95 offen offset:1024 lds\n"
      "ds_write_b64 v80, v[64:65]\n"
      "ds_write_b64 v80, v[66:67] offset:32\n"
      "s_mov_b32 s87, 0\n"
      "s_lshl_b32 s88, s87, 10\n"
      "v_add_u32 v82, s88, v81\n"
      "s_add_u32 s87, s87, 1\n"
      "s_cmp_eq_u32 s87, 5\n"
      "s_cselect_b32 s87, 0, s87\n"
      "s_lshl_b32 s88, s87, 10\n"
      "v_add_u32 v84, s88, v81\n"
      "s_add_u32 s87, s87, 1\n"
      "s_cmp_eq_u32 s87, 5\n"
      "s_cselect_b32 s87, 0, s87\n"
      "s_lshl_b32 s88, s87, 10\n"
      "v_add_u32 v85, s88, v81\n"
      "s_add_u32 s87, s87, 1\n"
      "s_cmp_eq_u32 s87, 5\n"
      "s_cselect_b32 s87, 0, s87\n"
      "s_lshl_b32 s88, s87, 10\n"
      "v_add_u32 v86, s88, v81\n"
      "buffer_load_dwordx4 v6, s[20:23], s95 offen offset:2048 lds\n"
      "s_add_u32 s87, s87, 1\n"
      "s_cmp_eq_u32 s87, 5\n"
      "s_cselect_b32 s87, 0, s87\n"
      "s_lshl_b32 s88, s87, 10\n"
      "v_add_u32 v87, s88, v81\n"
      "s_waitcnt lgkmcnt(0)\n"
      "s_barrier\n"
      "v_accvgpr_read_b32 v15, a246\n"
      "buffer_load_dword v20, v15, s[12:15], s0 offen\n"
      "ds_read_b128 v[32:35], v83 offset:0\n"
      "ds_read_b128 v[36:39], v83 offset:1024\n"
      "ds_read_b128 v[40:43], v83 offset:2048\n"
      "ds_read_b128 v[44:47], v83 offset:3072\n"
      "ds_read_b128 v[48:51], v83 offset:256\n"
      "ds_read_b128 v[52:55], v83 offset:1280\n"
      "ds_read_b128 v[56:59], v83 offset:2304\n"
      "ds_read_b128 v[60:63], v83 offset:3328\n"
      "s_waitcnt lgkmcnt(5)\n"
      "v_pk_fma_f32 v[96:97], v[96:97], v[40:41], v[32:33]\n"
      "v_pk_fma_f32 v[98:99], v[98:99], v[42:43], v[34:35]\n"
      "s_waitcnt lgkmcnt(4)\n"
      "v_pk_fma_f32 v[100:101], v[100:101], v[44:45], v[36:37]\n"
      "v_pk_fma_f32 v[102:103], v[102:103], v[46:47], v[38:39]\n"
      "v_cvt_pk_bf16_f32 v64, v96, v97\n"
      "v_cvt_pk_bf16_f32 v65, v98, v99\n"
      "v_cvt_pk_bf16_f32 v66, v100, v101\n"
      "v_cvt_pk_bf16_f32 v67, v102, v103\n"
      "ds_write_b128 v82, v[64:67] offset:0\n"
      "v_pk_fma_f32 v[128:129], v[128:129], v[40:41], v[32:33]\n"
      "v_pk_fma_f32 v[130:131], v[130:131], v[42:43], v[34:35]\n"
      "v_pk_fma_f32 v[132:133], v[132:133], v[44:45], v[36:37]\n"
      "v_pk_fma_f32 v[134:135], v[134:135], v[46:47], v[38:39]\n"
      "v_cvt_pk_bf16_f32 v68, v128, v129\n"
      "v_cvt_pk_bf16_f32 v69, v130, v131\n"
      "v_cvt_pk_bf16_f32 v70, v132, v133\n"
      "v_cvt_pk_bf16_f32 v71, v134, v135\n"
      "ds_write_b128 v84, v[68:71] offset:0\n"
      "v_pk_fma_f32 v[160:161], v[160:161], v[40:41], v[32:33]\n"
      "v_pk_fma_f32 v[162:163], v[162:163], v[42:43], v[34:35]\n"
      "v_pk_fma_f32 v[164:165], v[164:165], v[44:45], v[36:37]\n"
      "v_pk_fma_f32 v[166:167], v[166:167], v[46:47], v[38:39]\n"
      "v_cvt_pk_bf16_f32 v72, v160, v161\n"
      "v_cvt_pk_bf16_f32 v73, v162, v163\n"
      "v_cvt_pk_bf16_f32 v74, v164, v165\n"
      "v_cvt_pk_bf16_f32 v75, v166, v167\n"
      "ds_write_b128 v85, v[72:75] offset:0\n"
      "v_pk_fma_f32 v[192:193], v[192:193], v[40:41], v[32:33]\n"
      "v_pk_fma_f32 v[194:195], v[194:195], v[42:43], v[34:35]\n"
      "v_pk_fma_f32 v[196:197], v[196:197], v[44:45], v[36:37]\n"
      "v_pk_fma_f32 v[198:199], v[198:199], v[46:47], v[38:39]\n"
      "v_cvt_pk_bf16_f32 v76, v192, v193\n"
      "v_cvt_pk_bf16_f32 v77, v194, v195\n"
      "v_cvt_pk_bf16_f32 v78, v196, v197\n"
      "v_cvt_pk_bf16_f32 v79, v198, v199\n"
      "ds_write_b128 v86, v[76:79] offset:0\n"
      "v_pk_fma_f32 v[224:225], v[224:225], v[40:41], v[32:33]\n"
      "v_pk_fma_f32 v[226:227], v[226:227], v[42:43], v[34:35]\n"
      "v_pk_fma_f32 v[228:229], v[228:229], v[44:45], v[36:37]\n"
      "v_pk_fma_f32 v[230:231], v[230:231], v[46:47], v[38:39]\n"
      "v_cvt_pk_bf16_f32 v64, v224, v225\n"
      "v_cvt_pk_bf16_f32 v65, v226, v227\n"
      "v_cvt_pk_bf16_f32 v66, v228, v229\n"
      "v_cvt_pk_bf16_f32 v67, v230, v231\n"
      "ds_write_b128 v87, v[64:67] offset:0\n"
      "v_accvgpr_read_b32 v15, a245\n"
      "buffer_load_dword v20, v15, s[32:35], 0 offen\n"
      "s_cmp_lt_u32 s84, 2\n"
      "s_cbranch_scc0 .Lno_11\n"
      "ds_read_b64_tr_b16 a[64:65], v93 offset:0\n"
      "ds_read_b64_tr_b16 a[66:67], v93 offset:768\n"
      "ds_read_b64_tr_b16 a[68:69], v93 offset:32\n"
      "ds_read_b64_tr_b16 a[70:71], v93 offset:800\n"
      "ds_read_b64_tr_b16 a[72:73], v93 offset:64\n"
      "ds_read_b64_tr_b16 a[74:75], v93 offset:832\n"
      "ds_read_b64_tr_b16 a[76:77], v93 offset:96\n"
      "ds_read_b64_tr_b16 a[78:79], v93 offset:864\n"
      "ds_read_b64_tr_b16 a[80:81], v93 offset:128\n"
      "ds_read_b64_tr_b16 a[82:83], v93 offset:896\n"
      "ds_read_b64_tr_b16 a[84:85], v93 offset:160\n"
      "ds_read_b64_tr_b16 a[86:87], v93 offset:928\n"
      ".Lno_11:\n"
      "s_cmp_ge_u32 s84, 2\n"
      "s_cbranch_scc0 .Lno_12\n"
      "ds_read_b64_tr_b16 a[64:65], v93 offset:0\n"
      "ds_read_b64_tr_b16 a[66:67], v93 offset:512\n"
      "ds_read_b64_tr_b16 a[68:69], v93 offset:64\n"
      "ds_read_b64_tr_b16 a[70:71], v93 offset:576\n"
      "ds_read_b64_tr_b16 a[72:73], v93 offset:12288\n"
      "ds_read_b64_tr_b16 a[74:75], v93 offset:12800\n"
      "ds_read_b64_tr_b16 a[76:77], v93 offset:12352\n"
      "ds_read_b64_tr_b16 a[78:79], v93 offset:12864\n"
      ".Lno_12:\n"
      "ds_read_b128 v[32:35], v83 offset:512\n"
      "v_accvgpr_read_b32 v15, a247\n"
      "buffer_load_dword v20, v15, s[4:7], 0 offen sc1\n"
      "ds_read_b128 v[36:39], v83 offset:1536\n"
      "ds_read_b128 v[40:43], v83 offset:2560\n"
      "ds_read_b128 v[44:47], v83 offset:3584\n"
      "s_waitcnt lgkmcnt(10)\n"
      "v_pk_fma_f32 v[104:105], v[104:105], v[56:57], v[48:49]\n"
      "v_pk_fma_f32 v[106:107], v[106:107], v[58:59], v[50:51]\n"
      "s_waitcnt lgkmcnt(9)\n"
      "v_pk_fma_f32 v[108:109], v[108:109], v[60:61], v[52:53]\n"
      "v_pk_fma_f32 v[110:111], v[110:111], v[62:63], v[54:55]\n"
      "v_cvt_pk_bf16_f32 v68, v104, v105\n"
      "v_cvt_pk_bf16_f32 v69, v106, v107\n"
      "v_cvt_pk_bf16_f32 v70, v108, v109\n"
      "v_cvt_pk_bf16_f32 v71, v110, v111\n"
      "ds_write_b128 v82, v[68:71] offset:20480\n"
      "v_pk_fma_f32 v[136:137], v[136:137], v[56:57], v[48:49]\n"
      "v_pk_fma_f32 v[138:139], v[138:139], v[58:59], v[50:51]\n"
      "v_pk_fma_f32 v[140:141], v[140:141], v[60:61], v[52:53]\n"
      "v_pk_fma_f32 v[142:143], v[142:143], v[62:63], v[54:55]\n"
      "v_cvt_pk_bf16_f32 v72, v136, v137\n"
      "v_cvt_pk_bf16_f32 v73, v138, v139\n"
      "v_cvt_pk_bf16_f32 v74, v140, v141\n"
      "v_cvt_pk_bf16_f32 v75, v142, v143\n"
      "ds_write_b128 v84, v[72:75] offset:20480\n"
      "v_pk_fma_f32 v[168:169], v[168:169], v[56:57], v[48:49]\n"
      "v_pk_fma_f32 v[170:171], v[170:171], v[58:59], v[50:51]\n"
      "v_pk_fma_f32 v[172:173], v[172:173], v[60:61], v[52:53]\n"
      "v_pk_fma_f32 v[174:175], v[174:175], v[62:63], v[54:55]\n"
      "v_cvt_pk_bf16_f32 v76, v168, v169\n"
      "v_cvt_pk_bf16_f32 v77, v170, v171\n"
      "v_cvt_pk_bf16_f32 v78, v172, v173\n"
      "v_cvt_pk_bf16_f32 v79, v174, v175\n"
      "ds_write_b128 v85, v[76:79] offset:20480\n"
      "v_pk_fma_f32 v[200:201], v[200:201], v[56:57], v[48:49]\n"
      "v_pk_fma_f32 v[202:203], v[202:203], v[58:59], v[50:51]\n"
      "v_pk_fma_f32 v[204:205], v[204:205], v[60:61], v[52:53]\n"
      "v_pk_fma_f32 v[206:207], v[206:207], v[62:63], v[54:55]\n"
      "v_cvt_pk_bf16_f32 v64, v200, v201\n"
      "v_cvt_pk_bf16_f32 v65, v202, v203\n"
      "v_cvt_pk_bf16_f32 v66, v204, v205\n"
      "v_cvt_pk_bf16_f32 v67, v206, v207\n"
      "ds_write_b128 v86, v[64:67] offset:20480\n"
      "v_pk_fma_f32 v[232:233], v[232:233], v[56:57], v[48:49]\n"
      "v_pk_fma_f32 v[234:235], v[234:235], v[58:59], v[50:51]\n"
      "v_pk_fma_f32 v[236:237], v[236:237], v[60:61], v[52:53]\n"
      "v_pk_fma_f32 v[238:239], v[238:239], v[62:63], v[54:55]\n"
      "v_cvt_pk_bf16_f32 v68, v232, v233\n"
      "v_cvt_pk_bf16_f32 v69, v234, v235\n"
      "v_cvt_pk_bf16_f32 v70, v236, v237\n"
      "v_cvt_pk_bf16_f32 v71, v238, v239\n"
      "ds_write_b128 v87, v[68:71] offset:20480\n"
      "ds_read_b128 v[48:51], v83 offset:768\n"
      "ds_read_b128 v[52:55], v83 offset:1792\n"
      "ds_read_b128 v[56:59], v83 offset:2816\n"
      "ds_read_b128 v[60:63], v83 offset:3840\n"
      "s_waitcnt lgkmcnt(10)\n"
      "v_pk_fma_f32 v[112:113], v[112:113], v[40:41], v[32:33]\n"
      "v_pk_fma_f32 v[114:115], v[114:115], v[42:43], v[34:35]\n"
      "s_waitcnt lgkmcnt(9)\n"
      "v_pk_fma_f32 v[116:117], v[116:117], v[44:45], v[36:37]\n"
      "v_pk_fma_f32 v[118:119], v[118:119], v[46:47], v[38:39]\n"
      "v_cvt_pk_bf16_f32 v72, v112, v113\n"
      "v_cvt_pk_bf16_f32 v73, v114, v115\n"
      "v_cvt_pk_bf16_f32 v74, v116, v117\n"
      "v_cvt_pk_bf16_f32 v75, v118, v119\n"
      "ds_write_b128 v82, v[72:75] offset:40960\n"
      "v_pk_fma_f32 v[144:145], v[144:145], v[40:41], v[32:33]\n"
      "v_pk_fma_f32 v[146:147], v[146:147], v[42:43], v[34:35]\n"
      "v_pk_fma_f32 v[148:149], v[148:149], v[44:45], v[36:37]\n"
      "v_pk_fma_f32 v[150:151], v[150:151], v[46:47], v[38:39]\n"
      "v_cvt_pk_bf16_f32 v76, v144, v145\n"
      "v_cvt_pk_bf16_f32 v77, v146, v147\n"
      "v_cvt_pk_bf16_f32 v78, v148, v149\n"
      "v_cvt_pk_bf16_f32 v79, v150, v151\n"
      "ds_write_b128 v84, v[76:79] offset:40960\n"
      "v_pk_fma_f32 v[176:177], v[176:177], v[40:41], v[32:33]\n"
      "v_pk_fma_f32 v[178:179], v[178:179], v[42:43], v[34:35]\n"
      "v_pk_fma_f32 v[180:181], v[180:181], v[44:45], v[36:37]\n"
      "v_pk_fma_f32 v[182:183], v[182:183], v[46:47], v[38:39]\n"
      "v_cvt_pk_bf16_f32 v64, v176, v177\n"
      "v_cvt_pk_bf16_f32 v65, v178, v179\n"
      "v_cvt_pk_bf16_f32 v66, v180, v181\n"
      "v_cvt_pk_bf16_f32 v67, v182, v183\n"
      "ds_write_b128 v85, v[64:67] offset:40960\n"
      "v_pk_fma_f32 v[208:209], v[208:209], v[40:41], v[32:33]\n"
      "v_pk_fma_f32 v[210:211], v[210:211], v[42:43], v[34:35]\n"
      "v_pk_fma_f32 v[212:213], v[212:213], v[44:45], v[36:37]\n"
      "v_pk_fma_f32 v[214:215], v[214:215], v[46:47], v[38:39]\n"
      "v_cvt_pk_bf16_f32 v68, v208, v209\n"
      "v_cvt_pk_bf16_f32 v69, v210, v211\n"
      "v_cvt_pk_bf16_f32 v70, v212, v213\n"
      "v_cvt_pk_bf16_f32 v71, v214, v215\n"
      "ds_write_b128 v86, v[68:71] offset:40960\n"
      "v_pk_fma_f32 v[240:241], v[240:241], v[40:41], v[32:33]\n"
      "v_pk_fma_f32 v[242:243], v[242:243], v[42:43], v[34:35]\n"
      "v_pk_fma_f32 v[244:245], v[244:245], v[44:45], v[36:37]\n"
      "v_pk_fma_f32 v[246:247], v[246:247], v[46:47], v[38:39]\n"
      "v_cvt_pk_bf16_f32 v72, v240, v241\n"
      "v_cvt_pk_bf16_f32 v73, v242, v243\n"
      "v_cvt_pk_bf16_f32 v74, v244, v245\n"
      "v_cvt_pk_bf16_f32 v75, v246, v247\n"
      "ds_write_b128 v87, v[72:75] offset:40960\n"
      "s_waitcnt lgkmcnt(6)\n"
      "v_pk_fma_f32 v[120:121], v[120:121], v[56:57], v[48:49]\n"
      "v_pk_fma_f32 v[122:123], v[122:123], v[58:59], v[50:51]\n"
      "s_waitcnt lgkmcnt(5)\n"
      "v_pk_fma_f32 v[124:125], v[124:125], v[60:61], v[52:53]\n"
      "v_pk_fma_f32 v[126:127], v[126:127], v[62:63], v[54:55]\n"
      "v_cvt_pk_bf16_f32 v76, v120, v121\n"
      "v_cvt_pk_bf16_f32 v77, v122, v123\n"
      "v_cvt_pk_bf16_f32 v78, v124, v125\n"
      "v_cvt_pk_bf16_f32 v79, v126, v127\n"
      "ds_write_b128 v82, v[76:79] offset:61440\n"
      "v_pk_fma_f32 v[152:153], v[152:153], v[56:57], v[48:49]\n"
      "v_pk_fma_f32 v[154:155], v[154:155], v[58:59], v[50:51]\n"
      "v_pk_fma_f32 v[156:157], v[156:157], v[60:61], v[52:53]\n"
      "v_pk_fma_f32 v[158:159], v[158:159], v[62:63], v[54:55]\n"
      "v_cvt_pk_bf16_f32 v64, v152, v153\n"
      "v_cvt_pk_bf16_f32 v65, v154, v155\n"
      "v_cvt_pk_bf16_f32 v66, v156, v157\n"
      "v_cvt_pk_bf16_f32 v67, v158, v159\n"
      "ds_write_b128 v84, v[64:67] offset:61440\n"
      "v_pk_fma_f32 v[184:185], v[184:185], v[56:57], v[48:49]\n"
      "v_pk_fma_f32 v[186:187], v[186:187], v[58:59], v[50:51]\n"
      "v_pk_fma_f32 v[188:189], v[188:189], v[60:61], v[52:53]\n"
      "v_pk_fma_f32 v[190:191], v[190:191], v[62:63], v[54:55]\n"
      "v_cvt_pk_bf16_f32 v68, v184, v185\n"
      "v_cvt_pk_bf16_f32 v69, v186, v187\n"
      "v_cvt_pk_bf16_f32 v70, v188, v189\n"
      "v_cvt_pk_bf16_f32 v71, v190, v191\n"
      "ds_write_b128 v85, v[68:71] offset:61440\n"
      "v_pk_fma_f32 v[216:217], v[216:217], v[56:57], v[48:49]\n"
      "v_pk_fma_f32 v[218:219], v[218:219], v[58:59], v[50:51]\n"
      "v_pk_fma_f32 v[220:221], v[220:221], v[60:61], v[52:53]\n"
      "v_pk_fma_f32 v[222:223], v[222:223], v[62:63], v[54:55]\n"
      "v_cvt_pk_bf16_f32 v72, v216, v217\n"
      "v_cvt_pk_bf16_f32 v73, v218, v219\n"
      "v_cvt_pk_bf16_f32 v74, v220, v221\n"
      "v_cvt_pk_bf16_f32 v75, v222, v223\n"
      "ds_write_b128 v86, v[72:75] offset:61440\n"
      "v_pk_fma_f32 v[248:249], v[248:249], v[56:57], v[48:49]\n"
      "v_pk_fma_f32 v[250:251], v[250:251], v[58:59], v[50:51]\n"
      "v_pk_fma_f32 v[252:253], v[252:253], v[60:61], v[52:53]\n"
      "v_pk_fma_f32 v[254:255], v[254:255], v[62:63], v[54:55]\n"
      "v_cvt_pk_bf16_f32 v76, v248, v249\n"
      "v_cvt_pk_bf16_f32 v77, v250, v251\n"
      "v_cvt_pk_bf16_f32 v78, v252, v253\n"
      "v_cvt_pk_bf16_f32 v79, v254, v255\n"
      "ds_write_b128 v87, v[76:79] offset:61440\n"
      "v_mov_b32 v21, v25\n"
      "v_mov_b32 v22, v26\n"
      "v_mov_b32 v23, s86\n"
      "v_mov_b32 v218, v31\n"
      "v_mov_b32 v220, v27\n"
      "v_mov_b32 v221, v28\n"
      "v_mov_b32 v222, v29\n"
      "v_mov_b32 v223, v30\n"
      "v_accvgpr_read_b32 v219, a244\n"
      "s_waitcnt vmcnt(9)\n"
      "s_waitcnt lgkmcnt(0)\n"
      "s_barrier\n"
      "ds_read_b64_tr_b16 a[24:25], v7 offset:1024\n"
      "s_add_u32 s95, s85, 4\n"
      "s_and_b32 s95, s95, 15\n"
      "s_add_u32 m0, s79, 131072\n"
      "s_mul_i32 s95, s95, 98304\n"
      "buffer_load_dwordx4 v6, s[20:23], s95 offen offset:0 lds\n"
      "ds_read_b64_tr_b16 a[26:27], v7 offset:1536\n"
      "ds_read_b128 a[0:3], v9 offset:0\n"
      "ds_read_b64_tr_b16 a[28:29], v7 offset:2048\n"
      "ds_read_b64_tr_b16 a[30:31], v7 offset:2560\n"
      "ds_read_b128 a[4:7], v9 offset:1024\n"
      "ds_read_b64_tr_b16 a[20:21], v7 offset:0\n"
      "ds_read_b64_tr_b16 a[22:23], v7 offset:512\n"
      "ds_read_b128 a[8:11], v9 offset:2048\n"
      "ds_read_b128 a[12:15], v9 offset:3072\n"
      "buffer_load_dwordx4 v6, s[20:23], s95 offen offset:1024 lds\n"
      "ds_read_b128 a[16:19], v9 offset:4096\n"
      "s_waitcnt lgkmcnt(7)\n"
      "v_mfma_f32_16x16x32_bf16 v[48:51], a[24:27], a[0:3], 0\n"
      "s_waitcnt lgkmcnt(5)\n"
      "ds_read_b64_tr_b16 a[56:57], v7 offset:13312\n"
      "ds_read_b64_tr_b16 a[58:59], v7 offset:13824\n"
      "v_mfma_f32_16x16x32_bf16 v[68:71], a[0:3], a[28:31], 0\n"
      "v_mfma_f32_16x16x32_bf16 v[52:55], a[24:27], a[4:7], 0\n"
      "ds_read_b128 a[32:35], v10 offset:5120\n"
      "ds_read_b64_tr_b16 a[60:61], v7 offset:14336\n"
      "v_mfma_f32_16x16x32_bf16 v[72:75], a[4:7], a[28:31], 0\n"
      "s_waitcnt lgkmcnt(6)\n"
      "buffer_load_dwordx4 v6, s[20:23], s95 offen offset:2048 lds\n"
      "v_mfma_f32_16x16x32_bf16 v[32:35], a[20:23], a[4:7], 0\n"
      "s_add_u32 s95, s85, 5\n"
      "ds_read_b64_tr_b16 a[62:63], v7 offset:14848\n"
      "ds_read_b128 a[36:39], v10 offset:6144\n"
      "v_mfma_f32_16x16x32_bf16 v[56:59], a[24:27], a[8:11], 0\n"
      "s_and_b32 s95, s95, 15\n"
      "v_mfma_f32_16x16x32_bf16 v[76:79], a[8:11], a[28:31], 0\n"
      "s_add_u32 m0, s79, 143360\n"
      "ds_read_b64_tr_b16 a[52:53], v7 offset:12288\n"
      "ds_read_b64_tr_b16 a[54:55], v7 offset:12800\n"
      "v_mfma_f32_16x16x32_bf16 v[36:39], a[20:23], a[8:11], 0\n"
      "s_waitcnt lgkmcnt(8)\n"
      "s_mul_i32 s95, s95, 98304\n"
      "buffer_load_dwordx4 v6, s[20:23], s95 offen offset:0 lds\n"
      "v_mfma_f32_16x16x32_bf16 v[60:63], a[24:27], a[12:15], 0\n"
      "ds_read_b128 a[40:43], v10 offset:7168\n"
      "ds_read_b128 a[44:47], v10 offset:8192\n"
      "v_mfma_f32_16x16x32_bf16 v[80:83], a[12:15], a[28:31], 0\n"
      "v_mfma_f32_16x16x32_bf16 v[40:43], a[20:23], a[12:15], 0\n"
      "ds_read_b128 a[48:51], v10 offset:9216\n"
      "v_mfma_f32_16x16x32_bf16 v[64:67], a[24:27], a[16:19], 0\n"
      "buffer_load_dwordx4 v6, s[20:23], s95 offen offset:1024 lds\n"
      "v_mfma_f32_16x16x32_bf16 v[84:87], a[16:19], a[28:31], 0\n"
      "v_mfma_f32_16x16x32_bf16 v[44:47], a[20:23], a[16:19], 0\n"
      "s_waitcnt vmcnt(8)\n"
      "s_waitcnt lgkmcnt(3)\n"
      "s_barrier\n"
      "v_mfma_f32_16x16x32_bf16 v[48:51], a[56:59], a[32:35], v[48:51]\n"
      "ds_read_b64_tr_b16 a[24:25], v7 offset:25600\n"
      "ds_read_b64_tr_b16 a[26:27], v7 offset:26112\n"
      "v_mfma_f32_16x16x32_bf16 v[68:71], a[32:35], a[60:63], v[68:71]\n"
      "buffer_load_dwordx4 v6, s[20:23], s95 offen offset:2048 lds\n"
      "v_mfma_f32_16x16x32_bf16 v[52:55], a[56:59], a[36:39], v[52:55]\n"
      "s_add_u32 m0, s79, 81920\n"
      "ds_read_b128 a[0:3], v9 offset:10240\n"
      "ds_read_b64_tr_b16 a[28:29], v7 offset:26624\n"
      "v_mfma_f32_16x16x32_bf16 v[72:75], a[36:39], a[60:63], v[72:75]\n"
      "v_mfma_f32_16x16x32_bf16 v[32:35], a[52:55], a[36:39], v[32:35]\n"
      "s_waitcnt lgkmcnt(5)\n"
      "ds_read_b64_tr_b16 a[30:31], v7 offset:27136\n"
      "ds_read_b128 a[4:7], v9 offset:11264\n"
      "v_mfma_f32_16x16x32_bf16 v[56:59], a[56:59], a[40:43], v[56:59]\n"
      "s_add_u32 s95, s85, 6\n"
      "s_and_b32 s95, s95, 15\n"
      "s_mul_i32 s95, s95, 98304\n"
      "buffer_load_dwordx4 v6, s[20:23], s95 offen offset:0 lds\n"
      "v_mfma_f32_16x16x32_bf16 v[76:79], a[40:43], a[60:63], v[76:79]\n"
      "ds_read_b64_tr_b16 a[20:21], v7 offset:24576\n"
      "ds_read_b64_tr_b16 a[22:23], v7 offset:25088\n"
      "v_mfma_f32_16x16x32_bf16 v[36:39], a[52:55], a[40:43], v[36:39]\n"
      "v_mfma_f32_16x16x32_bf16 v[60:63], a[56:59], a[44:47], v[60:63]\n"
      "ds_read_b128 a[8:11], v9 offset:12288\n"
      "ds_read_b128 a[12:15], v9 offset:13312\n"
      "v_mfma_f32_16x16x32_bf16 v[80:83], a[44:47], a[60:63], v[80:83]\n"
      "buffer_load_dwordx4 v6, s[20:23], s95 offen offset:1024 lds\n"
      "v_mfma_f32_16x16x32_bf16 v[40:43], a[52:55], a[44:47], v[40:43]\n"
      "s_waitcnt lgkmcnt(9)\n"
      "ds_read_b128 a[16:19], v9 offset:14336\n"
      "v_mfma_f32_16x16x32_bf16 v[64:67], a[56:59], a[48:51], v[64:67]\n"
      "v_mfma_f32_16x16x32_bf16 v[84:87], a[48:51], a[60:63], v[84:87]\n"
      "buffer_load_dwordx4 v6, s[20:23], s95 offen offset:2048 lds\n"
      "v_mfma_f32_16x16x32_bf16 v[44:47], a[52:55], a[48:51], v[44:47]\n"
      "s_waitcnt lgkmcnt(7)\n"
      "v_mfma_f32_16x16x32_bf16 v[48:51], a[24:27], a[0:3], v[48:51]\n"
      "s_waitcnt lgkmcnt(5)\n"
      "ds_read_b64_tr_b16 a[56:57], v7 offset:37888\n"
      "ds_read_b64_tr_b16 a[58:59], v7 offset:38400\n"
      "v_mfma_f32_16x16x32_bf16 v[68:71], a[0:3], a[28:31], v[68:71]\n"
      "s_add_u32 s95, s85, 7\n"
      "v_mfma_f32_16x16x32_bf16 v[52:55], a[24:27], a[4:7], v[52:55]\n"
      "s_and_b32 s95, s95, 15\n"
      "ds_read_b128 a[32:35], v10 offset:15360\n"
      "ds_read_b64_tr_b16 a[60:61], v7 offset:38912\n"
      "v_mfma_f32_16x16x32_bf16 v[72:75], a[4:7], a[28:31], v[72:75]\n"
      "s_waitcnt lgkmcnt(6)\n"
      "s_add_u32 m0, s79, 94208\n"
      "s_mul_i32 s95, s95, 98304\n"
      "buffer_load_dwordx4 v6, s[20:23], s95 offen offset:0 lds\n"
      "v_mfma_f32_16x16x32_bf16 v[32:35], a[20:23], a[4:7], v[32:35]\n"
      "ds_read_b64_tr_b16 a[62:63], v7 offset:39424\n"
      "ds_read_b128 a[36:39], v10 offset:16384\n"
      "v_mfma_f32_16x16x32_bf16 v[56:59], a[24:27], a[8:11], v[56:59]\n"
      "v_mfma_f32_16x16x32_bf16 v[76:79], a[8:11], a[28:31], v[76:79]\n"
      "ds_read_b64_tr_b16 a[52:53], v7 offset:36864\n"
      "ds_read_b64_tr_b16 a[54:55], v7 offset:37376\n"
      "v_mfma_f32_16x16x32_bf16 v[36:39], a[20:23], a[8:11], v[36:39]\n"
      "s_waitcnt lgkmcnt(8)\n"
      "buffer_load_dwordx4 v6, s[20:23], s95 offen offset:1024 lds\n"
      "v_mfma_f32_16x16x32_bf16 v[60:63], a[24:27], a[12:15], v[60:63]\n"
      "ds_read_b128 a[40:43], v10 offset:17408\n"
      "ds_read_b128 a[44:47], v10 offset:18432\n"
      "v_mfma_f32_16x16x32_bf16 v[80:83], a[12:15], a[28:31], v[80:83]\n"
      "v_mfma_f32_16x16x32_bf16 v[40:43], a[20:23], a[12:15], v[40:43]\n"
      "ds_read_b128 a[48:51], v10 offset:19456\n"
      "v_mfma_f32_16x16x32_bf16 v[64:67], a[24:27], a[16:19], v[64:67]\n"
      "buffer_load_dwordx4 v6, s[20:23], s95 offen offset:2048 lds\n"
      "v_mfma_f32_16x16x32_bf16 v[84:87], a[16:19], a[28:31], v[84:87]\n"
      "v_mfma_f32_16x16x32_bf16 v[44:47], a[20:23], a[16:19], v[44:47]\n"
      "s_waitcnt vmcnt(6)\n"
      "s_waitcnt lgkmcnt(3)\n"
      "s_barrier\n"
      "v_mfma_f32_16x16x32_bf16 v[48:51], a[56:59], a[32:35], v[48:51]\n"
      "s_add_u32 m0, s79, 106496\n"
      "ds_read_b64_tr_b16 a[24:25], v8 offset:1024\n"
      "ds_read_b64_tr_b16 a[26:27], v8 offset:1536\n"
      "v_mfma_f32_16x16x32_bf16 v[68:71], a[32:35], a[60:63], v[68:71]\n"
      "s_add_u32 s95, s85, 8\n"
      "s_and_b32 s95, s95, 15\n"
      "s_mul_i32 s95, s95, 98304\n"
      "buffer_load_dwordx4 v6, s[20:23], s95 offen offset:0 lds\n"
      "v_mfma_f32_16x16x32_bf16 v[52:55], a[56:59], a[36:39], v[52:55]\n"
      "ds_read_b128 a[0:3], v9 offset:20480\n"
      "ds_read_b64_tr_b16 a[28:29], v8 offset:2048\n"
      "v_mfma_f32_16x16x32_bf16 v[72:75], a[36:39], a[60:63], v[72:75]\n"
      "v_mfma_f32_16x16x32_bf16 v[32:35], a[52:55], a[36:39], v[32:35]\n"
      "s_waitcnt lgkmcnt(5)\n"
      "ds_read_b64_tr_b16 a[30:31], v8 offset:2560\n"
      "ds_read_b128 a[4:7], v9 offset:21504\n"
      "v_mfma_f32_16x16x32_bf16 v[56:59], a[56:59], a[40:43], v[56:59]\n"
      "buffer_load_dwordx4 v6, s[20:23], s95 offen offset:1024 lds\n"
      "v_mfma_f32_16x16x32_bf16 v[76:79], a[40:43], a[60:63], v[76:79]\n"
      "ds_read_b64_tr_b16 a[20:21], v8 offset:0\n"
      "ds_read_b64_tr_b16 a[22:23], v8 offset:512\n"
      "v_mfma_f32_16x16x32_bf16 v[36:39], a[52:55], a[40:43], v[36:39]\n"
      "v_mfma_f32_16x16x32_bf16 v[60:63], a[56:59], a[44:47], v[60:63]\n"
      "ds_read_b128 a[8:11], v9 offset:22528\n"
      "ds_read_b128 a[12:15], v9 offset:23552\n"
      "v_mfma_f32_16x16x32_bf16 v[80:83], a[44:47], a[60:63], v[80:83]\n"
      "buffer_load_dwordx4 v6, s[20:23], s95 offen offset:2048 lds\n"
      "v_mfma_f32_16x16x32_bf16 v[40:43], a[52:55], a[44:47], v[40:43]\n"
      "s_waitcnt lgkmcnt(9)\n"
      "ds_read_b128 a[16:19], v9 offset:24576\n"
      "v_mfma_f32_16x16x32_bf16 v[64:67], a[56:59], a[48:51], v[64:67]\n"
      "s_add_u32 m0, s79, 118784\n"
      "v_mfma_f32_16x16x32_bf16 v[84:87], a[48:51], a[60:63], v[84:87]\n"
      "s_add_u32 s95, s85, 9\n"
      "s_and_b32 s95, s95, 15\n"
      "s_mul_i32 s95, s95, 98304\n"
      "buffer_load_dwordx4 v6, s[20:23], s95 offen offset:0 lds\n"
      "v_mfma_f32_16x16x32_bf16 v[44:47], a[52:55], a[48:51], v[44:47]\n"
      "s_waitcnt lgkmcnt(7)\n"
      "v_mfma_f32_16x16x32_bf16 v[48:51], a[24:27], a[0:3], v[48:51]\n"
      "s_waitcnt lgkmcnt(5)\n"
      "ds_read_b64_tr_b16 a[56:57], v8 offset:13312\n"
      "ds_read_b64_tr_b16 a[58:59], v8 offset:13824\n"
      "v_mfma_f32_16x16x32_bf16 v[68:71], a[0:3], a[28:31], v[68:71]\n"
      "buffer_load_dwordx4 v6, s[20:23], s95 offen offset:1024 lds\n"
      "v_mfma_f32_16x16x32_bf16 v[52:55], a[24:27], a[4:7], v[52:55]\n"
      "ds_read_b128 a[32:35], v10 offset:25600\n"
      "ds_read_b64_tr_b16 a[60:61], v8 offset:14336\n"
      "v_mfma_f32_16x16x32_bf16 v[72:75], a[4:7], a[28:31], v[72:75]\n"
      "s_waitcnt lgkmcnt(6)\n"
      "v_mfma_f32_16x16x32_bf16 v[32:35], a[20:23], a[4:7], v[32:35]\n"
      "ds_read_b64_tr_b16 a[62:63], v8 offset:14848\n"
      "ds_read_b128 a[36:39], v10 offset:26624\n"
      "v_mfma_f32_16x16x32_bf16 v[56:59], a[24:27], a[8:11], v[56:59]\n"
      "buffer_load_dwordx4 v6, s[20:23], s95 offen offset:2048 lds\n"
      "v_mfma_f32_16x16x32_bf16 v[76:79], a[8:11], a[28:31], v[76:79]\n"
      "ds_read_b64_tr_b16 a[52:53], v8 offset:12288\n"
      "ds_read_b64_tr_b16 a[54:55], v8 offset:12800\n"
      "v_mfma_f32_16x16x32_bf16 v[36:39], a[20:23], a[8:11], v[36:39]\n"
      "s_waitcnt lgkmcnt(8)\n"
      "v_mfma_f32_16x16x32_bf16 v[60:63], a[24:27], a[12:15], v[60:63]\n"
      "ds_read_b128 a[40:43], v10 offset:27648\n"
      "ds_read_b128 a[44:47], v10 offset:28672\n"
      "v_mfma_f32_16x16x32_bf16 v[80:83], a[12:15], a[28:31], v[80:83]\n"
      "buffer_store_dwordx4 a[64:67], v95, s[4:7], s49 offen sc1\n"
      "s_add_u32 s49, s49, s50\n"
      "v_mfma_f32_16x16x32_bf16 v[40:43], a[20:23], a[12:15], v[40:43]\n"
      "ds_read_b128 a[48:51], v10 offset:29696\n"
      "v_mfma_f32_16x16x32_bf16 v[64:67], a[24:27], a[16:19], v[64:67]\n"
      "v_mfma_f32_16x16x32_bf16 v[84:87], a[16:19], a[28:31], v[84:87]\n"
      "v_mfma_f32_16x16x32_bf16 v[44:47], a[20:23], a[16:19], v[44:47]\n"
      "s_waitcnt vmcnt(7)\n"
      "s_waitcnt lgkmcnt(3)\n"
      "s_barrier\n"
      "v_mfma_f32_16x16x32_bf16 v[48:51], a[56:59], a[32:35], v[48:51]\n"
      "ds_read_b64_tr_b16 a[24:25], v7 offset:1024\n"
      "ds_read_b64_tr_b16 a[26:27], v7 offset:1536\n"
      "v_mfma_f32_16x16x32_bf16 v[68:71], a[32:35], a[60:63], v[68:71]\n"
      "buffer_store_dwordx4 a[68:71], v95, s[4:7], s49 offen sc1\n"
      "s_mov_b32 s49, s51\n"
      "v_mfma_f32_16x16x32_bf16 v[52:55], a[56:59], a[36:39], v[52:55]\n"
      "ds_read_b128 a[0:3], v9 offset:30720\n"
      "ds_read_b64_tr_b16 a[28:29], v7 offset:2048\n"
      "v_mfma_f32_16x16x32_bf16 v[72:75], a[36:39], a[60:63], v[72:75]\n"
      "v_mfma_f32_16x16x32_bf16 v[32:35], a[52:55], a[36:39], v[32:35]\n"
      "s_waitcnt lgkmcnt(5)\n"
      "ds_read_b64_tr_b16 a[30:31], v7 offset:2560\n"
      "ds_read_b128 a[4:7], v9 offset:31744\n"
      "v_mfma_f32_16x16x32_bf16 v[56:59], a[56:59], a[40:43], v[56:59]\n"
      "v_mfma_f32_16x16x32_bf16 v[76:79], a[40:43], a[60:63], v[76:79]\n"
      "ds_read_b64_tr_b16 a[20:21], v7 offset:0\n"
      "ds_read_b64_tr_b16 a[22:23], v7 offset:512\n"
      "v_mfma_f32_16x16x32_bf16 v[36:39], a[52:55], a[40:43], v[36:39]\n"
      "v_mfma_f32_16x16x32_bf16 v[60:63], a[56:59], a[44:47], v[60:63]\n"
      "ds_read_b128 a[8:11], v9 offset:32768\n"
      "ds_read_b128 a[12:15], v9 offset:33792\n"
      "v_mfma_f32_16x16x32_bf16 v[80:83], a[44:47], a[60:63], v[80:83]\n"
      "v_mfma_f32_16x16x32_bf16 v[40:43], a[52:55], a[44:47], v[40:43]\n"
      "s_waitcnt lgkmcnt(9)\n"
      "ds_read_b128 a[16:19], v9 offset:34816\n"
      "v_mfma_f32_16x16x32_bf16 v[64:67], a[56:59], a[48:51], v[64:67]\n"
      "buffer_store_dwordx4 a[72:75], v95, s[4:7], s49 offen sc1\n"
      "s_add_u32 s49, s49, s50\n"
      "v_mfma_f32_16x16x32_bf16 v[84:87], a[48:51], a[60:63], v[84:87]\n"
      "v_mfma_f32_16x16x32_bf16 v[44:47], a[52:55], a[48:51], v[44:47]\n"
      "s_waitcnt lgkmcnt(7)\n"
      "v_mfma_f32_16x16x32_bf16 v[48:51], a[24:27], a[0:3], v[48:51]\n"
      "s_waitcnt lgkmcnt(5)\n"
      "ds_read_b64_tr_b16 a[56:57], v7 offset:13312\n"
      "ds_read_b64_tr_b16 a[58:59], v7 offset:13824\n"
      "v_mfma_f32_16x16x32_bf16 v[68:71], a[0:3], a[28:31], v[68:71]\n"
      "s_add_u32 m0, s79, 131072\n"
      "v_mfma_f32_16x16x32_bf16 v[52:55], a[24:27], a[4:7], v[52:55]\n"
      "ds_read_b128 a[32:35], v10 offset:35840\n"
      "ds_read_b64_tr_b16 a[60:61], v7 offset:14336\n"
      "v_mfma_f32_16x16x32_bf16 v[72:75], a[4:7], a[28:31], v[72:75]\n"
      "s_waitcnt lgkmcnt(6)\n"
      "v_mfma_f32_16x16x32_bf16 v[32:35], a[20:23], a[4:7], v[32:35]\n"
      "ds_read_b64_tr_b16 a[62:63], v7 offset:14848\n"
      "ds_read_b128 a[36:39], v10 offset:36864\n"
      "v_mfma_f32_16x16x32_bf16 v[56:59], a[24:27], a[8:11], v[56:59]\n"
      "s_add_u32 s95, s85, 10\n"
      "s_and_b32 s95, s95, 15\n"
      "s_mul_i32 s95, s95, 98304\n"
      "buffer_load_dwordx4 v6, s[20:23], s95 offen offset:0 lds\n"
      "v_mfma_f32_16x16x32_bf16 v[76:79], a[8:11], a[28:31], v[76:79]\n"
      "ds_read_b64_tr_b16 a[52:53], v7 offset:12288\n"
      "ds_read_b64_tr_b16 a[54:55], v7 offset:12800\n"
      "v_mfma_f32_16x16x32_bf16 v[36:39], a[20:23], a[8:11], v[36:39]\n"
      "s_waitcnt lgkmcnt(8)\n"
      "v_mfma_f32_16x16x32_bf16 v[60:63], a[24:27], a[12:15], v[60:63]\n"
      "ds_read_b128 a[40:43], v10 offset:37888\n"
      "ds_read_b128 a[44:47], v10 offset:38912\n"
      "v_mfma_f32_16x16x32_bf16 v[80:83], a[12:15], a[28:31], v[80:83]\n"
      "buffer_load_dwordx4 v6, s[20:23], s95 offen offset:1024 lds\n"
      "v_mfma_f32_16x16x32_bf16 v[40:43], a[20:23], a[12:15], v[40:43]\n"
      "ds_read_b128 a[48:51], v10 offset:39936\n"
      "v_mfma_f32_16x16x32_bf16 v[64:67], a[24:27], a[16:19], v[64:67]\n"
      "v_mfma_f32_16x16x32_bf16 v[84:87], a[16:19], a[28:31], v[84:87]\n"
      "v_mfma_f32_16x16x32_bf16 v[44:47], a[20:23], a[16:19], v[44:47]\n"
      "buffer_load_dwordx4 v6, s[20:23], s95 offen offset:2048 lds\n"
      "s_add_u32 s95, s85, 11\n"
      "s_and_b32 s95, s95, 15\n"
      "s_add_u32 m0, s79, 143360\n"
      "s_mul_i32 s95, s95, 98304\n"
      "buffer_load_dwordx4 v6, s[20:23], s95 offen offset:0 lds\n"
      "s_waitcnt vmcnt(7)\n"
      "s_waitcnt lgkmcnt(3)\n"
      "s_barrier\n"
      "v_add_u32 v9, 40960, v9\n"
      "v_add_u32 v10, 40960, v10\n"
      "v_mfma_f32_16x16x32_bf16 v[48:51], a[56:59], a[32:35], v[48:51]\n"
      "ds_read_b64_tr_b16 a[24:25], v7 offset:25600\n"
      "ds_read_b64_tr_b16 a[26:27], v7 offset:26112\n"
      "v_mfma_f32_16x16x32_bf16 v[68:71], a[32:35], a[60:63], v[68:71]\n"
      "buffer_load_dwordx4 v6, s[20:23], s95 offen offset:1024 lds\n"
      "v_mfma_f32_16x16x32_bf16 v[52:55], a[56:59], a[36:39], v[52:55]\n"
      "ds_read_b128 a[0:3], v9 offset:0\n"
      "ds_read_b64_tr_b16 a[28:29], v7 offset:26624\n"
      "v_mfma_f32_16x16x32_bf16 v[72:75], a[36:39], a[60:63], v[72:75]\n"
      "v_mfma_f32_16x16x32_bf16 v[32:35], a[52:55], a[36:39], v[32:35]\n"
      "s_waitcnt lgkmcnt(5)\n"
      "ds_read_b64_tr_b16 a[30:31], v7 offset:27136\n"
      "ds_read_b128 a[4:7], v9 offset:1024\n"
      "v_mfma_f32_16x16x32_bf16 v[56:59], a[56:59], a[40:43], v[56:59]\n"
      "buffer_load_dwordx4 v6, s[20:23], s95 offen offset:2048 lds\n"
      "v_mfma_f32_16x16x32_bf16 v[76:79], a[40:43], a[60:63], v[76:79]\n"
      "ds_read_b64_tr_b16 a[20:21], v7 offset:24576\n"
      "ds_read_b64_tr_b16 a[22:23], v7 offset:25088\n"
      "v_mfma_f32_16x16x32_bf16 v[36:39], a[52:55], a[40:43], v[36:39]\n"
      "v_mfma_f32_16x16x32_bf16 v[60:63], a[56:59], a[44:47], v[60:63]\n"
      "ds_read_b128 a[8:11], v9 offset:2048\n"
      "ds_read_b128 a[12:15], v9 offset:3072\n"
      "v_mfma_f32_16x16x32_bf16 v[80:83], a[44:47], a[60:63], v[80:83]\n"
      "buffer_store_dwordx4 a[76:79], v95, s[4:7], s49 offen sc1\n"
      "s_add_u32 s49, s49, s50\n"
      "v_mfma_f32_16x16x32_bf16 v[40:43], a[52:55], a[44:47], v[40:43]\n"
      "s_waitcnt lgkmcnt(9)\n"
      "ds_read_b128 a[16:19], v9 offset:4096\n"
      "v_mfma_f32_16x16x32_bf16 v[64:67], a[56:59], a[48:51], v[64:67]\n"
      "v_mfma_f32_16x16x32_bf16 v[84:87], a[48:51], a[60:63], v[84:87]\n"
      "v_mfma_f32_16x16x32_bf16 v[44:47], a[52:55], a[48:51], v[44:47]\n"
      "s_waitcnt lgkmcnt(7)\n"
      "v_mfma_f32_16x16x32_bf16 v[48:51], a[24:27], a[0:3], v[48:51]\n"
      "s_waitcnt lgkmcnt(5)\n"
      "ds_read_b64_tr_b16 a[56:57], v7 offset:37888\n"
      "ds_read_b64_tr_b16 a[58:59], v7 offset:38400\n"
      "v_mfma_f32_16x16x32_bf16 v[68:71], a[0:3], a[28:31], v[68:71]\n"
      "v_mfma_f32_16x16x32_bf16 v[52:55], a[24:27], a[4:7], v[52:55]\n"
      "ds_read_b128 a[32:35], v10 offset:5120\n"
      "ds_read_b64_tr_b16 a[60:61], v7 offset:38912\n"
      "v_mfma_f32_16x16x32_bf16 v[72:75], a[4:7], a[28:31], v[72:75]\n"
      "s_cmp_gt_u32 s52, 4\n"
      "s_cbranch_scc0 .Lsk_6\n"
      "buffer_store_dwordx4 a[80:83], v95, s[4:7], s49 offen sc1\n"
      ".Lsk_6:\n"
      "s_add_u32 s49, s49, s50\n"
      "s_cmp_gt_u32 s52, 5\n"
      "s_cbranch_scc0 .Lsk_7\n"
      "buffer_store_dwordx4 a[84:87], v95, s[4:7], s49 offen sc1\n"
      ".Lsk_7:\n"
      "s_add_u32 s49, s49, s50\n"
      "s_waitcnt lgkmcnt(6)\n"
      "v_mfma_f32_16x16x32_bf16 v[32:35], a[20:23], a[4:7], v[32:35]\n"
      "ds_read_b64_tr_b16 a[62:63], v7 offset:39424\n"
      "ds_read_b128 a[36:39], v10 offset:6144\n"
      "v_mfma_f32_16x16x32_bf16 v[56:59], a[24:27], a[8:11], v[56:59]\n"
      "v_mfma_f32_16x16x32_bf16 v[76:79], a[8:11], a[28:31], v[76:79]\n"
      "ds_read_b64_tr_b16 a[52:53], v7 offset:36864\n"
      "ds_read_b64_tr_b16 a[54:55], v7 offset:37376\n"
      "v_mfma_f32_16x16x32_bf16 v[36:39], a[20:23], a[8:11], v[36:39]\n"
      "s_waitcnt lgkmcnt(8)\n"
      "v_mfma_f32_16x16x32_bf16 v[60:63], a[24:27], a[12:15], v[60:63]\n"
      "ds_read_b128 a[40:43], v10 offset:7168\n"
      "ds_read_b128 a[44:47], v10 offset:8192\n"
      "v_mfma_f32_16x16x32_bf16 v[80:83], a[12:15], a[28:31], v[80:83]\n"
      "s_add_u32 s95, s85, 12\n"
      "v_mfma_f32_16x16x32_bf16 v[40:43], a[20:23], a[12:15], v[40:43]\n"
      "s_and_b32 s95, s95, 15\n"
      "ds_read_b128 a[48:51], v10 offset:9216\n"
      "v_mfma_f32_16x16x32_bf16 v[64:67], a[24:27], a[16:19], v[64:67]\n"
      "s_add_u32 m0, s79, 81920\n"
      "v_mfma_f32_16x16x32_bf16 v[84:87], a[16:19], a[28:31], v[84:87]\n"
      "s_mul_i32 s95, s95, 98304\n"
      "v_mfma_f32_16x16x32_bf16 v[44:47], a[20:23], a[16:19], v[44:47]\n"
      "s_add_u32 s93, s54, 0\n"
      "buffer_store_dwordx4 v[16:19], v89, s[4:7], s93 offen sc1\n"
      "buffer_load_dwordx4 v6, s[20:23], s95 offen offset:0 lds\n"
      "s_waitcnt vmcnt(3)\n"
      "s_waitcnt lgkmcnt(3)\n"
      "s_barrier\n"
      "v_mfma_f32_16x16x32_bf16 v[48:51], a[56:59], a[32:35], v[48:51]\n"
      "ds_read_b64_tr_b16 a[24:25], v8 offset:1024\n"
      "ds_read_b64_tr_b16 a[26:27], v8 offset:1536\n"
      "v_mfma_f32_16x16x32_bf16 v[68:71], a[32:35], a[60:63], v[68:71]\n"
      "buffer_load_dwordx4 v6, s[20:23], s95 offen offset:1024 lds\n"
      "v_mfma_f32_16x16x32_bf16 v[52:55], a[56:59], a[36:39], v[52:55]\n"
      "ds_read_b128 a[0:3], v9 offset:10240\n"
      "ds_read_b64_tr_b16 a[28:29], v8 offset:2048\n"
      "v_mfma_f32_16x16x32_bf16 v[72:75], a[36:39], a[60:63], v[72:75]\n"
      "v_mfma_f32_16x16x32_bf16 v[32:35], a[52:55], a[36:39], v[32:35]\n"
      "s_waitcnt lgkmcnt(5)\n"
      "ds_read_b64_tr_b16 a[30:31], v8 offset:2560\n"
      "ds_read_b128 a[4:7], v9 offset:11264\n"
      "v_mfma_f32_16x16x32_bf16 v[56:59], a[56:59], a[40:43], v[56:59]\n"
      "buffer_load_dwordx4 v6, s[20:23], s95 offen offset:2048 lds\n"
      "v_mfma_f32_16x16x32_bf16 v[76:79], a[40:43], a[60:63], v[76:79]\n"
      "s_add_u32 m0, s79, 94208\n"
      "ds_read_b64_tr_b16 a[20:21], v8 offset:0\n"
      "ds_read_b64_tr_b16 a[22:23], v8 offset:512\n"
      "v_mfma_f32_16x16x32_bf16 v[36:39], a[52:55], a[40:43], v[36:39]\n"
      "v_mfma_f32_16x16x32_bf16 v[60:63], a[56:59], a[44:47], v[60:63]\n"
      "ds_read_b128 a[8:11], v9 offset:12288\n"
      "ds_read_b128 a[12:15], v9 offset:13312\n"
      "v_mfma_f32_16x16x32_bf16 v[80:83], a[44:47], a[60:63], v[80:83]\n"
      "s_add_u32 s95, s85, 13\n"
      "s_and_b32 s95, s95, 15\n"
      "s_mul_i32 s95, s95, 98304\n"
      "buffer_load_dwordx4 v6, s[20:23], s95 offen offset:0 lds\n"
      "v_mfma_f32_16x16x32_bf16 v[40:43], a[52:55], a[44:47], v[40:43]\n"
      "s_waitcnt lgkmcnt(9)\n"
      "ds_read_b128 a[16:19], v9 offset:14336\n"
      "v_mfma_f32_16x16x32_bf16 v[64:67], a[56:59], a[48:51], v[64:67]\n"
      "v_mfma_f32_16x16x32_bf16 v[84:87], a[48:51], a[60:63], v[84:87]\n"
      "buffer_load_dwordx4 v6, s[20:23], s95 offen offset:1024 lds\n"
      "v_mfma_f32_16x16x32_bf16 v[44:47], a[52:55], a[48:51], v[44:47]\n"
      "s_waitcnt lgkmcnt(7)\n"
      "v_mfma_f32_16x16x32_bf16 v[48:51], a[24:27], a[0:3], v[48:51]\n"
      "s_waitcnt lgkmcnt(5)\n"
      "ds_read_b64_tr_b16 a[56:57], v8 offset:13312\n"
      "ds_read_b64_tr_b16 a[58:59], v8 offset:13824\n"
      "v_mfma_f32_16x16x32_bf16 v[68:71], a[0:3], a[28:31], v[68:71]\n"
      "v_mfma_f32_16x16x32_bf16 v[52:55], a[24:27], a[4:7], v[52:55]\n"
      "ds_read_b128 a[32:35], v10 offset:15360\n"
      "ds_read_b64_tr_b16 a[60:61], v8 offset:14336\n"
      "v_mfma_f32_16x16x32_bf16 v[72:75], a[4:7], a[28:31], v[72:75]\n"
      "s_waitcnt lgkmcnt(6)\n"
      "buffer_load_dwordx4 v6, s[20:23], s95 offen offset:2048 lds\n"
      "v_mfma_f32_16x16x32_bf16 v[32:35], a[20:23], a[4:7], v[32:35]\n"
      "s_add_u32 s93, s54, 65536\n"
      "ds_read_b64_tr_b16 a[62:63], v8 offset:14848\n"
      "ds_read_b128 a[36:39], v10 offset:16384\n"
      "v_mfma_f32_16x16x32_bf16 v[56:59], a[24:27], a[8:11], v[56:59]\n"
      "v_mfma_f32_16x16x32_bf16 v[76:79], a[8:11], a[28:31], v[76:79]\n"
      "ds_read_b64_tr_b16 a[52:53], v8 offset:12288\n"
      "ds_read_b64_tr_b16 a[54:55], v8 offset:12800\n"
      "v_mfma_f32_16x16x32_bf16 v[36:39], a[20:23], a[8:11], v[36:39]\n"
      "s_waitcnt lgkmcnt(8)\n"
      "buffer_store_dwordx4 v[16:19], v89, s[4:7], s93 offen sc1\n"
      "v_mfma_f32_16x16x32_bf16 v[60:63], a[24:27], a[12:15], v[60:63]\n"
      "ds_read_b128 a[40:43], v10 offset:17408\n"
      "ds_read_b128 a[44:47], v10 offset:18432\n"
      "v_mfma_f32_16x16x32_bf16 v[80:83], a[12:15], a[28:31], v[80:83]\n"
      "s_add_u32 m0, s79, 106496\n"
      "v_mfma_f32_16x16x32_bf16 v[40:43], a[20:23], a[12:15], v[40:43]\n"
      "ds_read_b128 a[48:51], v10 offset:19456\n"
      "v_mfma_f32_16x16x32_bf16 v[64:67], a[24:27], a[16:19], v[64:67]\n"
      "v_mfma_f32_16x16x32_bf16 v[84:87], a[16:19], a[28:31], v[84:87]\n"
      "v_mfma_f32_16x16x32_bf16 v[44:47], a[20:23], a[16:19], v[44:47]\n"
      "s_add_u32 s95, s85, 14\n"
      "s_and_b32 s95, s95, 15\n"
      "s_mul_i32 s95, s95, 98304\n"
      "buffer_load_dwordx4 v6, s[20:23], s95 offen offset:0 lds\n"
      "buffer_load_dwordx4 v6, s[20:23], s95 offen offset:1024 lds\n"
      "buffer_load_dwordx4 v6, s[20:23], s95 offen offset:2048 lds\n"
      "s_add_u32 s95, s85, 15\n"
      "s_and_b32 s95, s95, 15\n"
      "s_add_u32 m0, s79, 118784\n"
      "s_mul_i32 s95, s95, 98304\n"
      "buffer_load_dwordx4 v6, s[20:23], s95 offen offset:0 lds\n"
      "buffer_load_dwordx4 v6, s[20:23], s95 offen offset:1024 lds\n"
      "buffer_load_dwordx4 v6, s[20:23], s95 offen offset:2048 lds\n"
      "buffer_load_ushort v224, v219, s[8:11], s1 offen\n"
      "s_lshl_b32 s93, s84, 10\n"
      "s_add_u32 m0, s93, 0\n"
      "s_add_u32 s93, s0, 0\n"
      "buffer_load_dwordx4 v218, s[12:15], s93 offen lds\n"
      "s_lshl_b32 s93, s84, 10\n"
      "s_add_u32 m0, s93, 4096\n"
      "s_add_u32 s93, s0, 196608\n"
      "buffer_load_dwordx4 v218, s[12:15], s93 offen lds\n"
      "s_waitcnt vmcnt(10)\n"
      "s_waitcnt lgkmcnt(3)\n"
      "s_barrier\n"
      "s_lshl_b32 s93, s84, 10\n"
      "s_add_u32 m0, s93, 8192\n"
      "s_add_u32 s93, s0, 393216\n"
      "buffer_load_dwordx4 v218, s[12:15], s93 offen lds\n"
      "s_lshl_b32 s93, s84, 10\n"
      "s_add_u32 m0, s93, 12288\n"
      "s_add_u32 s93, s0, 589824\n"
      "buffer_load_dwordx4 v218, s[12:15], s93 offen lds\n"
      "s_mul_i32 s22, s84, 1032\n"
      "s_lshl_b32 s21, s84, 14\n"
      "v_mov_b32 v20, v24\n"
      "v_mfma_f32_16x16x32_bf16 v[48:51], a[56:59], a[32:35], v[48:51]\n"
      "ds_read_b64_tr_b16 a[24:25], v7 offset:1024\n"
      "ds_read_b64_tr_b16 a[26:27], v7 offset:1536\n"
      "v_mfma_f32_16x16x32_bf16 v[68:71], a[32:35], a[60:63], v[68:71]\n"
      "s_add_u32 m0, s22, 131456\n"
      "v_mfma_f32_16x16x32_bf16 v[52:55], a[56:59], a[36:39], v[52:55]\n"
      "ds_read_b128 a[0:3], v9 offset:20480\n"
      "ds_read_b64_tr_b16 a[28:29], v7 offset:2048\n"
      "v_mfma_f32_16x16x32_bf16 v[72:75], a[36:39], a[60:63], v[72:75]\n"
      "v_mfma_f32_16x16x32_bf16 v[32:35], a[52:55], a[36:39], v[32:35]\n"
      "s_waitcnt lgkmcnt(5)\n"
      "ds_read_b64_tr_b16 a[30:31], v7 offset:2560\n"
      "ds_read_b128 a[4:7], v9 offset:21504\n"
      "v_mfma_f32_16x16x32_bf16 v[56:59], a[56:59], a[40:43], v[56:59]\n"
      "s_add_u32 s93, s56, 12\n"
      "s_and_b32 s93, s93, 15\n"
      "s_lshl_b32 s93, s93, 15\n"
      "buffer_load_dwordx4 v88, s[32:35], s93 offen lds\n"
      "v_mfma_f32_16x16x32_bf16 v[76:79], a[40:43], a[60:63], v[76:79]\n"
      "s_add_u32 m0, s22, 135584\n"
      "ds_read_b64_tr_b16 a[20:21], v7 offset:0\n"
      "ds_read_b64_tr_b16 a[22:23], v7 offset:512\n"
      "v_mfma_f32_16x16x32_bf16 v[36:39], a[52:55], a[40:43], v[36:39]\n"
      "v_mfma_f32_16x16x32_bf16 v[60:63], a[56:59], a[44:47], v[60:63]\n"
      "ds_read_b128 a[8:11], v9 offset:22528\n"
      "ds_read_b128 a[12:15], v9 offset:23552\n"
      "v_mfma_f32_16x16x32_bf16 v[80:83], a[44:47], a[60:63], v[80:83]\n"
      "s_add_u32 s93, s56, 13\n"
      "s_and_b32 s93, s93, 15\n"
      "s_lshl_b32 s93, s93, 15\n"
      "buffer_load_dwordx4 v88, s[32:35], s93 offen lds\n"
      "v_mfma_f32_16x16x32_bf16 v[40:43], a[52:55], a[44:47], v[40:43]\n"
      "s_waitcnt lgkmcnt(9)\n"
      "ds_read_b128 a[16:19], v9 offset:24576\n"
      "v_mfma_f32_16x16x32_bf16 v[64:67], a[56:59], a[48:51], v[64:67]\n"
      "s_add_u32 m0, s22, 139712\n"
      "v_mfma_f32_16x16x32_bf16 v[84:87], a[48:51], a[60:63], v[84:87]\n"
      "s_add_u32 s93, s56, 14\n"
      "s_and_b32 s93, s93, 15\n"
      "s_lshl_b32 s93, s93, 15\n"
      "buffer_load_dwordx4 v88, s[32:35], s93 offen lds\n"
      "v_mfma_f32_16x16x32_bf16 v[44:47], a[52:55], a[48:51], v[44:47]\n"
      "s_waitcnt lgkmcnt(7)\n"
      "v_and_b32 v100, 15, v1\n"
      "v_and_b32 v101, 1, v1\n"
      "v_mfma_f32_16x16x32_bf16 v[48:51], a[24:27], a[0:3], v[48:51]\n"
      "s_waitcnt lgkmcnt(5)\n"
      "ds_read_b64_tr_b16 a[56:57], v7 offset:13312\n"
      "ds_read_b64_tr_b16 a[58:59], v7 offset:13824\n"
      "v_mfma_f32_16x16x32_bf16 v[68:71], a[0:3], a[28:31], v[68:71]\n"
      "s_add_u32 s93, s56, 15\n"
      "s_and_b32 s93, s93, 15\n"
      "s_add_u32 m0, s22, 143840\n"
      "s_lshl_b32 s93, s93, 15\n"
      "buffer_load_dwordx4 v88, s[32:35], s93 offen lds\n"
      "v_mfma_f32_16x16x32_bf16 v[52:55], a[24:27], a[4:7], v[52:55]\n"
      "ds_read_b128 a[32:35], v10 offset:25600\n"
      "ds_read_b64_tr_b16 a[60:61], v7 offset:14336\n"
      "v_mfma_f32_16x16x32_bf16 v[72:75], a[4:7], a[28:31], v[72:75]\n"
      "s_waitcnt lgkmcnt(6)\n"
      "v_cmp_eq_u32 s[8:9], 1, v101\n"
      "v_lshl_add_u32 v102, v12, 2, v101\n"
      "v_mfma_f32_16x16x32_bf16 v[32:35], a[20:23], a[4:7], v[32:35]\n"
      "ds_read_b64_tr_b16 a[62:63], v7 offset:14848\n"
      "ds_read_b128 a[36:39], v10 offset:26624\n"
      "v_mfma_f32_16x16x32_bf16 v[56:59], a[24:27], a[8:11], v[56:59]\n"
      "buffer_load_dwordx4 v[24:27], v5, s[16:19], s66 offen offset:0\n"
      "v_mfma_f32_16x16x32_bf16 v[76:79], a[8:11], a[28:31], v[76:79]\n"
      "ds_read_b64_tr_b16 a[52:53], v7 offset:12288\n"
      "ds_read_b64_tr_b16 a[54:55], v7 offset:12800\n"
      "v_mfma_f32_16x16x32_bf16 v[36:39], a[20:23], a[8:11], v[36:39]\n"
      "s_waitcnt lgkmcnt(8)\n"
      "v_cmp_le_u32 s[10:11], v102, v100\n"
      "v_add_u32 v102, 2, v102\n"
      "v_mfma_f32_16x16x32_bf16 v[60:63], a[24:27], a[12:15], v[60:63]\n"
      "ds_read_b128 a[40:43], v10 offset:27648\n"
      "ds_read_b128 a[44:47], v10 offset:28672\n"
      "v_mfma_f32_16x16x32_bf16 v[80:83], a[12:15], a[28:31], v[80:83]\n"
      "buffer_load_dwordx4 v[28:31], v5, s[16:19], s66 offen offset:64\n"
      "v_mfma_f32_16x16x32_bf16 v[40:43], a[20:23], a[12:15], v[40:43]\n"
      "ds_read_b128 a[48:51], v10 offset:29696\n"
      "v_cmp_le_u32 s[12:13], v102, v100\n"
      "v_mfma_f32_16x16x32_bf16 v[64:67], a[24:27], a[16:19], v[64:67]\n"
      "v_mov_b32 v100, 0x0c0c0100\n"
      "v_mov_b32 v101, 0x01000c0c\n"
      "v_mfma_f32_16x16x32_bf16 v[84:87], a[16:19], a[28:31], v[84:87]\n"
      "v_cndmask_b32 v100, v100, v101, s[8:9]\n"
      "v_mov_b32 v101, 0x0c0c0302\n"
      "v_mfma_f32_16x16x32_bf16 v[44:47], a[20:23], a[16:19], v[44:47]\n"
      "s_waitcnt vmcnt(11)\n"
      "s_waitcnt lgkmcnt(3)\n"
      "s_barrier\n"
      "buffer_store_dword v23, v21, s[4:7], s72 offen sc1\n"
      "v_mfma_f32_16x16x32_bf16 v[48:51], a[56:59], a[32:35], v[48:51]\n"
      "s_add_u32 m0, s22, 81920\n"
      "ds_read_b64_tr_b16 a[24:25], v7 offset:25600\n"
      "ds_read_b64_tr_b16 a[26:27], v7 offset:26112\n"
      "v_mfma_f32_16x16x32_bf16 v[68:71], a[32:35], a[60:63], v[68:71]\n"
      "s_add_u32 s93, s56, 0\n"
      "s_and_b32 s93, s93, 15\n"
      "s_lshl_b32 s93, s93, 15\n"
      "buffer_load_dwordx4 v88, s[32:35], s93 offen lds\n"
      "v_mfma_f32_16x16x32_bf16 v[52:55], a[56:59], a[36:39], v[52:55]\n"
      "s_add_u32 m0, s22, 86048\n"
      "ds_read_b128 a[0:3], v9 offset:30720\n"
      "ds_read_b64_tr_b16 a[28:29], v7 offset:26624\n"
      "v_mfma_f32_16x16x32_bf16 v[72:75], a[36:39], a[60:63], v[72:75]\n"
      "v_mov_b32 v102, 0x03020c0c\n"
      "v_cndmask_b32 v101, v101, v102, s[8:9]\n"
      "v_mfma_f32_16x16x32_bf16 v[32:35], a[52:55], a[36:39], v[32:35]\n"
      "s_waitcnt lgkmcnt(5)\n"
      "ds_read_b64_tr_b16 a[30:31], v7 offset:27136\n"
      "ds_read_b128 a[4:7], v9 offset:31744\n"
      "v_mfma_f32_16x16x32_bf16 v[56:59], a[56:59], a[40:43], v[56:59]\n"
      "s_add_u32 s93, s56, 1\n"
      "s_and_b32 s93, s93, 15\n"
      "s_lshl_b32 s93, s93, 15\n"
      "buffer_load_dwordx4 v88, s[32:35], s93 offen lds\n"
      "v_mfma_f32_16x16x32_bf16 v[76:79], a[40:43], a[60:63], v[76:79]\n"
      "s_add_u32 m0, s22, 90176\n"
      "ds_read_b64_tr_b16 a[20:21], v7 offset:24576\n"
      "ds_read_b64_tr_b16 a[22:23], v7 offset:25088\n"
      "v_mfma_f32_16x16x32_bf16 v[36:39], a[52:55], a[40:43], v[36:39]\n"
      "v_mov_b32 v102, 0x0c0c0c0c\n"
      "v_cndmask_b32 v212, v100, v102, s[10:11]\n"
      "v_mfma_f32_16x16x32_bf16 v[60:63], a[56:59], a[44:47], v[60:63]\n"
      "ds_read_b128 a[8:11], v9 offset:32768\n"
      "ds_read_b128 a[12:15], v9 offset:33792\n"
      "v_mfma_f32_16x16x32_bf16 v[80:83], a[44:47], a[60:63], v[80:83]\n"
      "s_add_u32 s93, s56, 2\n"
      "s_and_b32 s93, s93, 15\n"
      "s_lshl_b32 s93, s93, 15\n"
      "buffer_load_dwordx4 v88, s[32:35], s93 offen lds\n"
      "v_mfma_f32_16x16x32_bf16 v[40:43], a[52:55], a[44:47], v[40:43]\n"
      "s_waitcnt lgkmcnt(9)\n"
      "ds_read_b128 a[16:19], v9 offset:34816\n"
      "v_cndmask_b32 v213, v101, v102, s[12:13]\n"
      "v_mfma_f32_16x16x32_bf16 v[64:67], a[56:59], a[48:51], v[64:67]\n"
      "s_add_u32 m0, s22, 94304\n"
      "v_cndmask_b32 v214, v102, v100, s[10:11]\n"
      "v_cndmask_b32 v215, v102, v101, s[12:13]\n"
      "v_mfma_f32_16x16x32_bf16 v[84:87], a[48:51], a[60:63], v[84:87]\n"
      "s_add_u32 s93, s56, 3\n"
      "s_and_b32 s93, s93, 15\n"
      "s_lshl_b32 s93, s93, 15\n"
      "buffer_load_dwordx4 v88, s[32:35], s93 offen lds\n"
      "v_mfma_f32_16x16x32_bf16 v[44:47], a[52:55], a[48:51], v[44:47]\n"
      "s_waitcnt lgkmcnt(7)\n"
      "v_mov_b32 v100, s77\n"
      "v_cndmask_b32 v216, v100, 0, s[10:11]\n"
      "v_mfma_f32_16x16x32_bf16 v[48:51], a[24:27], a[0:3], v[48:51]\n"
      "s_waitcnt lgkmcnt(5)\n"
      "ds_read_b64_tr_b16 a[56:57], v7 offset:37888\n"
      "ds_read_b64_tr_b16 a[58:59], v7 offset:38400\n"
      "v_mfma_f32_16x16x32_bf16 v[68:71], a[0:3], a[28:31], v[68:71]\n"
      "s_add_u32 s93, s56, 4\n"
      "s_and_b32 s93, s93, 15\n"
      "s_add_u32 m0, s22, 98432\n"
      "s_lshl_b32 s93, s93, 15\n"
      "buffer_load_dwordx4 v88, s[32:35], s93 offen lds\n"
      "v_mfma_f32_16x16x32_bf16 v[52:55], a[24:27], a[4:7], v[52:55]\n"
      "s_mov_b32 s95, 0x3eb8aa3b\n"
      "ds_read_b128 a[32:35], v10 offset:35840\n"
      "ds_read_b64_tr_b16 a[60:61], v7 offset:38912\n"
      "v_mfma_f32_16x16x32_bf16 v[72:75], a[4:7], a[28:31], v[72:75]\n"
      "s_waitcnt lgkmcnt(6)\n"
      "v_cndmask_b32 v217, v100, 0, s[12:13]\n"
      "v_mfma_f32_16x16x32_bf16 v[32:35], a[20:23], a[4:7], v[32:35]\n"
      "ds_read_b64_tr_b16 a[62:63], v7 offset:39424\n"
      "ds_read_b128 a[36:39], v10 offset:36864\n"
      "v_mfma_f32_16x16x32_bf16 v[56:59], a[24:27], a[8:11], v[56:59]\n"
      "s_add_u32 s93, s64, 0\n"
      "buffer_store_dwordx4 v[16:19], v94, s[4:7], s93 offen sc1\n"
      "v_mfma_f32_16x16x32_bf16 v[76:79], a[8:11], a[28:31], v[76:79]\n"
      "ds_read_b64_tr_b16 a[52:53], v7 offset:36864\n"
      "ds_read_b64_tr_b16 a[54:55], v7 offset:37376\n"
      "v_mfma_f32_16x16x32_bf16 v[36:39], a[20:23], a[8:11], v[36:39]\n"
      "s_waitcnt lgkmcnt(8)\n"
      "v_mfma_f32_16x16x32_bf16 v[60:63], a[24:27], a[12:15], v[60:63]\n"
      "ds_read_b128 a[40:43], v10 offset:37888\n"
      "ds_read_b128 a[44:47], v10 offset:38912\n"
      "v_mfma_f32_16x16x32_bf16 v[80:83], a[12:15], a[28:31], v[80:83]\n"
      "v_mfma_f32_16x16x32_bf16 v[40:43], a[20:23], a[12:15], v[40:43]\n"
      "ds_read_b128 a[48:51], v10 offset:39936\n"
      "v_mfma_f32_16x16x32_bf16 v[64:67], a[24:27], a[16:19], v[64:67]\n"
      "v_mfma_f32_16x16x32_bf16 v[84:87], a[16:19], a[28:31], v[84:87]\n"
      "s_add_u32 s93, s64, 4096\n"
      "buffer_store_dwordx4 v[16:19], v94, s[4:7], s93 offen sc1\n"
      "v_mfma_f32_16x16x32_bf16 v[44:47], a[20:23], a[16:19], v[44:47]\n"
      "s_waitcnt lgkmcnt(7)\n"
      "v_mfma_f32_16x16x32_bf16 v[48:51], a[56:59], a[32:35], v[48:51]\n"
      "s_waitcnt lgkmcnt(5)\n"
      "v_mfma_f32_16x16x32_bf16 v[68:71], a[32:35], a[60:63], v[68:71]\n"
      "v_mfma_f32_16x16x32_bf16 v[52:55], a[56:59], a[36:39], v[52:55]\n"
      "v_mfma_f32_16x16x32_bf16 v[72:75], a[36:39], a[60:63], v[72:75]\n"
      "s_waitcnt lgkmcnt(2)\n"
      "v_mfma_f32_16x16x32_bf16 v[32:35], a[52:55], a[36:39], v[32:35]\n"
      "v_mfma_f32_16x16x32_bf16 v[56:59], a[56:59], a[40:43], v[56:59]\n"
      "v_mfma_f32_16x16x32_bf16 v[76:79], a[40:43], a[60:63], v[76:79]\n"
      "v_mfma_f32_16x16x32_bf16 v[36:39], a[52:55], a[40:43], v[36:39]\n"
      "s_waitcnt lgkmcnt(1)\n"
      "v_mfma_f32_16x16x32_bf16 v[60:63], a[56:59], a[44:47], v[60:63]\n"
      "s_add_u32 s93, s58, 0\n"
      "s_cmp_eq_u32 s59, 0\n"
      "s_cbranch_scc0 .Lsk_8\n"
      "buffer_store_dwordx4 v[16:19], v2, s[4:7], s93 offen sc1\n"
      ".Lsk_8:\n"
      "v_mfma_f32_16x16x32_bf16 v[80:83], a[44:47], a[60:63], v[80:83]\n"
      "v_mfma_f32_16x16x32_bf16 v[40:43], a[52:55], a[44:47], v[40:43]\n"
      "s_waitcnt lgkmcnt(0)\n"
      "v_mfma_f32_16x16x32_bf16 v[64:67], a[56:59], a[48:51], v[64:67]\n"
      "v_mfma_f32_16x16x32_bf16 v[84:87], a[48:51], a[60:63], v[84:87]\n"
      "v_mfma_f32_16x16x32_bf16 v[44:47], a[52:55], a[48:51], v[44:47]\n"
      "s_cmp_eq_u32 s46, 0\n"
      "s_cbranch_scc0 .Lno_13\n"
      "s_waitcnt vmcnt(18)\n"
      "v_lshlrev_b32 v224, 16, v224\n"
      "v_mul_f32 v225, 0xbfb8aa3b, v224\n"
      "v_exp_f32 v225, v225\n"
      "s_nop 0\n"
      "v_add_f32 v225, 1.0, v225\n"
      "v_rcp_f32 v225, v225\n"
      "s_nop 0\n"
      "v_mul_f32 v225, v225, v224\n"
      "v_cvt_pk_bf16_f32 v225, v225, v225\n"
      "ds_write_b16 v219, v225 offset:16384\n"
      "s_waitcnt vmcnt(14)\n"
      "s_waitcnt lgkmcnt(0)\n"
      "s_barrier\n"
      "ds_read_b128 a[0:3], v222 offset:16384\n"
      "ds_read_b64_tr_b16 a[4:5], v220 offset:0\n"
      "ds_read_b64_tr_b16 a[6:7], v220 offset:512\n"
      "ds_read_b64_tr_b16 a[8:9], v221 offset:0\n"
      "ds_read_b64_tr_b16 a[10:11], v221 offset:512\n"
      "ds_read_b128 a[16:19], v222 offset:16448\n"
      "ds_read_b64_tr_b16 a[20:21], v220 offset:4096\n"
      "ds_read_b64_tr_b16 a[22:23], v220 offset:4608\n"
      "ds_read_b64_tr_b16 a[24:25], v221 offset:4096\n"
      "ds_read_b64_tr_b16 a[26:27], v221 offset:4608\n"
      "ds_read_b128 a[32:35], v222 offset:16512\n"
      "ds_read_b64_tr_b16 a[36:37], v220 offset:8192\n"
      "ds_read_b64_tr_b16 a[38:39], v220 offset:8704\n"
      "ds_read_b64_tr_b16 a[40:41], v221 offset:8192\n"
      "ds_read_b64_tr_b16 a[42:43], v221 offset:8704\n"
      "ds_read_b128 a[48:51], v222 offset:16576\n"
      "ds_read_b64_tr_b16 a[52:53], v220 offset:12288\n"
      "ds_read_b64_tr_b16 a[54:55], v220 offset:12800\n"
      "ds_read_b64_tr_b16 a[56:57], v221 offset:12288\n"
      "ds_read_b64_tr_b16 a[58:59], v221 offset:12800\n"
      "s_waitcnt lgkmcnt(15)\n"
      "v_mfma_f32_16x16x32_bf16 v[102:105], a[0:3], a[4:7], 0\n"
      "v_mfma_f32_16x16x32_bf16 v[106:109], a[0:3], a[8:11], 0\n"
      "s_waitcnt lgkmcnt(11)\n"
      "v_mfma_f32_16x16x32_bf16 v[102:105], a[16:19], a[20:23], v[102:105]\n"
      "s_waitcnt lgkmcnt(9)\n"
      "v_mfma_f32_16x16x32_bf16 v[106:109], a[16:19], a[24:27], v[106:109]\n"
      "s_waitcnt lgkmcnt(6)\n"
      "v_mfma_f32_16x16x32_bf16 v[102:105], a[32:35], a[36:39], v[102:105]\n"
      "s_waitcnt lgkmcnt(4)\n"
      "v_mfma_f32_16x16x32_bf16 v[106:109], a[32:35], a[40:43], v[106:109]\n"
      "s_waitcnt lgkmcnt(2)\n"
      "v_mfma_f32_16x16x32_bf16 v[102:105], a[48:51], a[52:55], v[102:105]\n"
      "s_waitcnt lgkmcnt(0)\n"
      "v_mfma_f32_16x16x32_bf16 v[106:109], a[48:51], a[56:59], v[106:109]\n"
      "v_mov_b32 v101, s86\n"
      "s_nop 6\n"
      "v_cvt_pk_bf16_f32 v100, v102, v106\n"
      "buffer_store_dwordx2 v[100:101], v223, s[4:7], s44 offen sc1\n"
      ".Lno_13:\n"
      "v_cvt_pk_bf16_f32 v128, v48, v49\n"
      "v_cvt_pk_bf16_f32 v129, v50, v51\n"
      "v_cvt_pk_bf16_f32 v130, v52, v53\n"
      "v_cvt_pk_bf16_f32 v131, v54, v55\n"
      "v_cvt_pk_bf16_f32 v120, v32, v33\n"
      "v_cvt_pk_bf16_f32 v121, v34, v35\n"
      "v_cvt_pk_bf16_f32 v132, v56, v57\n"
      "v_cvt_pk_bf16_f32 v133, v58, v59\n"
      "v_cvt_pk_bf16_f32 v122, v36, v37\n"
      "v_cvt_pk_bf16_f32 v123, v38, v39\n"
      "v_cvt_pk_bf16_f32 v134, v60, v61\n"
      "v_cvt_pk_bf16_f32 v135, v62, v63\n"
      "v_cvt_pk_bf16_f32 v124, v40, v41\n"
      "v_cvt_pk_bf16_f32 v125, v42, v43\n"
      "v_cvt_pk_bf16_f32 v136, v64, v65\n"
      "v_cvt_pk_bf16_f32 v137, v66, v67\n"
      "v_cvt_pk_bf16_f32 v126, v44, v45\n"
      "v_cvt_pk_bf16_f32 v127, v46, v47\n"
      "s_barrier\n"
      "v_mfma_f32_16x16x16_bf16 v[148:151], v[128:129], v[120:121], 0\n"
      "s_add_u32 s93, s56, 5\n"
      "s_and_b32 s93, s93, 15\n"
      "s_add_u32 m0, s22, 102560\n"
      "s_lshl_b32 s93, s93, 15\n"
      "buffer_load_dwordx4 v88, s[32:35], s93 offen lds\n"
      "v_cvt_pk_bf16_f32 v138, v68, v69\n"
      "v_mfma_f32_16x16x16_bf16 v[152:155], v[130:131], v[120:121], 0\n"
      "s_add_u32 s93, s56, 6\n"
      "v_cvt_pk_bf16_f32 v139, v70, v71\n"
      "v_mfma_f32_16x16x16_bf16 v[156:159], v[130:131], v[122:123], 0\n"
      "s_and_b32 s93, s93, 15\n"
      "v_cvt_pk_bf16_f32 v140, v72, v73\n"
      "v_mfma_f32_16x16x16_bf16 v[160:163], v[132:133], v[122:123], 0\n"
      "s_add_u32 m0, s22, 106688\n"
      "v_cvt_pk_bf16_f32 v141, v74, v75\n"
      "v_cvt_pk_bf16_f32 v142, v76, v77\n"
      "s_lshl_b32 s93, s93, 15\n"
      "buffer_load_dwordx4 v88, s[32:35], s93 offen lds\n"
      "v_mfma_f32_16x16x16_bf16 v[164:167], v[132:133], v[124:125], 0\n"
      "s_add_u32 m0, s22, 110816\n"
      "v_cvt_pk_bf16_f32 v143, v78, v79\n"
      "v_mfma_f32_16x16x16_bf16 v[168:171], v[134:135], v[124:125], 0\n"
      "v_cvt_pk_bf16_f32 v144, v80, v81\n"
      "v_mfma_f32_16x16x16_bf16 v[172:175], v[134:135], v[126:127], 0\n"
      "v_cvt_pk_bf16_f32 v145, v82, v83\n"
      "v_mfma_f32_16x16x16_bf16 v[176:179], v[136:137], v[126:127], 0\n"
      "s_add_u32 s93, s56, 7\n"
      "s_and_b32 s93, s93, 15\n"
      "s_lshl_b32 s93, s93, 15\n"
      "buffer_load_dwordx4 v88, s[32:35], s93 offen lds\n"
      "v_cvt_pk_bf16_f32 v146, v84, v85\n"
      "v_cvt_pk_bf16_f32 v147, v86, v87\n"
      "v_cndmask_b32 v148, v148, v149, s[8:9]\n"
      "v_cndmask_b32 v152, v152, v153, s[8:9]\n"
      "v_cndmask_b32 v180, v148, v152, s[10:11]\n"
      "v_add_f32 v180, v216, v180\n"
      "v_cndmask_b32 v150, v150, v151, s[8:9]\n"
      "v_cndmask_b32 v154, v154, v155, s[8:9]\n"
      "v_cndmask_b32 v181, v150, v154, s[12:13]\n"
      "v_add_f32 v181, v217, v181\n"
      "v_cndmask_b32 v156, v156, v157, s[8:9]\n"
      "v_cndmask_b32 v160, v160, v161, s[8:9]\n"
      "v_cndmask_b32 v182, v156, v160, s[10:11]\n"
      "v_cndmask_b32 v158, v158, v159, s[8:9]\n"
      "v_cndmask_b32 v162, v162, v163, s[8:9]\n"
      "v_cndmask_b32 v183, v158, v162, s[12:13]\n"
      "v_cndmask_b32 v164, v164, v165, s[8:9]\n"
      "v_cndmask_b32 v168, v168, v169, s[8:9]\n"
      "v_cndmask_b32 v184, v164, v168, s[10:11]\n"
      "v_cndmask_b32 v166, v166, v167, s[8:9]\n"
      "v_cndmask_b32 v170, v170, v171, s[8:9]\n"
      "v_cndmask_b32 v185, v166, v170, s[12:13]\n"
      "s_add_u32 s93, s56, 8\n"
      "s_and_b32 s93, s93, 15\n"
      "s_add_u32 m0, s22, 114944\n"
      "s_lshl_b32 s93, s93, 15\n"
      "buffer_load_dwordx4 v88, s[32:35], s93 offen lds\n"
      "v_cndmask_b32 v172, v172, v173, s[8:9]\n"
      "v_cndmask_b32 v176, v176, v177, s[8:9]\n"
      "v_cndmask_b32 v186, v172, v176, s[10:11]\n"
      "v_cndmask_b32 v174, v174, v175, s[8:9]\n"
      "v_cndmask_b32 v178, v178, v179, s[8:9]\n"
      "v_cndmask_b32 v187, v174, v178, s[12:13]\n"
      "v_max_f32 v188, v180, v181\n"
      "v_max_f32 v189, v182, v183\n"
      "v_max_f32 v190, v184, v185\n"
      "v_max_f32 v191, v186, v187\n"
      "v_permlane32_swap_b32 v188, v189\n"
      "s_nop 0\n"
      "v_permlane32_swap_b32 v190, v191\n"
      "v_max_f32 v188, v188, v189\n"
      "v_max_f32 v190, v190, v191\n"
      "s_nop 1\n"
      "v_permlane16_swap_b32 v188, v190\n"
      "v_max_f32 v188, v188, v190\n"
      "v_mul_f32 v188, s95, v188\n"
      "v_mov_b32 v190, v188\n"
      "s_nop 1\n"
      "v_permlane16_swap_b32 v188, v190\n"
      "v_mov_b32 v189, v188\n"
      "v_mov_b32 v191, v190\n"
      "s_nop 0\n"
      "v_permlane32_swap_b32 v188, v189\n"
      "v_permlane32_swap_b32 v190, v191\n"
      "s_add_u32 s93, s56, 9\n"
      "s_and_b32 s93, s93, 15\n"
      "s_add_u32 m0, s22, 119072\n"
      "s_lshl_b32 s93, s93, 15\n"
      "buffer_load_dwordx4 v88, s[32:35], s93 offen lds\n"
      "v_fma_f32 v180, v180, s95, -v188\n"
      "buffer_load_dword v97, v20, s[4:7], s73 offen sc1\n"
      "buffer_load_dword v96, v11, s[4:7], s71 offen sc1\n"
      "v_fma_f32 v181, v181, s95, -v188\n"
      "v_fma_f32 v182, v182, s95, -v189\n"
      "v_fma_f32 v183, v183, s95, -v189\n"
      "v_fma_f32 v184, v184, s95, -v190\n"
      "v_fma_f32 v185, v185, s95, -v190\n"
      "v_fma_f32 v186, v186, s95, -v191\n"
      "v_fma_f32 v187, v187, s95, -v191\n"
      "v_exp_f32 v180, v180\n"
      "v_exp_f32 v181, v181\n"
      "v_exp_f32 v182, v182\n"
      "v_exp_f32 v183, v183\n"
      "v_exp_f32 v184, v184\n"
      "v_exp_f32 v185, v185\n"
      "v_exp_f32 v186, v186\n"
      "s_add_u32 s93, s56, 10\n"
      "s_and_b32 s93, s93, 15\n"
      "s_add_u32 m0, s22, 123200\n"
      "s_lshl_b32 s93, s93, 15\n"
      "buffer_load_dwordx4 v88, s[32:35], s93 offen lds\n"
      "v_exp_f32 v187, v187\n"
      "v_add_f32 v188, v180, v181\n"
      "v_add_f32 v189, v182, v183\n"
      "v_add_f32 v190, v184, v185\n"
      "v_add_f32 v191, v186, v187\n"
      "v_permlane32_swap_b32 v188, v189\n"
      "s_nop 0\n"
      "v_permlane32_swap_b32 v190, v191\n"
      "v_add_f32 v188, v188, v189\n"
      "v_add_f32 v190, v190, v191\n"
      "s_nop 1\n"
      "v_permlane16_swap_b32 v188, v190\n"
      "v_add_f32 v188, v188, v190\n"
      "v_rcp_f32 v188, v188\n"
      "s_nop 0\n"
      "v_mov_b32 v190, v188\n"
      "s_nop 1\n"
      "v_permlane16_swap_b32 v188, v190\n"
      "v_mov_b32 v189, v188\n"
      "v_mov_b32 v191, v190\n"
      "s_nop 0\n"
      "v_permlane32_swap_b32 v188, v189\n"
      "v_permlane32_swap_b32 v190, v191\n"
      "v_mul_f32 v180, v180, v188\n"
      "s_add_u32 s93, s56, 11\n"
      "s_and_b32 s93, s93, 15\n"
      "s_add_u32 m0, s22, 127328\n"
      "s_lshl_b32 s93, s93, 15\n"
      "buffer_load_dwordx4 v88, s[32:35], s93 offen lds\n"
      "v_mul_f32 v181, v181, v188\n"
      "v_mul_f32 v182, v182, v189\n"
      "v_mul_f32 v183, v183, v189\n"
      "v_mul_f32 v184, v184, v190\n"
      "v_mul_f32 v185, v185, v190\n"
      "v_mul_f32 v186, v186, v191\n"
      "v_mul_f32 v187, v187, v191\n"
      "v_cvt_pk_bf16_f32 v188, v180, v181\n"
      "v_cvt_pk_bf16_f32 v189, v182, v183\n"
      "v_cvt_pk_bf16_f32 v190, v184, v185\n"
      "v_cvt_pk_bf16_f32 v191, v186, v187\n"
      "v_perm_b32 v196, v188, v188, v212\n"
      "v_perm_b32 v197, v188, v188, v213\n"
      "v_perm_b32 v198, v188, v188, v214\n"
      "v_perm_b32 v199, v188, v188, v215\n"
      "v_perm_b32 v200, v189, v189, v212\n"
      "v_perm_b32 v201, v189, v189, v213\n"
      "v_mfma_f32_16x16x32_bf16 v[148:151], v[138:141], v[196:199], 0\n"
      "v_perm_b32 v202, v189, v189, v214\n"
      "v_perm_b32 v203, v189, v189, v215\n"
      "v_perm_b32 v204, v190, v190, v212\n"
      "v_perm_b32 v205, v190, v190, v213\n"
      "v_mfma_f32_16x16x32_bf16 v[152:155], v[140:143], v[200:203], 0\n"
      "v_perm_b32 v206, v190, v190, v214\n"
      "v_perm_b32 v207, v190, v190, v215\n"
      "v_perm_b32 v208, v191, v191, v212\n"
      "v_perm_b32 v209, v191, v191, v213\n"
      "v_mfma_f32_16x16x32_bf16 v[156:159], v[142:145], v[204:207], 0\n"
      "s_add_u32 s87, s54, 0\n"
      "v_perm_b32 v210, v191, v191, v214\n"
      "v_perm_b32 v211, v191, v191, v215\n"
      "s_nop 1\n"
      "v_mfma_f32_16x16x32_bf16 v[160:163], v[144:147], v[208:211], 0\n"
      "v_cvt_pk_bf16_f32 v180, v148, v149\n"
      "v_cvt_pk_bf16_f32 v181, v150, v151\n"
      "v_cvt_pk_bf16_f32 v182, v152, v153\n"
      "v_cvt_pk_bf16_f32 v183, v154, v155\n"
      "s_nop 0\n"
      "v_permlane16_swap_b32 v180, v182\n"
      "v_permlane16_swap_b32 v181, v183\n"
      "buffer_store_dwordx4 v[180:183], v89, s[4:7], s87 offen sc1\n"
      "v_cvt_pk_bf16_f32 v184, v156, v157\n"
      "v_cvt_pk_bf16_f32 v185, v158, v159\n"
      "v_cvt_pk_bf16_f32 v186, v160, v161\n"
      "v_cvt_pk_bf16_f32 v187, v162, v163\n"
      "s_nop 0\n"
      "v_permlane16_swap_b32 v184, v186\n"
      "v_permlane16_swap_b32 v185, v187\n"
      "s_add_u32 s87, s54, 65536\n"
      "buffer_store_dwordx4 v[184:187], v89, s[4:7], s87 offen sc1\n"
      "v_accvgpr_read_b32 v127, a255\n"
      "v_accvgpr_read_b32 v9, a254\n"
      "v_accvgpr_read_b32 v10, a253\n"
      "v_accvgpr_read_b32 v104, a252\n"
      "v_accvgpr_read_b32 v7, a251\n"
      "v_accvgpr_read_b32 v8, a250\n"
      "v_accvgpr_read_b32 v124, a249\n"
      "v_accvgpr_read_b32 v125, a248\n"
      "v_mov_b32 v6, v94\n"
      "s_waitcnt vmcnt(4)\n"
      "v_xor_b32 v98, s86, v96\n"
      "v_bitop3_b32 v98, v98, v97, s86 bitop3:0xf6\n"
      "v_cmp_ne_u32 vcc, 0, v98\n"
      "s_cbranch_vccnz .Lretry_14\n"
      ".Lback_15:\n"
      "s_waitcnt vmcnt(2)\n"
      "s_barrier\n"
      "buffer_store_dword v23, v22, s[4:7], s70 offen sc1\n"
      "s_mov_b32 s95, 0xbfb8aa3b\n"
      "ds_read_b64_tr_b16 a[0:1], v9 offset:0\n"
      "ds_read_b64_tr_b16 a[2:3], v9 offset:512\n"
      "s_add_u32 m0, s21, 2048\n"
      "s_add_u32 s94, s63, 0\n"
      "buffer_load_dwordx4 v3, s[4:7], s94 offen offset:0 sc1 lds\n"
      "ds_read_b64_tr_b16 a[4:5], v9 offset:8\n"
      "ds_read_b64_tr_b16 a[6:7], v9 offset:520\n"
      "ds_read_b64_tr_b16 a[8:9], v10 offset:0\n"
      "ds_read_b64_tr_b16 a[10:11], v10 offset:512\n"
      "ds_read_b64_tr_b16 a[12:13], v10 offset:8\n"
      "ds_read_b64_tr_b16 a[14:15], v10 offset:520\n"
      "ds_read_b64_tr_b16 a[16:17], v9 offset:4128\n"
      "ds_read_b64_tr_b16 a[18:19], v9 offset:4640\n"
      "ds_read_b64_tr_b16 a[20:21], v9 offset:4136\n"
      "buffer_load_dwordx4 v3, s[4:7], s94 offen offset:2048 sc1 lds\n"
      "ds_read_b64_tr_b16 a[22:23], v9 offset:4648\n"
      "ds_read_b64_tr_b16 a[24:25], v10 offset:4128\n"
      "ds_read_b64_tr_b16 a[26:27], v10 offset:4640\n"
      "ds_read_b64_tr_b16 a[28:29], v10 offset:4136\n"
      "ds_read_b64_tr_b16 a[30:31], v10 offset:4648\n"
      "ds_read_b64_tr_b16 a[32:33], v9 offset:8256\n"
      "ds_read_b64_tr_b16 a[34:35], v9 offset:8768\n"
      "ds_read_b64_tr_b16 a[36:37], v9 offset:8264\n"
      "ds_read_b64_tr_b16 a[38:39], v9 offset:8776\n"
      "s_add_u32 m0, s21, 6144\n"
      "s_add_u32 s94, s94, 4096\n"
      "buffer_load_dwordx4 v3, s[4:7], s94 offen offset:0 sc1 lds\n"
      "ds_read_b64_tr_b16 a[40:41], v10 offset:8256\n"
      "ds_read_b64_tr_b16 a[42:43], v10 offset:8768\n"
      "ds_read_b64_tr_b16 a[44:45], v10 offset:8264\n"
      "ds_read_b64_tr_b16 a[46:47], v10 offset:8776\n"
      "ds_read_b64_tr_b16 a[48:49], v9 offset:12384\n"
      "ds_read_b64_tr_b16 a[50:51], v9 offset:12896\n"
      "ds_read_b64_tr_b16 a[52:53], v9 offset:12392\n"
      "ds_read_b64_tr_b16 a[54:55], v9 offset:12904\n"
      "ds_read_b64_tr_b16 a[56:57], v10 offset:12384\n"
      "buffer_load_dwordx4 v3, s[4:7], s94 offen offset:2048 sc1 lds\n"
      "ds_read_b64_tr_b16 a[58:59], v10 offset:12896\n"
      "ds_read_b64_tr_b16 a[60:61], v10 offset:12392\n"
      "ds_read_b64_tr_b16 a[62:63], v10 offset:12904\n"
      "ds_read_b64_tr_b16 a[64:65], v9 offset:16512\n"
      "ds_read_b64_tr_b16 a[66:67], v9 offset:17024\n"
      "ds_read_b64_tr_b16 a[68:69], v9 offset:16520\n"
      "ds_read_b64_tr_b16 a[70:71], v9 offset:17032\n"
      "ds_read_b64_tr_b16 a[72:73], v10 offset:16512\n"
      "ds_read_b64_tr_b16 a[74:75], v10 offset:17024\n"
      "ds_read_b64_tr_b16 a[76:77], v10 offset:16520\n"
      "ds_read_b64_tr_b16 a[78:79], v10 offset:17032\n"
      "ds_read_b64_tr_b16 a[80:81], v9 offset:20640\n"
      "ds_read_b64_tr_b16 a[82:83], v9 offset:21152\n"
      "s_add_u32 m0, s21, 10240\n"
      "s_add_u32 s94, s94, 4096\n"
      "buffer_load_dwordx4 v3, s[4:7], s94 offen offset:0 sc1 lds\n"
      "ds_read_b64_tr_b16 a[84:85], v9 offset:20648\n"
      "ds_read_b64_tr_b16 a[86:87], v9 offset:21160\n"
      "ds_read_b64_tr_b16 a[88:89], v10 offset:20640\n"
      "ds_read_b64_tr_b16 a[90:91], v10 offset:21152\n"
      "ds_read_b64_tr_b16 a[92:93], v10 offset:20648\n"
      "ds_read_b64_tr_b16 a[94:95], v10 offset:21160\n"
      "ds_read_b64_tr_b16 a[96:97], v9 offset:24768\n"
      "ds_read_b64_tr_b16 a[98:99], v9 offset:25280\n"
      "ds_read_b64_tr_b16 a[100:101], v9 offset:24776\n"
      "ds_read_b64_tr_b16 a[102:103], v9 offset:25288\n"
      "ds_read_b64_tr_b16 a[104:105], v10 offset:24768\n"
      "ds_read_b64_tr_b16 a[106:107], v10 offset:25280\n"
      "ds_read_b64_tr_b16 a[108:109], v10 offset:24776\n"
      "buffer_load_dwordx4 v3, s[4:7], s94 offen offset:2048 sc1 lds\n"
      "ds_read_b64_tr_b16 a[110:111], v10 offset:25288\n"
      "ds_read_b64_tr_b16 a[112:113], v9 offset:28896\n"
      "ds_read_b64_tr_b16 a[114:115], v9 offset:29408\n"
      "ds_read_b64_tr_b16 a[116:117], v9 offset:28904\n"
      "ds_read_b64_tr_b16 a[118:119], v9 offset:29416\n"
      "buffer_load_dwordx4 v[100:103], v127, s[4:7], s67 offen sc1\n"
      "ds_read_b64_tr_b16 a[120:121], v10 offset:28896\n"
      "ds_read_b64_tr_b16 a[122:123], v10 offset:29408\n"
      "ds_read_b64_tr_b16 a[124:125], v10 offset:28904\n"
      "ds_read_b64_tr_b16 a[126:127], v10 offset:29416\n"
      "ds_read_b64_tr_b16 a[128:129], v9 offset:33024\n"
      "ds_read_b64_tr_b16 a[130:131], v9 offset:33536\n"
      "ds_read_b64_tr_b16 a[132:133], v9 offset:33032\n"
      "ds_read_b64_tr_b16 a[134:135], v9 offset:33544\n"
      "ds_read_b64_tr_b16 a[136:137], v10 offset:33024\n"
      "ds_read_b64_tr_b16 a[138:139], v10 offset:33536\n"
      "ds_read_b64_tr_b16 a[140:141], v10 offset:33032\n"
      "ds_read_b64_tr_b16 a[142:143], v10 offset:33544\n"
      "ds_read_b64_tr_b16 a[144:145], v9 offset:37152\n"
      "s_add_u32 s93, s58, 8192\n"
      "buffer_store_dwordx4 v[16:19], v2, s[4:7], s93 offen sc1\n"
      "ds_read_b64_tr_b16 a[146:147], v9 offset:37664\n"
      "ds_read_b64_tr_b16 a[148:149], v9 offset:37160\n"
      "ds_read_b64_tr_b16 a[150:151], v9 offset:37672\n"
      "ds_read_b64_tr_b16 a[152:153], v10 offset:37152\n"
      "ds_read_b64_tr_b16 a[154:155], v10 offset:37664\n"
      "ds_read_b64_tr_b16 a[156:157], v10 offset:37160\n"
      "ds_read_b64_tr_b16 a[158:159], v10 offset:37672\n"
      "ds_read_b64_tr_b16 a[160:161], v9 offset:41280\n"
      "ds_read_b64_tr_b16 a[162:163], v9 offset:41792\n"
      "ds_read_b64_tr_b16 a[164:165], v9 offset:41288\n"
      "ds_read_b64_tr_b16 a[166:167], v9 offset:41800\n"
      "ds_read_b64_tr_b16 a[168:169], v10 offset:41280\n"
      "ds_read_b64_tr_b16 a[170:171], v10 offset:41792\n"
      "ds_read_b64_tr_b16 a[172:173], v10 offset:41288\n"
      "ds_read_b64_tr_b16 a[174:175], v10 offset:41800\n"
      "ds_read_b64_tr_b16 a[176:177], v9 offset:45408\n"
      "ds_read_b64_tr_b16 a[178:179], v9 offset:45920\n"
      "ds_read_b64_tr_b16 a[180:181], v9 offset:45416\n"
      "ds_read_b64_tr_b16 a[182:183], v9 offset:45928\n"
      "ds_read_b64_tr_b16 a[184:185], v10 offset:45408\n"
      "ds_read_b64_tr_b16 a[186:187], v10 offset:45920\n"
      "ds_read_b64_tr_b16 a[188:189], v10 offset:45416\n"
      "ds_read_b64_tr_b16 a[190:191], v10 offset:45928\n"
      "ds_read_b64_tr_b16 a[192:193], v9 offset:49536\n"
      "ds_read_b64_tr_b16 a[194:195], v9 offset:50048\n"
      "ds_read_b64_tr_b16 a[196:197], v9 offset:49544\n"
      "ds_read_b64_tr_b16 a[198:199], v9 offset:50056\n"
      "ds_read_b64_tr_b16 a[200:201], v10 offset:49536\n"
      "ds_read_b64_tr_b16 a[202:203], v10 offset:50048\n"
      "ds_read_b64_tr_b16 a[204:205], v10 offset:49544\n"
      "ds_read_b64_tr_b16 a[206:207], v10 offset:50056\n"
      "ds_read_b64_tr_b16 a[208:209], v9 offset:53664\n"
      "ds_read_b64_tr_b16 a[210:211], v9 offset:54176\n"
      "ds_read_b64_tr_b16 a[212:213], v9 offset:53672\n"
      "ds_read_b64_tr_b16 a[214:215], v9 offset:54184\n"
      "ds_read_b64_tr_b16 a[216:217], v10 offset:53664\n"
      "ds_read_b64_tr_b16 a[218:219], v10 offset:54176\n"
      "ds_read_b64_tr_b16 a[220:221], v10 offset:53672\n"
      "ds_read_b64_tr_b16 a[222:223], v10 offset:54184\n"
      "ds_read_b64_tr_b16 a[224:225], v9 offset:57792\n"
      "ds_read_b64_tr_b16 a[226:227], v9 offset:58304\n"
      "ds_read_b64_tr_b16 a[228:229], v9 offset:57800\n"
      "s_add_u32 m0, s21, 14336\n"
      "s_add_u32 s94, s94, 4096\n"
      "buffer_load_dwordx4 v3, s[4:7], s94 offen offset:0 sc1 lds\n"
      "ds_read_b64_tr_b16 a[230:231], v9 offset:58312\n"
      "ds_read_b64_tr_b16 a[232:233], v10 offset:57792\n"
      "ds_read_b64_tr_b16 a[234:235], v10 offset:58304\n"
      "ds_read_b64_tr_b16 a[236:237], v10 offset:57800\n"
      "ds_read_b64_tr_b16 a[238:239], v10 offset:58312\n"
      "ds_read_b64_tr_b16 a[240:241], v9 offset:61920\n"
      "ds_read_b64_tr_b16 a[242:243], v9 offset:62432\n"
      "ds_read_b64_tr_b16 a[244:245], v9 offset:61928\n"
      "ds_read_b64_tr_b16 a[246:247], v9 offset:62440\n"
      "ds_read_b64_tr_b16 a[248:249], v10 offset:61920\n"
      "ds_read_b64_tr_b16 a[250:251], v10 offset:62432\n"
      "ds_read_b64_tr_b16 a[252:253], v10 offset:61928\n"
      "ds_read_b64_tr_b16 a[254:255], v10 offset:62440\n"
      "buffer_load_dwordx4 v3, s[4:7], s94 offen offset:2048 sc1 lds\n"
      "s_waitcnt vmcnt(11)\n"
      "s_sleep 2\n"
      "s_add_u32 s89, s56, 0\n"
      "s_and_b32 s89, s89, 15\n"
      "s_lshl_b32 s89, s89, 11\n"
      "s_add_u32 s89, s89, s55\n"
      "buffer_load_dwordx4 v[128:131], v2, s[4:7], s89 offen offset:0 sc1\n"
      "v_lshlrev_b32 v80, 16, v24\n"
      "v_and_b32 v81, s83, v24\n"
      "v_lshlrev_b32 v82, 16, v25\n"
      "v_and_b32 v83, s83, v25\n"
      "buffer_load_dwordx4 v[132:135], v2, s[4:7], s89 offen offset:2048 sc1\n"
      "v_lshlrev_b32 v84, 16, v26\n"
      "v_and_b32 v85, s83, v26\n"
      "v_lshlrev_b32 v86, 16, v27\n"
      "v_and_b32 v87, s83, v27\n"
      "s_add_u32 s89, s89, 4096\n"
      "buffer_load_dwordx4 v[136:139], v2, s[4:7], s89 offen offset:0 sc1\n"
      "v_lshlrev_b32 v88, 16, v28\n"
      "v_and_b32 v89, s83, v28\n"
      "v_lshlrev_b32 v90, 16, v29\n"
      "v_and_b32 v91, s83, v29\n"
      "v_lshlrev_b32 v92, 16, v30\n"
      "buffer_load_dwordx4 v[140:143], v2, s[4:7], s89 offen offset:2048 sc1\n"
      "v_and_b32 v93, s83, v30\n"
      "v_lshlrev_b32 v94, 16, v31\n"
      "v_and_b32 v95, s83, v31\n"
      "s_waitcnt vmcnt(7)\n"
      "v_xor_b32 v105, s86, v101\n"
      "s_add_u32 s89, s56, 4\n"
      "s_and_b32 s89, s89, 15\n"
      "s_lshl_b32 s89, s89, 11\n"
      "s_add_u32 s89, s89, s55\n"
      "buffer_load_dwordx4 v[144:147], v2, s[4:7], s89 offen offset:0 sc1\n"
      "v_bitop3_b32 v105, v105, v103, s86 bitop3:0xf6\n"
      "v_cmp_ne_u32 vcc, 0, v105\n"
      "s_cbranch_vccnz .Lretry_16\n"
      ".Lback_17:\n"
      "v_and_b32 v101, s83, v100\n"
      "v_lshlrev_b32 v100, 16, v100\n"
      "v_and_b32 v103, s83, v102\n"
      "buffer_load_dwordx4 v[148:151], v2, s[4:7], s89 offen offset:2048 sc1\n"
      "v_lshlrev_b32 v102, 16, v102\n"
      "s_nop 1\n"
      "v_permlane32_swap_b32 v100, v102\n"
      "v_permlane32_swap_b32 v101, v103\n"
      "v_add_f32 v100, v100, v102\n"
      "s_add_u32 s89, s89, 4096\n"
      "buffer_load_dwordx4 v[152:155], v2, s[4:7], s89 offen offset:0 sc1\n"
      "v_add_f32 v101, v101, v103\n"
      "s_nop 1\n"
      "v_permlane16_swap_b32 v100, v101\n"
      "v_add_f32 v100, v100, v101\n"
      "v_cvt_pk_bf16_f32 v100, v100, v100\n"
      "v_lshlrev_b32 v100, 16, v100\n"
      "buffer_load_dwordx4 v[156:159], v2, s[4:7], s89 offen offset:2048 sc1\n"
      "v_add_f32 v100, s81, v100\n"
      "v_cvt_pk_bf16_f32 v100, v100, v100\n"
      "v_lshlrev_b32 v100, 16, v100\n"
      "ds_write_b32 v104, v100\n"
      "s_add_u32 s89, s56, 8\n"
      "s_and_b32 s89, s89, 15\n"
      "s_lshl_b32 s89, s89, 11\n"
      "s_add_u32 s89, s89, s55\n"
      "buffer_load_dwordx4 v[160:163], v2, s[4:7], s89 offen offset:0 sc1\n"
      "v_lshlrev_b32 v99, 4, v12\n"
      "v_add_u32 v99, 155648, v99\n"
      "v_lshlrev_b32 v9, 4, v1\n"
      "v_add_u32 v10, 81920, v9\n"
      "v_add_u32 v9, 2048, v9\n"
      "s_waitcnt lgkmcnt(0)\n"
      "s_barrier\n"
      "s_waitcnt vmcnt(8)\n"
      "v_max_u32 v107, v128, v129\n"
      "v_max3_u32 v107, v107, v130, v131\n"
      "s_waitcnt vmcnt(7)\n"
      "v_max3_u32 v107, v107, v132, v133\n"
      "v_max3_u32 v107, v107, v134, v135\n"
      "buffer_load_dwordx4 v[164:167], v2, s[4:7], s89 offen offset:2048 sc1\n"
      "s_waitcnt vmcnt(7)\n"
      "v_max3_u32 v107, v107, v136, v137\n"
      "v_max3_u32 v107, v107, v138, v139\n"
      "s_waitcnt vmcnt(6)\n"
      "v_max3_u32 v107, v107, v140, v141\n"
      "v_max3_u32 v107, v107, v142, v143\n"
      "s_add_u32 s89, s89, 4096\n"
      "buffer_load_dwordx4 v[168:171], v2, s[4:7], s89 offen offset:0 sc1\n"
      "v_cmp_eq_u32 vcc, -1, v107\n"
      "s_cbranch_vccnz .Lretry_18\n"
      ".Lback_19:\n"
      "v_mfma_f32_16x16x32_bf16 v[64:67], a[0:3], v[128:131], 0\n"
      "buffer_load_dwordx4 v[172:175], v2, s[4:7], s89 offen offset:2048 sc1\n"
      "v_mfma_f32_16x16x32_bf16 v[68:71], a[4:7], v[128:131], 0\n"
      "ds_read_b128 v[108:111], v99 offset:0\n"
      "ds_read_b128 v[112:115], v99 offset:1024\n"
      "v_mfma_f32_16x16x32_bf16 v[72:75], a[8:11], v[128:131], 0\n"
      "ds_read_b128 v[116:119], v99 offset:64\n"
      "ds_read_b128 v[120:123], v99 offset:1088\n"
      "v_mfma_f32_16x16x32_bf16 v[76:79], a[12:15], v[128:131], 0\n"
      "s_waitcnt vmcnt(7)\n"
      "v_max_u32 v107, v144, v145\n"
      "v_max3_u32 v107, v107, v146, v147\n"
      "v_mfma_f32_16x16x32_bf16 v[64:67], a[16:19], v[132:135], v[64:67]\n"
      "s_add_u32 s89, s56, 12\n"
      "s_and_b32 s89, s89, 15\n"
      "s_lshl_b32 s89, s89, 11\n"
      "s_add_u32 s89, s89, s55\n"
      "buffer_load_dwordx4 v[176:179], v2, s[4:7], s89 offen offset:0 sc1\n"
      "v_mfma_f32_16x16x32_bf16 v[68:71], a[20:23], v[132:135], v[68:71]\n"
      "ds_read_b128 v[32:35], v99 offset:128\n"
      "ds_read_b128 v[36:39], v99 offset:1152\n"
      "v_mfma_f32_16x16x32_bf16 v[72:75], a[24:27], v[132:135], v[72:75]\n"
      "ds_read_b128 v[40:43], v99 offset:192\n"
      "ds_read_b128 v[44:47], v99 offset:1216\n"
      "v_mfma_f32_16x16x32_bf16 v[76:79], a[28:31], v[132:135], v[76:79]\n"
      "s_waitcnt vmcnt(7)\n"
      "v_max3_u32 v107, v107, v148, v149\n"
      "v_max3_u32 v107, v107, v150, v151\n"
      "v_mfma_f32_16x16x32_bf16 v[64:67], a[32:35], v[136:139], v[64:67]\n"
      "buffer_load_dwordx4 v[180:183], v2, s[4:7], s89 offen offset:2048 sc1\n"
      "v_mfma_f32_16x16x32_bf16 v[68:71], a[36:39], v[136:139], v[68:71]\n"
      "ds_read_b128 v[48:51], v99 offset:256\n"
      "ds_read_b128 v[52:55], v99 offset:1280\n"
      "v_mfma_f32_16x16x32_bf16 v[72:75], a[40:43], v[136:139], v[72:75]\n"
      "ds_read_b128 v[56:59], v99 offset:320\n"
      "ds_read_b128 v[60:63], v99 offset:1344\n"
      "v_mfma_f32_16x16x32_bf16 v[76:79], a[44:47], v[136:139], v[76:79]\n"
      "v_mfma_f32_16x16x32_bf16 v[64:67], a[48:51], v[140:143], v[64:67]\n"
      "s_add_u32 s89, s89, 4096\n"
      "buffer_load_dwordx4 v[184:187], v2, s[4:7], s89 offen offset:0 sc1\n"
      "v_mfma_f32_16x16x32_bf16 v[68:71], a[52:55], v[140:143], v[68:71]\n"
      "s_waitcnt vmcnt(8)\n"
      "v_max3_u32 v107, v107, v152, v153\n"
      "v_max3_u32 v107, v107, v154, v155\n"
      "v_mfma_f32_16x16x32_bf16 v[72:75], a[56:59], v[140:143], v[72:75]\n"
      "v_mfma_f32_16x16x32_bf16 v[76:79], a[60:63], v[140:143], v[76:79]\n"
      "s_waitcnt vmcnt(7)\n"
      "v_max3_u32 v107, v107, v156, v157\n"
      "v_max3_u32 v107, v107, v158, v159\n"
      "v_cmp_eq_u32 vcc, -1, v107\n"
      "s_cbranch_vccnz .Lretry_20\n"
      ".Lback_21:\n"
      "v_mfma_f32_16x16x32_bf16 v[64:67], a[64:67], v[144:147], v[64:67]\n"
      "buffer_load_dwordx4 v[188:191], v2, s[4:7], s89 offen offset:2048 sc1\n"
      "v_mfma_f32_16x16x32_bf16 v[68:71], a[68:71], v[144:147], v[68:71]\n"
      "v_mfma_f32_16x16x32_bf16 v[72:75], a[72:75], v[144:147], v[72:75]\n"
      "v_mfma_f32_16x16x32_bf16 v[76:79], a[76:79], v[144:147], v[76:79]\n"
      "s_add_u32 s94, s63, 65536\n"
      "v_mfma_f32_16x16x32_bf16 v[64:67], a[80:83], v[148:151], v[64:67]\n"
      "s_waitcnt vmcnt(7)\n"
      "v_max_u32 v107, v160, v161\n"
      "v_max3_u32 v107, v107, v162, v163\n"
      "v_mfma_f32_16x16x32_bf16 v[68:71], a[84:87], v[148:151], v[68:71]\n"
      "v_mfma_f32_16x16x32_bf16 v[72:75], a[88:91], v[148:151], v[72:75]\n"
      "v_mfma_f32_16x16x32_bf16 v[76:79], a[92:95], v[148:151], v[76:79]\n"
      "s_add_u32 m0, s21, 3072\n"
      "s_nop 0\n"
      "buffer_load_dwordx4 v2, s[4:7], s94 offen offset:0 sc1 lds\n"
      "v_mfma_f32_16x16x32_bf16 v[64:67], a[96:99], v[152:155], v[64:67]\n"
      "v_mfma_f32_16x16x32_bf16 v[68:71], a[100:103], v[152:155], v[68:71]\n"
      "v_mfma_f32_16x16x32_bf16 v[72:75], a[104:107], v[152:155], v[72:75]\n"
      "buffer_load_dwordx4 v2, s[4:7], s94 offen offset:2048 sc1 lds\n"
      "v_mfma_f32_16x16x32_bf16 v[76:79], a[108:111], v[152:155], v[76:79]\n"
      "s_add_u32 s94, s94, 4096\n"
      "v_mfma_f32_16x16x32_bf16 v[64:67], a[112:115], v[156:159], v[64:67]\n"
      "v_mfma_f32_16x16x32_bf16 v[68:71], a[116:119], v[156:159], v[68:71]\n"
      "v_mfma_f32_16x16x32_bf16 v[72:75], a[120:123], v[156:159], v[72:75]\n"
      "s_waitcnt vmcnt(8)\n"
      "v_max3_u32 v107, v107, v164, v165\n"
      "v_max3_u32 v107, v107, v166, v167\n"
      "v_mfma_f32_16x16x32_bf16 v[76:79], a[124:127], v[156:159], v[76:79]\n"
      "s_add_u32 m0, s21, 7168\n"
      "s_nop 0\n"
      "buffer_load_dwordx4 v2, s[4:7], s94 offen offset:0 sc1 lds\n"
      "buffer_load_dwordx4 v2, s[4:7], s94 offen offset:2048 sc1 lds\n"
      "s_waitcnt vmcnt(9)\n"
      "v_max3_u32 v107, v107, v168, v169\n"
      "v_max3_u32 v107, v107, v170, v171\n"
      "s_add_u32 m0, s21, 11264\n"
      "s_add_u32 s94, s94, 4096\n"
      "buffer_load_dwordx4 v2, s[4:7], s94 offen offset:0 sc1 lds\n"
      "s_waitcnt vmcnt(9)\n"
      "v_max3_u32 v107, v107, v172, v173\n"
      "v_max3_u32 v107, v107, v174, v175\n"
      "v_cmp_eq_u32 vcc, -1, v107\n"
      "s_cbranch_vccnz .Lretry_22\n"
      ".Lback_23:\n"
      "v_mfma_f32_16x16x32_bf16 v[64:67], a[128:131], v[160:163], v[64:67]\n"
      "buffer_load_dwordx4 v2, s[4:7], s94 offen offset:2048 sc1 lds\n"
      "v_mfma_f32_16x16x32_bf16 v[68:71], a[132:135], v[160:163], v[68:71]\n"
      "v_mfma_f32_16x16x32_bf16 v[72:75], a[136:139], v[160:163], v[72:75]\n"
      "v_mfma_f32_16x16x32_bf16 v[76:79], a[140:143], v[160:163], v[76:79]\n"
      "v_mfma_f32_16x16x32_bf16 v[64:67], a[144:147], v[164:167], v[64:67]\n"
      "s_waitcnt vmcnt(9)\n"
      "v_max_u32 v107, v176, v177\n"
      "v_max3_u32 v107, v107, v178, v179\n"
      "v_mfma_f32_16x16x32_bf16 v[68:71], a[148:151], v[164:167], v[68:71]\n"
      "v_mfma_f32_16x16x32_bf16 v[72:75], a[152:155], v[164:167], v[72:75]\n"
      "s_add_u32 s93, s58, 24576\n"
      "buffer_store_dwordx4 v[16:19], v2, s[4:7], s93 offen sc1\n"
      "v_mfma_f32_16x16x32_bf16 v[76:79], a[156:159], v[164:167], v[76:79]\n"
      "v_mfma_f32_16x16x32_bf16 v[64:67], a[160:163], v[168:171], v[64:67]\n"
      "v_mfma_f32_16x16x32_bf16 v[68:71], a[164:167], v[168:171], v[68:71]\n"
      "s_waitcnt vmcnt(9)\n"
      "v_max3_u32 v107, v107, v180, v181\n"
      "v_max3_u32 v107, v107, v182, v183\n"
      "v_mfma_f32_16x16x32_bf16 v[72:75], a[168:171], v[168:171], v[72:75]\n"
      "v_mfma_f32_16x16x32_bf16 v[76:79], a[172:175], v[168:171], v[76:79]\n"
      "v_mfma_f32_16x16x32_bf16 v[64:67], a[176:179], v[172:175], v[64:67]\n"
      "v_mfma_f32_16x16x32_bf16 v[68:71], a[180:183], v[172:175], v[68:71]\n"
      "v_mfma_f32_16x16x32_bf16 v[72:75], a[184:187], v[172:175], v[72:75]\n"
      "s_waitcnt vmcnt(8)\n"
      "v_max3_u32 v107, v107, v184, v185\n"
      "v_max3_u32 v107, v107, v186, v187\n"
      "v_mfma_f32_16x16x32_bf16 v[76:79], a[188:191], v[172:175], v[76:79]\n"
      "s_waitcnt vmcnt(7)\n"
      "v_max3_u32 v107, v107, v188, v189\n"
      "v_max3_u32 v107, v107, v190, v191\n"
      "v_cmp_eq_u32 vcc, -1, v107\n"
      "s_cbranch_vccnz .Lretry_24\n"
      ".Lback_25:\n"
      "v_mfma_f32_16x16x32_bf16 v[64:67], a[192:195], v[176:179], v[64:67]\n"
      "s_add_u32 s94, s94, 4096\n"
      "v_mfma_f32_16x16x32_bf16 v[68:71], a[196:199], v[176:179], v[68:71]\n"
      "v_mfma_f32_16x16x32_bf16 v[72:75], a[200:203], v[176:179], v[72:75]\n"
      "v_mfma_f32_16x16x32_bf16 v[76:79], a[204:207], v[176:179], v[76:79]\n"
      "v_mfma_f32_16x16x32_bf16 v[64:67], a[208:211], v[180:183], v[64:67]\n"
      "s_add_u32 m0, s21, 15360\n"
      "s_nop 0\n"
      "buffer_load_dwordx4 v2, s[4:7], s94 offen offset:0 sc1 lds\n"
      "v_mfma_f32_16x16x32_bf16 v[68:71], a[212:215], v[180:183], v[68:71]\n"
      "v_mfma_f32_16x16x32_bf16 v[72:75], a[216:219], v[180:183], v[72:75]\n"
      "v_mfma_f32_16x16x32_bf16 v[76:79], a[220:223], v[180:183], v[76:79]\n"
      "v_mfma_f32_16x16x32_bf16 v[64:67], a[224:227], v[184:187], v[64:67]\n"
      "v_mfma_f32_16x16x32_bf16 v[68:71], a[228:231], v[184:187], v[68:71]\n"
      "v_mfma_f32_16x16x32_bf16 v[72:75], a[232:235], v[184:187], v[72:75]\n"
      "buffer_load_dwordx4 v2, s[4:7], s94 offen offset:2048 sc1 lds\n"
      "v_mfma_f32_16x16x32_bf16 v[76:79], a[236:239], v[184:187], v[76:79]\n"
      "s_add_u32 m0, s21, 81920\n"
      "v_mfma_f32_16x16x32_bf16 v[64:67], a[240:243], v[188:191], v[64:67]\n"
      "s_add_u32 s94, s63, 131072\n"
      "v_mfma_f32_16x16x32_bf16 v[68:71], a[244:247], v[188:191], v[68:71]\n"
      "v_mfma_f32_16x16x32_bf16 v[72:75], a[248:251], v[188:191], v[72:75]\n"
      "v_mfma_f32_16x16x32_bf16 v[76:79], a[252:255], v[188:191], v[76:79]\n"
      "s_waitcnt lgkmcnt(11)\n"
      "v_pk_fma_f32 v[64:65], v[64:65], v[108:109], v[80:81]\n"
      "buffer_load_dwordx4 v2, s[4:7], s94 offen offset:0 sc1 lds\n"
      "v_pk_fma_f32 v[66:67], v[66:67], v[110:111], v[82:83]\n"
      "s_waitcnt lgkmcnt(10)\n"
      "v_pk_fma_f32 v[68:69], v[68:69], v[112:113], v[84:85]\n"
      "v_pk_fma_f32 v[70:71], v[70:71], v[114:115], v[86:87]\n"
      "s_waitcnt lgkmcnt(9)\n"
      "v_pk_fma_f32 v[72:73], v[72:73], v[116:117], v[88:89]\n"
      "v_pk_fma_f32 v[74:75], v[74:75], v[118:119], v[90:91]\n"
      "s_waitcnt lgkmcnt(8)\n"
      "v_pk_fma_f32 v[76:77], v[76:77], v[120:121], v[92:93]\n"
      "v_pk_fma_f32 v[78:79], v[78:79], v[122:123], v[94:95]\n"
      "v_cvt_pk_bf16_f32 v24, v64, v65\n"
      "v_cvt_pk_bf16_f32 v25, v66, v67\n"
      "v_cvt_pk_bf16_f32 v26, v68, v69\n"
      "v_cvt_pk_bf16_f32 v27, v70, v71\n"
      "v_cvt_pk_bf16_f32 v28, v72, v73\n"
      "v_cvt_pk_bf16_f32 v29, v74, v75\n"
      "v_cvt_pk_bf16_f32 v30, v76, v77\n"
      "v_cvt_pk_bf16_f32 v31, v78, v79\n"
      "buffer_load_dwordx4 v2, s[4:7], s94 offen offset:2048 sc1 lds\n"
      "v_dot2_f32_bf16 v96, v24, s82, 0\n"
      "v_dot2_f32_bf16 v98, v24, v24, 0\n"
      "s_nop 1\n"
      "v_dot2c_f32_bf16 v96, s82, v25\n"
      "v_dot2c_f32_bf16 v98, v25, v25\n"
      "v_dot2c_f32_bf16 v96, s82, v26\n"
      "v_dot2c_f32_bf16 v98, v26, v26\n"
      "v_dot2c_f32_bf16 v96, s82, v27\n"
      "v_dot2c_f32_bf16 v98, v27, v27\n"
      "v_dot2c_f32_bf16 v96, s82, v28\n"
      "v_dot2c_f32_bf16 v98, v28, v28\n"
      "v_dot2c_f32_bf16 v96, s82, v29\n"
      "v_dot2c_f32_bf16 v98, v29, v29\n"
      "v_dot2c_f32_bf16 v96, s82, v30\n"
      "v_dot2c_f32_bf16 v98, v30, v30\n"
      "v_dot2c_f32_bf16 v96, s82, v31\n"
      "v_dot2c_f32_bf16 v98, v31, v31\n"
      "v_mov_b32 v97, s86\n"
      "s_nop 1\n"
      "v_permlane32_swap_b32 v96, v98\n"
      "v_add_f32 v96, v96, v98\n"
      "v_mov_b32 v98, v96\n"
      "s_nop 1\n"
      "v_permlane16_swap_b32 v96, v98\n"
      "s_add_u32 m0, s21, 86016\n"
      "s_add_u32 s94, s94, 4096\n"
      "buffer_load_dwordx4 v2, s[4:7], s94 offen offset:0 sc1 lds\n"
      "v_add_f32 v96, v96, v98\n"
      "buffer_store_dwordx2 v[96:97], v124, s[4:7], s69 offen sc1\n"
      "v_lshlrev_b32 v80, 16, v24\n"
      "v_and_b32 v81, s83, v24\n"
      "v_lshlrev_b32 v82, 16, v25\n"
      "v_and_b32 v83, s83, v25\n"
      "v_lshlrev_b32 v84, 16, v26\n"
      "v_and_b32 v85, s83, v26\n"
      "v_lshlrev_b32 v86, 16, v27\n"
      "v_and_b32 v87, s83, v27\n"
      "v_lshlrev_b32 v88, 16, v28\n"
      "v_and_b32 v89, s83, v28\n"
      "v_lshlrev_b32 v90, 16, v29\n"
      "v_and_b32 v91, s83, v29\n"
      "v_lshlrev_b32 v92, 16, v30\n"
      "v_and_b32 v93, s83, v30\n"
      "v_lshlrev_b32 v94, 16, v31\n"
      "buffer_load_dwordx4 v2, s[4:7], s94 offen offset:2048 sc1 lds\n"
      "v_and_b32 v95, s83, v31\n"
      "v_and_b32 v100, 15, v1\n"
      "v_and_b32 v101, 7, v1\n"
      "v_xor_b32 v102, v101, v12\n"
      "v_lshlrev_b32 v102, 4, v102\n"
      "v_lshl_add_u32 v102, v100, 7, v102\n"
      "s_lshl_b32 s87, s84, 11\n"
      "s_add_u32 s87, s87, 69632\n"
      "v_add_u32 v102, s87, v102\n"
      "v_xor_b32 v103, 64, v102\n"
      "ds_write_b128 v102, v[24:27]\n"
      "ds_write_b128 v103, v[28:31]\n"
      "s_add_u32 m0, s21, 90112\n"
      "s_add_u32 s94, s94, 4096\n"
      "buffer_load_dwordx4 v2, s[4:7], s94 offen offset:0 sc1 lds\n"
      "v_lshrrev_b32 v100, 3, v1\n"
      "v_xor_b32 v102, v101, v100\n"
      "v_lshlrev_b32 v102, 4, v102\n"
      "v_lshl_add_u32 v102, v100, 7, v102\n"
      "v_add_u32 v102, s87, v102\n"
      "v_lshlrev_b32 v101, 4, v101\n"
      "v_lshl_add_u32 v5, v100, 10, v101\n"
      "ds_read_b128 v[24:27], v102\n"
      "ds_read_b128 v[28:31], v102 offset:1024\n"
      "v_mov_b32 v100, 2147483648\n"
      "v_cmp_eq_u32 vcc, 0, v1\n"
      "v_mov_b32 v102, s78\n"
      "buffer_load_dwordx4 v2, s[4:7], s94 offen offset:2048 sc1 lds\n"
      "v_cndmask_b32 v100, v100, v102, vcc\n"
      "s_add_u32 s93, s58, 40960\n"
      "buffer_store_dwordx4 v[16:19], v2, s[4:7], s93 offen sc1\n"
      "s_add_u32 m0, s21, 94208\n"
      "s_add_u32 s94, s94, 4096\n"
      "buffer_load_dwordx4 v2, s[4:7], s94 offen offset:0 sc1 lds\n"
      "buffer_load_dwordx4 v2, s[4:7], s94 offen offset:2048 sc1 lds\n"
      "s_add_u32 s93, s58, 57344\n"
      "buffer_store_dwordx4 v[16:19], v2, s[4:7], s93 offen sc1\n"
      "s_add_u32 s93, s58, 73728\n"
      "buffer_store_dwordx4 v[16:19], v2, s[4:7], s93 offen sc1\n"
      "s_waitcnt vmcnt(8)\n"
      "s_sleep 4\n"
      "s_add_u32 m0, s21, 82944\n"
      "s_add_u32 s94, s63, 196608\n"
      "buffer_load_dwordx4 v2, s[4:7], s94 offen offset:0 sc1 lds\n"
      "buffer_load_dwordx2 v[64:65], v125, s[4:7], s68 offen offset:0 sc1\n"
      "buffer_load_dwordx2 v[66:67], v125, s[4:7], s68 offen offset:256 sc1\n"
      "buffer_load_dwordx2 v[68:69], v125, s[4:7], s68 offen offset:512 sc1\n"
      "buffer_load_dwordx2 v[70:71], v125, s[4:7], s68 offen offset:768 sc1\n"
      "buffer_load_dword v126, v11, s[4:7], s74 offen sc1\n"
      "buffer_load_dwordx4 v2, s[4:7], s94 offen offset:2048 sc1 lds\n"
      "s_add_u32 m0, s21, 87040\n"
      "s_add_u32 s94, s94, 4096\n"
      "buffer_load_dwordx4 v2, s[4:7], s94 offen offset:0 sc1 lds\n"
      "buffer_load_dwordx4 v2, s[4:7], s94 offen offset:2048 sc1 lds\n"
      "s_waitcnt vmcnt(7)\n"
      "v_xor_b32 v72, s86, v65\n"
      "s_add_u32 m0, s21, 91136\n"
      "s_add_u32 s94, s94, 4096\n"
      "buffer_load_dwordx4 v2, s[4:7], s94 offen offset:0 sc1 lds\n"
      "s_waitcnt vmcnt(7)\n"
      "v_bitop3_b32 v72, v72, v67, s86 bitop3:0xf6\n"
      "s_waitcnt vmcnt(6)\n"
      "v_bitop3_b32 v72, v72, v69, s86 bitop3:0xf6\n"
      "s_waitcnt vmcnt(5)\n"
      "v_bitop3_b32 v72, v72, v71, s86 bitop3:0xf6\n"
      "buffer_load_dwordx4 v2, s[4:7], s94 offen offset:2048 sc1 lds\n"
      "v_cmp_ne_u32 vcc, 0, v72\n"
      "s_cbranch_vccnz .Lretry_26\n"
      ".Lback_27:\n"
      "v_add_f32 v64, v64, v66\n"
      "v_add_f32 v68, v68, v70\n"
      "v_add_f32 v64, v64, v68\n"
      "v_mov_b32 v65, v64\n"
      "s_nop 1\n"
      "v_permlane16_swap_b32 v64, v65\n"
      "v_add_f32 v64, v64, v65\n"
      "v_mul_f32 v64, 0x3b000000, v64\n"
      "v_mov_b32 v65, v64\n"
      "s_nop 1\n"
      "v_permlane32_swap_b32 v64, v65\n"
      "v_fma_f32 v65, -v64, v64, v65\n"
      "v_max_f32 v65, 0, v65\n"
      "v_add_f32 v65, 0x3727c5ac, v65\n"
      "v_rsq_f32 v66, v65\n"
      "s_nop 0\n"
      "v_mul_f32_e64 v67, -v64, v66\n"
      "v_pk_fma_f32 v[80:81], v[80:81], v[66:67], v[66:67] op_sel:[0,0,1] op_sel_hi:[1,0,1]\n"
      "v_pk_fma_f32 v[82:83], v[82:83], v[66:67], v[66:67] op_sel:[0,0,1] op_sel_hi:[1,0,1]\n"
      "v_pk_fma_f32 v[84:85], v[84:85], v[66:67], v[66:67] op_sel:[0,0,1] op_sel_hi:[1,0,1]\n"
      "v_pk_fma_f32 v[86:87], v[86:87], v[66:67], v[66:67] op_sel:[0,0,1] op_sel_hi:[1,0,1]\n"
      "s_waitcnt lgkmcnt(7)\n"
      "v_pk_fma_f32 v[80:81], v[80:81], v[48:49], v[32:33]\n"
      "v_pk_fma_f32 v[82:83], v[82:83], v[50:51], v[34:35]\n"
      "s_waitcnt lgkmcnt(6)\n"
      "v_pk_fma_f32 v[84:85], v[84:85], v[52:53], v[36:37]\n"
      "v_pk_fma_f32 v[86:87], v[86:87], v[54:55], v[38:39]\n"
      "v_cvt_pk_bf16_f32 v72, v80, v81\n"
      "v_cvt_pk_bf16_f32 v73, v82, v83\n"
      "v_cvt_pk_bf16_f32 v74, v84, v85\n"
      "v_cvt_pk_bf16_f32 v75, v86, v87\n"
      "s_add_u32 s87, s64, 0\n"
      "buffer_store_dwordx4 v[72:75], v6, s[4:7], s87 offen sc1\n"
      "v_pk_fma_f32 v[88:89], v[88:89], v[66:67], v[66:67] op_sel:[0,0,1] op_sel_hi:[1,0,1]\n"
      "v_pk_fma_f32 v[90:91], v[90:91], v[66:67], v[66:67] op_sel:[0,0,1] op_sel_hi:[1,0,1]\n"
      "v_pk_fma_f32 v[92:93], v[92:93], v[66:67], v[66:67] op_sel:[0,0,1] op_sel_hi:[1,0,1]\n"
      "v_pk_fma_f32 v[94:95], v[94:95], v[66:67], v[66:67] op_sel:[0,0,1] op_sel_hi:[1,0,1]\n"
      "s_waitcnt lgkmcnt(5)\n"
      "v_pk_fma_f32 v[88:89], v[88:89], v[56:57], v[40:41]\n"
      "v_pk_fma_f32 v[90:91], v[90:91], v[58:59], v[42:43]\n"
      "s_waitcnt lgkmcnt(4)\n"
      "v_pk_fma_f32 v[92:93], v[92:93], v[60:61], v[44:45]\n"
      "v_pk_fma_f32 v[94:95], v[94:95], v[62:63], v[46:47]\n"
      "v_cvt_pk_bf16_f32 v76, v88, v89\n"
      "v_cvt_pk_bf16_f32 v77, v90, v91\n"
      "v_cvt_pk_bf16_f32 v78, v92, v93\n"
      "v_cvt_pk_bf16_f32 v79, v94, v95\n"
      "s_add_u32 s87, s64, 4096\n"
      "buffer_store_dwordx4 v[76:79], v6, s[4:7], s87 offen sc1\n"
      "buffer_store_dword v23, v100, s[4:7], 0 offen sc1\n"
      "s_waitcnt vmcnt(8)\n"
      "v_cmp_ne_u32 vcc, s86, v126\n"
      "s_cbranch_vccnz .Lretry_28\n"
      ".Lback_29:\n"
      "v_add_u32 v11, 155648, v101\n"
      "s_add_u32 m0, s21, 95232\n"
      "s_add_u32 s94, s94, 4096\n"
      "buffer_load_dwordx4 v2, s[4:7], s94 offen offset:0 sc1 lds\n"
      "buffer_load_dwordx4 v2, s[4:7], s94 offen offset:2048 sc1 lds\n"
      "s_waitcnt vmcnt(3)\n"
      "s_sleep 8\n"
      "s_mov_b32 s89, s65\n"
      "buffer_load_dwordx4 v[128:131], v2, s[4:7], s89 offen offset:0 sc1\n"
      "buffer_load_dwordx4 v[132:135], v2, s[4:7], s89 offen offset:2048 sc1\n"
      "s_add_u32 s89, s89, 4096\n"
      "buffer_load_dwordx4 v[136:139], v2, s[4:7], s89 offen offset:0 sc1\n"
      "buffer_load_dwordx4 v[140:143], v2, s[4:7], s89 offen offset:2048 sc1\n"
      "s_add_u32 s89, s89, 4096\n"
      "buffer_load_dwordx4 v[144:147], v2, s[4:7], s89 offen offset:0 sc1\n"
      "buffer_load_dwordx4 v[148:151], v2, s[4:7], s89 offen offset:2048 sc1\n"
      "s_add_u32 s89, s89, 4096\n"
      "buffer_load_dwordx4 v[152:155], v2, s[4:7], s89 offen offset:0 sc1\n"
      "buffer_load_dwordx4 v[156:159], v2, s[4:7], s89 offen offset:2048 sc1\n"
      "s_add_u32 s89, s89, 4096\n"
      "buffer_load_dwordx4 v[160:163], v2, s[4:7], s89 offen offset:0 sc1\n"
      "buffer_load_dwordx4 v[164:167], v2, s[4:7], s89 offen offset:2048 sc1\n"
      "s_add_u32 s89, s89, 4096\n"
      "buffer_load_dwordx4 v[168:171], v2, s[4:7], s89 offen offset:0 sc1\n"
      "buffer_load_dwordx4 v[172:175], v2, s[4:7], s89 offen offset:2048 sc1\n"
      "s_barrier\n"
      "ds_read_b128 v[96:99], v9 offset:0\n"
      "ds_read_b128 v[100:103], v9 offset:1024\n"
      "ds_read_b128 v[104:107], v9 offset:2048\n"
      "ds_read_b128 v[108:111], v9 offset:3072\n"
      "ds_read_b128 v[112:115], v9 offset:4096\n"
      "s_waitcnt vmcnt(11)\n"
      "v_max_u32 v15, v128, v129\n"
      "v_max3_u32 v15, v15, v130, v131\n"
      "s_waitcnt vmcnt(10)\n"
      "v_max3_u32 v15, v15, v132, v133\n"
      "v_max3_u32 v15, v15, v134, v135\n"
      "s_waitcnt vmcnt(9)\n"
      "v_max3_u32 v15, v15, v136, v137\n"
      "v_max3_u32 v15, v15, v138, v139\n"
      "s_waitcnt vmcnt(8)\n"
      "v_max3_u32 v15, v15, v140, v141\n"
      "v_max3_u32 v15, v15, v142, v143\n"
      "v_cmp_eq_u32 vcc, -1, v15\n"
      "s_cbranch_vccnz .Lretry_30\n"
      ".Lback_31:\n"
      "s_waitcnt lgkmcnt(3)\n"
      "v_mfma_f32_32x32x16_bf16 v[32:47], v[96:99], v[128:131], 0\n"
      "s_add_u32 s89, s89, 4096\n"
      "ds_read_b128 v[116:119], v9 offset:5120\n"
      "buffer_load_dwordx4 v[176:179], v2, s[4:7], s89 offen offset:0 sc1\n"
      "v_mfma_f32_32x32x16_bf16 v[48:63], v[100:103], v[128:131], 0\n"
      "s_waitcnt lgkmcnt(2)\n"
      "ds_read_b128 v[96:99], v9 offset:6144\n"
      "v_mfma_f32_32x32x16_bf16 v[32:47], v[104:107], v[132:135], v[32:47]\n"
      "ds_read_b128 v[100:103], v9 offset:7168\n"
      "buffer_load_dwordx4 v[180:183], v2, s[4:7], s89 offen offset:2048 sc1\n"
      "s_waitcnt vmcnt(9)\n"
      "v_max_u32 v15, v144, v145\n"
      "v_max3_u32 v15, v15, v146, v147\n"
      "v_mfma_f32_32x32x16_bf16 v[48:63], v[108:111], v[132:135], v[48:63]\n"
      "s_waitcnt lgkmcnt(2)\n"
      "ds_read_b128 v[104:107], v9 offset:8192\n"
      "v_mfma_f32_32x32x16_bf16 v[32:47], v[112:115], v[136:139], v[32:47]\n"
      "s_add_u32 s89, s89, 4096\n"
      "ds_read_b128 v[108:111], v9 offset:9216\n"
      "buffer_load_dwordx4 v[184:187], v2, s[4:7], s89 offen offset:0 sc1\n"
      "s_waitcnt vmcnt(9)\n"
      "v_max3_u32 v15, v15, v148, v149\n"
      "v_mfma_f32_32x32x16_bf16 v[48:63], v[116:119], v[136:139], v[48:63]\n"
      "s_waitcnt lgkmcnt(2)\n"
      "ds_read_b128 v[112:115], v9 offset:10240\n"
      "v_max3_u32 v15, v15, v150, v151\n"
      "v_mfma_f32_32x32x16_bf16 v[32:47], v[96:99], v[140:143], v[32:47]\n"
      "ds_read_b128 v[116:119], v9 offset:11264\n"
      "buffer_load_dwordx4 v[188:191], v2, s[4:7], s89 offen offset:2048 sc1\n"
      "v_mfma_f32_32x32x16_bf16 v[48:63], v[100:103], v[140:143], v[48:63]\n"
      "ds_read_b128 v[96:99], v9 offset:12288\n"
      "s_waitcnt vmcnt(9)\n"
      "v_max3_u32 v15, v15, v152, v153\n"
      "v_max3_u32 v15, v15, v154, v155\n"
      "s_waitcnt vmcnt(8)\n"
      "v_max3_u32 v15, v15, v156, v157\n"
      "v_max3_u32 v15, v15, v158, v159\n"
      "v_cmp_eq_u32 vcc, -1, v15\n"
      "s_cbranch_vccnz .Lretry_32\n"
      ".Lback_33:\n"
      "s_waitcnt lgkmcnt(3)\n"
      "v_mfma_f32_32x32x16_bf16 v[32:47], v[104:107], v[144:147], v[32:47]\n"
      "s_add_u32 s89, s89, 4096\n"
      "ds_read_b128 v[100:103], v9 offset:13312\n"
      "buffer_load_dwordx4 v[192:195], v2, s[4:7], s89 offen offset:0 sc1\n"
      "v_mfma_f32_32x32x16_bf16 v[48:63], v[108:111], v[144:147], v[48:63]\n"
      "s_waitcnt lgkmcnt(2)\n"
      "ds_read_b128 v[104:107], v9 offset:14336\n"
      "v_mfma_f32_32x32x16_bf16 v[32:47], v[112:115], v[148:151], v[32:47]\n"
      "ds_read_b128 v[108:111], v9 offset:15360\n"
      "buffer_load_dwordx4 v[196:199], v2, s[4:7], s89 offen offset:2048 sc1\n"
      "s_waitcnt vmcnt(9)\n"
      "v_max_u32 v15, v160, v161\n"
      "v_max3_u32 v15, v15, v162, v163\n"
      "v_mfma_f32_32x32x16_bf16 v[48:63], v[116:119], v[148:151], v[48:63]\n"
      "s_waitcnt lgkmcnt(2)\n"
      "ds_read_b128 v[112:115], v9 offset:16384\n"
      "v_mfma_f32_32x32x16_bf16 v[32:47], v[96:99], v[152:155], v[32:47]\n"
      "s_add_u32 s89, s89, 4096\n"
      "ds_read_b128 v[116:119], v9 offset:17408\n"
      "buffer_load_dwordx4 v[200:203], v2, s[4:7], s89 offen offset:0 sc1\n"
      "s_waitcnt vmcnt(9)\n"
      "v_max3_u32 v15, v15, v164, v165\n"
      "v_mfma_f32_32x32x16_bf16 v[48:63], v[100:103], v[152:155], v[48:63]\n"
      "s_waitcnt lgkmcnt(2)\n"
      "ds_read_b128 v[96:99], v9 offset:18432\n"
      "v_max3_u32 v15, v15, v166, v167\n"
      "v_mfma_f32_32x32x16_bf16 v[32:47], v[104:107], v[156:159], v[32:47]\n"
      "ds_read_b128 v[100:103], v9 offset:19456\n"
      "buffer_load_dwordx4 v[204:207], v2, s[4:7], s89 offen offset:2048 sc1\n"
      "v_mfma_f32_32x32x16_bf16 v[48:63], v[108:111], v[156:159], v[48:63]\n"
      "ds_read_b128 v[104:107], v9 offset:20480\n"
      "s_waitcnt vmcnt(9)\n"
      "v_max3_u32 v15, v15, v168, v169\n"
      "v_max3_u32 v15, v15, v170, v171\n"
      "s_waitcnt vmcnt(8)\n"
      "v_max3_u32 v15, v15, v172, v173\n"
      "v_max3_u32 v15, v15, v174, v175\n"
      "v_cmp_eq_u32 vcc, -1, v15\n"
      "s_cbranch_vccnz .Lretry_34\n"
      ".Lback_35:\n"
      "s_waitcnt lgkmcnt(3)\n"
      "v_mfma_f32_32x32x16_bf16 v[32:47], v[112:115], v[160:163], v[32:47]\n"
      "s_add_u32 s89, s89, 4096\n"
      "ds_read_b128 v[108:111], v9 offset:21504\n"
      "buffer_load_dwordx4 v[208:211], v2, s[4:7], s89 offen offset:0 sc1\n"
      "v_mfma_f32_32x32x16_bf16 v[48:63], v[116:119], v[160:163], v[48:63]\n"
      "s_waitcnt lgkmcnt(2)\n"
      "ds_read_b128 v[112:115], v9 offset:22528\n"
      "v_mfma_f32_32x32x16_bf16 v[32:47], v[96:99], v[164:167], v[32:47]\n"
      "ds_read_b128 v[116:119], v9 offset:23552\n"
      "buffer_load_dwordx4 v[212:215], v2, s[4:7], s89 offen offset:2048 sc1\n"
      "v_mfma_f32_32x32x16_bf16 v[48:63], v[100:103], v[164:167], v[48:63]\n"
      "s_add_u32 s89, s89, 4096\n"
      "ds_read_b128 v[96:99], v9 offset:24576\n"
      "s_waitcnt vmcnt(9)\n"
      "v_max_u32 v15, v176, v177\n"
      "v_max3_u32 v15, v15, v178, v179\n"
      "s_waitcnt lgkmcnt(3)\n"
      "v_mfma_f32_32x32x16_bf16 v[32:47], v[104:107], v[168:171], v[32:47]\n"
      "ds_read_b128 v[100:103], v9 offset:25600\n"
      "buffer_load_dwordx4 v[216:219], v2, s[4:7], s89 offen offset:0 sc1\n"
      "v_mfma_f32_32x32x16_bf16 v[48:63], v[108:111], v[168:171], v[48:63]\n"
      "s_waitcnt lgkmcnt(2)\n"
      "ds_read_b128 v[104:107], v9 offset:26624\n"
      "v_mfma_f32_32x32x16_bf16 v[32:47], v[112:115], v[172:175], v[32:47]\n"
      "ds_read_b128 v[108:111], v9 offset:27648\n"
      "buffer_load_dwordx4 v[220:223], v2, s[4:7], s89 offen offset:2048 sc1\n"
      "s_waitcnt vmcnt(10)\n"
      "v_max3_u32 v15, v15, v180, v181\n"
      "v_max3_u32 v15, v15, v182, v183\n"
      "v_mfma_f32_32x32x16_bf16 v[48:63], v[116:119], v[172:175], v[48:63]\n"
      "ds_read_b128 v[112:115], v9 offset:28672\n"
      "s_waitcnt vmcnt(9)\n"
      "v_max3_u32 v15, v15, v184, v185\n"
      "v_max3_u32 v15, v15, v186, v187\n"
      "s_waitcnt vmcnt(8)\n"
      "v_max3_u32 v15, v15, v188, v189\n"
      "v_max3_u32 v15, v15, v190, v191\n"
      "v_cmp_eq_u32 vcc, -1, v15\n"
      "s_cbranch_vccnz .Lretry_36\n"
      ".Lback_37:\n"
      "s_waitcnt lgkmcnt(3)\n"
      "v_mfma_f32_32x32x16_bf16 v[32:47], v[96:99], v[176:179], v[32:47]\n"
      "s_add_u32 s89, s89, 4096\n"
      "ds_read_b128 v[116:119], v9 offset:29696\n"
      "buffer_load_dwordx4 v[224:227], v2, s[4:7], s89 offen offset:0 sc1\n"
      "v_mfma_f32_32x32x16_bf16 v[48:63], v[100:103], v[176:179], v[48:63]\n"
      "s_waitcnt lgkmcnt(2)\n"
      "ds_read_b128 v[96:99], v9 offset:30720\n"
      "v_mfma_f32_32x32x16_bf16 v[32:47], v[104:107], v[180:183], v[32:47]\n"
      "ds_read_b128 v[100:103], v9 offset:31744\n"
      "buffer_load_dwordx4 v[228:231], v2, s[4:7], s89 offen offset:2048 sc1\n"
      "s_waitcnt vmcnt(9)\n"
      "v_max_u32 v15, v192, v193\n"
      "v_max3_u32 v15, v15, v194, v195\n"
      "v_mfma_f32_32x32x16_bf16 v[48:63], v[108:111], v[180:183], v[48:63]\n"
      "s_waitcnt lgkmcnt(2)\n"
      "ds_read_b128 v[104:107], v9 offset:32768\n"
      "v_mfma_f32_32x32x16_bf16 v[32:47], v[112:115], v[184:187], v[32:47]\n"
      "s_add_u32 s89, s89, 4096\n"
      "ds_read_b128 v[108:111], v9 offset:33792\n"
      "buffer_load_dwordx4 v[232:235], v2, s[4:7], s89 offen offset:0 sc1\n"
      "s_waitcnt vmcnt(9)\n"
      "v_max3_u32 v15, v15, v196, v197\n"
      "v_mfma_f32_32x32x16_bf16 v[48:63], v[116:119], v[184:187], v[48:63]\n"
      "s_waitcnt lgkmcnt(2)\n"
      "ds_read_b128 v[112:115], v9 offset:34816\n"
      "v_max3_u32 v15, v15, v198, v199\n"
      "v_mfma_f32_32x32x16_bf16 v[32:47], v[96:99], v[188:191], v[32:47]\n"
      "ds_read_b128 v[116:119], v9 offset:35840\n"
      "buffer_load_dwordx4 v[236:239], v2, s[4:7], s89 offen offset:2048 sc1\n"
      "v_mfma_f32_32x32x16_bf16 v[48:63], v[100:103], v[188:191], v[48:63]\n"
      "ds_read_b128 v[96:99], v9 offset:36864\n"
      "s_waitcnt vmcnt(9)\n"
      "v_max3_u32 v15, v15, v200, v201\n"
      "v_max3_u32 v15, v15, v202, v203\n"
      "s_waitcnt vmcnt(8)\n"
      "v_max3_u32 v15, v15, v204, v205\n"
      "v_max3_u32 v15, v15, v206, v207\n"
      "v_cmp_eq_u32 vcc, -1, v15\n"
      "s_cbranch_vccnz .Lretry_38\n"
      ".Lback_39:\n"
      "s_waitcnt lgkmcnt(3)\n"
      "v_mfma_f32_32x32x16_bf16 v[32:47], v[104:107], v[192:195], v[32:47]\n"
      "s_add_u32 s89, s89, 4096\n"
      "ds_read_b128 v[100:103], v9 offset:37888\n"
      "buffer_load_dwordx4 v[240:243], v2, s[4:7], s89 offen offset:0 sc1\n"
      "v_mfma_f32_32x32x16_bf16 v[48:63], v[108:111], v[192:195], v[48:63]\n"
      "s_waitcnt lgkmcnt(2)\n"
      "ds_read_b128 v[104:107], v9 offset:38912\n"
      "v_mfma_f32_32x32x16_bf16 v[32:47], v[112:115], v[196:199], v[32:47]\n"
      "ds_read_b128 v[108:111], v9 offset:39936\n"
      "buffer_load_dwordx4 v[244:247], v2, s[4:7], s89 offen offset:2048 sc1\n"
      "s_waitcnt vmcnt(9)\n"
      "v_max_u32 v15, v208, v209\n"
      "v_max3_u32 v15, v15, v210, v211\n"
      "v_mfma_f32_32x32x16_bf16 v[48:63], v[116:119], v[196:199], v[48:63]\n"
      "s_waitcnt lgkmcnt(2)\n"
      "ds_read_b128 v[112:115], v9 offset:40960\n"
      "v_mfma_f32_32x32x16_bf16 v[32:47], v[96:99], v[200:203], v[32:47]\n"
      "s_add_u32 s89, s89, 4096\n"
      "ds_read_b128 v[116:119], v9 offset:41984\n"
      "buffer_load_dwordx4 v[248:251], v2, s[4:7], s89 offen offset:0 sc1\n"
      "s_waitcnt vmcnt(9)\n"
      "v_max3_u32 v15, v15, v212, v213\n"
      "v_mfma_f32_32x32x16_bf16 v[48:63], v[100:103], v[200:203], v[48:63]\n"
      "s_waitcnt lgkmcnt(2)\n"
      "ds_read_b128 v[96:99], v9 offset:43008\n"
      "v_max3_u32 v15, v15, v214, v215\n"
      "v_mfma_f32_32x32x16_bf16 v[32:47], v[104:107], v[204:207], v[32:47]\n"
      "ds_read_b128 v[100:103], v9 offset:44032\n"
      "buffer_load_dwordx4 v[252:255], v2, s[4:7], s89 offen offset:2048 sc1\n"
      "v_mfma_f32_32x32x16_bf16 v[48:63], v[108:111], v[204:207], v[48:63]\n"
      "ds_read_b128 v[104:107], v9 offset:45056\n"
      "s_waitcnt vmcnt(9)\n"
      "v_max3_u32 v15, v15, v216, v217\n"
      "v_max3_u32 v15, v15, v218, v219\n"
      "s_waitcnt vmcnt(8)\n"
      "v_max3_u32 v15, v15, v220, v221\n"
      "v_max3_u32 v15, v15, v222, v223\n"
      "v_cmp_eq_u32 vcc, -1, v15\n"
      "s_cbranch_vccnz .Lretry_40\n"
      ".Lback_41:\n"
      "s_waitcnt lgkmcnt(3)\n"
      "v_mfma_f32_32x32x16_bf16 v[32:47], v[112:115], v[208:211], v[32:47]\n"
      "s_add_u32 s94, s62, 0\n"
      "ds_read_b128 v[108:111], v9 offset:46080\n"
      "v_mfma_f32_32x32x16_bf16 v[48:63], v[116:119], v[208:211], v[48:63]\n"
      "s_waitcnt lgkmcnt(2)\n"
      "ds_read_b128 v[112:115], v9 offset:47104\n"
      "v_mfma_f32_32x32x16_bf16 v[32:47], v[96:99], v[212:215], v[32:47]\n"
      "ds_read_b128 v[116:119], v9 offset:48128\n"
      "buffer_load_dwordx4 a[0:3], v2, s[4:7], s94 offen offset:0 sc1\n"
      "v_mfma_f32_32x32x16_bf16 v[48:63], v[100:103], v[212:215], v[48:63]\n"
      "ds_read_b128 v[96:99], v9 offset:49152\n"
      "s_waitcnt vmcnt(8)\n"
      "v_max_u32 v15, v224, v225\n"
      "v_max3_u32 v15, v15, v226, v227\n"
      "s_waitcnt lgkmcnt(3)\n"
      "v_mfma_f32_32x32x16_bf16 v[32:47], v[104:107], v[216:219], v[32:47]\n"
      "ds_read_b128 v[100:103], v9 offset:50176\n"
      "buffer_load_dwordx4 a[4:7], v2, s[4:7], s94 offen offset:2048 sc1\n"
      "v_mfma_f32_32x32x16_bf16 v[48:63], v[108:111], v[216:219], v[48:63]\n"
      "s_waitcnt lgkmcnt(2)\n"
      "ds_read_b128 v[104:107], v9 offset:51200\n"
      "v_mfma_f32_32x32x16_bf16 v[32:47], v[112:115], v[220:223], v[32:47]\n"
      "s_add_u32 s94, s94, 4096\n"
      "ds_read_b128 v[108:111], v9 offset:52224\n"
      "buffer_load_dwordx4 a[8:11], v4, s[4:7], s94 offen offset:0 sc1\n"
      "s_waitcnt vmcnt(9)\n"
      "v_max3_u32 v15, v15, v228, v229\n"
      "v_max3_u32 v15, v15, v230, v231\n"
      "v_mfma_f32_32x32x16_bf16 v[48:63], v[116:119], v[220:223], v[48:63]\n"
      "ds_read_b128 v[112:115], v9 offset:53248\n"
      "buffer_load_dwordx4 a[12:15], v4, s[4:7], s94 offen offset:2048 sc1\n"
      "s_waitcnt vmcnt(9)\n"
      "v_max3_u32 v15, v15, v232, v233\n"
      "v_max3_u32 v15, v15, v234, v235\n"
      "s_add_u32 s94, s94, 4096\n"
      "buffer_load_dwordx4 a[16:19], v2, s[4:7], s94 offen offset:0 sc1\n"
      "s_waitcnt vmcnt(9)\n"
      "v_max3_u32 v15, v15, v236, v237\n"
      "v_max3_u32 v15, v15, v238, v239\n"
      "v_cmp_eq_u32 vcc, -1, v15\n"
      "s_cbranch_vccnz .Lretry_42\n"
      ".Lback_43:\n"
      "s_waitcnt lgkmcnt(3)\n"
      "v_mfma_f32_32x32x16_bf16 v[32:47], v[96:99], v[224:227], v[32:47]\n"
      "ds_read_b128 v[116:119], v9 offset:54272\n"
      "v_mfma_f32_32x32x16_bf16 v[48:63], v[100:103], v[224:227], v[48:63]\n"
      "s_waitcnt lgkmcnt(2)\n"
      "ds_read_b128 v[96:99], v9 offset:55296\n"
      "buffer_load_dwordx4 a[20:23], v2, s[4:7], s94 offen offset:2048 sc1\n"
      "v_mfma_f32_32x32x16_bf16 v[32:47], v[104:107], v[228:231], v[32:47]\n"
      "s_add_u32 s94, s94, 4096\n"
      "ds_read_b128 v[100:103], v9 offset:56320\n"
      "s_waitcnt vmcnt(9)\n"
      "v_max_u32 v15, v240, v241\n"
      "v_max3_u32 v15, v15, v242, v243\n"
      "v_mfma_f32_32x32x16_bf16 v[48:63], v[108:111], v[228:231], v[48:63]\n"
      "s_waitcnt lgkmcnt(2)\n"
      "ds_read_b128 v[104:107], v9 offset:57344\n"
      "buffer_load_dwordx4 a[24:27], v2, s[4:7], s94 offen offset:0 sc1\n"
      "v_mfma_f32_32x32x16_bf16 v[32:47], v[112:115], v[232:235], v[32:47]\n"
      "ds_read_b128 v[108:111], v9 offset:58368\n"
      "v_mfma_f32_32x32x16_bf16 v[48:63], v[116:119], v[232:235], v[48:63]\n"
      "ds_read_b128 v[112:115], v9 offset:59392\n"
      "buffer_load_dwordx4 a[28:31], v2, s[4:7], s94 offen offset:2048 sc1\n"
      "s_waitcnt vmcnt(10)\n"
      "v_max3_u32 v15, v15, v244, v245\n"
      "v_max3_u32 v15, v15, v246, v247\n"
      "s_waitcnt lgkmcnt(3)\n"
      "v_mfma_f32_32x32x16_bf16 v[32:47], v[96:99], v[236:239], v[32:47]\n"
      "s_add_u32 s94, s94, 4096\n"
      "ds_read_b128 v[116:119], v9 offset:60416\n"
      "v_mfma_f32_32x32x16_bf16 v[48:63], v[100:103], v[236:239], v[48:63]\n"
      "ds_read_b128 v[96:99], v9 offset:61440\n"
      "buffer_load_dwordx4 a[32:35], v2, s[4:7], s94 offen offset:0 sc1\n"
      "s_waitcnt vmcnt(10)\n"
      "v_max3_u32 v15, v15, v248, v249\n"
      "v_max3_u32 v15, v15, v250, v251\n"
      "buffer_load_dwordx4 a[36:39], v2, s[4:7], s94 offen offset:2048 sc1\n"
      "s_waitcnt vmcnt(10)\n"
      "v_max3_u32 v15, v15, v252, v253\n"
      "v_max3_u32 v15, v15, v254, v255\n"
      "v_cmp_eq_u32 vcc, -1, v15\n"
      "s_cbranch_vccnz .Lretry_44\n"
      ".Lback_45:\n"
      "s_waitcnt lgkmcnt(3)\n"
      "v_mfma_f32_32x32x16_bf16 v[32:47], v[104:107], v[240:243], v[32:47]\n"
      "ds_read_b128 v[100:103], v9 offset:62464\n"
      "v_mfma_f32_32x32x16_bf16 v[48:63], v[108:111], v[240:243], v[48:63]\n"
      "s_waitcnt lgkmcnt(2)\n"
      "ds_read_b128 v[104:107], v9 offset:63488\n"
      "v_mfma_f32_32x32x16_bf16 v[32:47], v[112:115], v[244:247], v[32:47]\n"
      "ds_read_b128 v[108:111], v9 offset:64512\n"
      "s_waitcnt lgkmcnt(0)\n"
      "s_barrier\n"
      "buffer_store_dword v23, v22, s[4:7], s75 offen sc1\n"
      "v_mfma_f32_32x32x16_bf16 v[48:63], v[116:119], v[244:247], v[48:63]\n"
      "s_add_u32 m0, s21, 2048\n"
      "ds_read_b128 v[112:115], v10 offset:0\n"
      "s_add_u32 s94, s63, 262144\n"
      "buffer_load_dwordx4 v2, s[4:7], s94 offen offset:0 sc1 lds\n"
      "v_mfma_f32_32x32x16_bf16 v[32:47], v[96:99], v[248:251], v[32:47]\n"
      "ds_read_b128 v[116:119], v10 offset:2048\n"
      "v_mfma_f32_32x32x16_bf16 v[48:63], v[100:103], v[248:251], v[48:63]\n"
      "ds_read_b128 v[96:99], v10 offset:4096\n"
      "buffer_load_dwordx4 v2, s[4:7], s94 offen offset:2048 sc1 lds\n"
      "v_mfma_f32_32x32x16_bf16 v[32:47], v[104:107], v[252:255], v[32:47]\n"
      "s_add_u32 m0, s21, 6144\n"
      "ds_read_b128 v[100:103], v10 offset:6144\n"
      "v_mfma_f32_32x32x16_bf16 v[48:63], v[108:111], v[252:255], v[48:63]\n"
      "s_waitcnt lgkmcnt(2)\n"
      "ds_read_b128 v[104:107], v10 offset:8192\n"
      "s_add_u32 s94, s94, 4096\n"
      "buffer_load_dwordx4 v2, s[4:7], s94 offen offset:0 sc1 lds\n"
      "v_mfma_f32_32x32x16_bf16 v[64:79], v[112:115], v[128:131], 0\n"
      "ds_read_b128 v[108:111], v10 offset:10240\n"
      "v_mfma_f32_32x32x16_bf16 v[64:79], v[116:119], v[132:135], v[64:79]\n"
      "s_waitcnt lgkmcnt(2)\n"
      "ds_read_b128 v[112:115], v10 offset:12288\n"
      "buffer_load_dwordx4 v2, s[4:7], s94 offen offset:2048 sc1 lds\n"
      "v_mul_f32 v32, s95, v32\n"
      "v_mul_f32 v33, s95, v33\n"
      "v_mfma_f32_32x32x16_bf16 v[64:79], v[96:99], v[136:139], v[64:79]\n"
      "s_add_u32 m0, s21, 10240\n"
      "ds_read_b128 v[116:119], v10 offset:14336\n"
      "v_exp_f32 v32, v32\n"
      "s_nop 0\n"
      "v_add_f32 v32, 1.0, v32\n"
      "v_exp_f32 v33, v33\n"
      "v_mfma_f32_32x32x16_bf16 v[64:79], v[100:103], v[140:143], v[64:79]\n"
      "s_waitcnt lgkmcnt(2)\n"
      "ds_read_b128 v[96:99], v10 offset:16384\n"
      "s_add_u32 s94, s94, 4096\n"
      "buffer_load_dwordx4 v2, s[4:7], s94 offen offset:0 sc1 lds\n"
      "v_mul_f32 v34, s95, v34\n"
      "v_mfma_f32_32x32x16_bf16 v[64:79], v[104:107], v[144:147], v[64:79]\n"
      "ds_read_b128 v[100:103], v10 offset:18432\n"
      "v_rcp_f32 v32, v32\n"
      "v_add_f32 v33, 1.0, v33\n"
      "v_exp_f32 v34, v34\n"
      "v_mfma_f32_32x32x16_bf16 v[64:79], v[108:111], v[148:151], v[64:79]\n"
      "s_waitcnt lgkmcnt(2)\n"
      "ds_read_b128 v[104:107], v10 offset:20480\n"
      "buffer_load_dwordx4 v2, s[4:7], s94 offen offset:2048 sc1 lds\n"
      "v_mul_f32 v35, s95, v35\n"
      "v_mul_f32 v32, v32, v40\n"
      "v_mfma_f32_32x32x16_bf16 v[64:79], v[112:115], v[152:155], v[64:79]\n"
      "s_add_u32 m0, s21, 14336\n"
      "ds_read_b128 v[108:111], v10 offset:22528\n"
      "v_rcp_f32 v33, v33\n"
      "v_add_f32 v34, 1.0, v34\n"
      "v_exp_f32 v35, v35\n"
      "v_mfma_f32_32x32x16_bf16 v[64:79], v[116:119], v[156:159], v[64:79]\n"
      "s_waitcnt lgkmcnt(2)\n"
      "ds_read_b128 v[112:115], v10 offset:24576\n"
      "s_add_u32 s94, s94, 4096\n"
      "buffer_load_dwordx4 v2, s[4:7], s94 offen offset:0 sc1 lds\n"
      "v_mul_f32 v36, s95, v36\n"
      "v_mfma_f32_32x32x16_bf16 v[64:79], v[96:99], v[160:163], v[64:79]\n"
      "ds_read_b128 v[116:119], v10 offset:26624\n"
      "v_mul_f32 v33, v33, v41\n"
      "v_rcp_f32 v34, v34\n"
      "v_add_f32 v35, 1.0, v35\n"
      "v_mul_f32 v37, s95, v37\n"
      "v_mfma_f32_32x32x16_bf16 v[64:79], v[100:103], v[164:167], v[64:79]\n"
      "s_waitcnt lgkmcnt(2)\n"
      "ds_read_b128 v[96:99], v10 offset:28672\n"
      "buffer_load_dwordx4 v2, s[4:7], s94 offen offset:2048 sc1 lds\n"
      "v_exp_f32 v36, v36\n"
      "v_mfma_f32_32x32x16_bf16 v[64:79], v[104:107], v[168:171], v[64:79]\n"
      "s_add_u32 m0, s21, 3072\n"
      "ds_read_b128 v[100:103], v10 offset:30720\n"
      "v_mul_f32 v34, v34, v42\n"
      "v_rcp_f32 v35, v35\n"
      "v_add_f32 v36, 1.0, v36\n"
      "v_mul_f32 v38, s95, v38\n"
      "v_mfma_f32_32x32x16_bf16 v[64:79], v[108:111], v[172:175], v[64:79]\n"
      "s_waitcnt lgkmcnt(2)\n"
      "ds_read_b128 v[104:107], v10 offset:32768\n"
      "s_add_u32 s94, s63, 327680\n"
      "buffer_load_dwordx4 v2, s[4:7], s94 offen offset:0 sc1 lds\n"
      "v_mul_f32 v35, v35, v43\n"
      "v_mfma_f32_32x32x16_bf16 v[64:79], v[112:115], v[176:179], v[64:79]\n"
      "ds_read_b128 v[108:111], v10 offset:34816\n"
      "v_exp_f32 v37, v37\n"
      "v_rcp_f32 v36, v36\n"
      "v_add_f32 v37, 1.0, v37\n"
      "v_mfma_f32_32x32x16_bf16 v[64:79], v[116:119], v[180:183], v[64:79]\n"
      "s_waitcnt lgkmcnt(2)\n"
      "ds_read_b128 v[112:115], v10 offset:36864\n"
      "buffer_load_dwordx4 v2, s[4:7], s94 offen offset:2048 sc1 lds\n"
      "v_exp_f32 v38, v38\n"
      "v_mfma_f32_32x32x16_bf16 v[64:79], v[96:99], v[184:187], v[64:79]\n"
      "s_add_u32 m0, s21, 7168\n"
      "ds_read_b128 v[116:119], v10 offset:38912\n"
      "v_mul_f32 v39, s95, v39\n"
      "v_mul_f32 v36, v36, v44\n"
      "v_rcp_f32 v37, v37\n"
      "v_add_f32 v38, 1.0, v38\n"
      "v_mfma_f32_32x32x16_bf16 v[64:79], v[100:103], v[188:191], v[64:79]\n"
      "s_waitcnt lgkmcnt(2)\n"
      "ds_read_b128 v[96:99], v10 offset:40960\n"
      "s_add_u32 s94, s94, 4096\n"
      "buffer_load_dwordx4 v2, s[4:7], s94 offen offset:0 sc1 lds\n"
      "v_mul_f32 v37, v37, v45\n"
      "v_mfma_f32_32x32x16_bf16 v[64:79], v[104:107], v[192:195], v[64:79]\n"
      "ds_read_b128 v[100:103], v10 offset:43008\n"
      "v_exp_f32 v39, v39\n"
      "v_rcp_f32 v38, v38\n"
      "v_add_f32 v39, 1.0, v39\n"
      "v_mfma_f32_32x32x16_bf16 v[64:79], v[108:111], v[196:199], v[64:79]\n"
      "s_waitcnt lgkmcnt(2)\n"
      "ds_read_b128 v[104:107], v10 offset:45056\n"
      "buffer_load_dwordx4 v2, s[4:7], s94 offen offset:2048 sc1 lds\n"
      "v_mul_f32 v38, v38, v46\n"
      "v_mfma_f32_32x32x16_bf16 v[64:79], v[112:115], v[200:203], v[64:79]\n"
      "s_add_u32 m0, s21, 11264\n"
      "ds_read_b128 v[108:111], v10 offset:47104\n"
      "v_rcp_f32 v39, v39\n"
      "s_nop 0\n"
      "v_mul_f32 v39, v39, v47\n"
      "v_cvt_pk_bf16_f32 v120, v32, v33\n"
      "v_cvt_pk_bf16_f32 v121, v34, v35\n"
      "v_mfma_f32_32x32x16_bf16 v[64:79], v[116:119], v[204:207], v[64:79]\n"
      "s_waitcnt lgkmcnt(2)\n"
      "ds_read_b128 v[112:115], v10 offset:49152\n"
      "s_add_u32 s94, s94, 4096\n"
      "buffer_load_dwordx4 v2, s[4:7], s94 offen offset:0 sc1 lds\n"
      "v_cvt_pk_bf16_f32 v122, v36, v37\n"
      "v_mfma_f32_32x32x16_bf16 v[64:79], v[96:99], v[208:211], v[64:79]\n"
      "ds_read_b128 v[116:119], v10 offset:51200\n"
      "v_cvt_pk_bf16_f32 v123, v38, v39\n"
      "v_mfma_f32_32x32x16_bf16 v[64:79], v[100:103], v[212:215], v[64:79]\n"
      "s_waitcnt lgkmcnt(2)\n"
      "ds_read_b128 v[96:99], v10 offset:53248\n"
      "buffer_load_dwordx4 v2, s[4:7], s94 offen offset:2048 sc1 lds\n"
      "v_mfma_f32_32x32x16_bf16 v[64:79], v[104:107], v[216:219], v[64:79]\n"
      "s_add_u32 m0, s21, 15360\n"
      "ds_read_b128 v[100:103], v10 offset:55296\n"
      "v_mfma_f32_32x32x16_bf16 v[64:79], v[108:111], v[220:223], v[64:79]\n"
      "s_waitcnt lgkmcnt(2)\n"
      "ds_read_b128 v[104:107], v10 offset:57344\n"
      "s_add_u32 s94, s94, 4096\n"
      "buffer_load_dwordx4 v2, s[4:7], s94 offen offset:0 sc1 lds\n"
      "v_mfma_f32_32x32x16_bf16 v[64:79], v[112:115], v[224:227], v[64:79]\n"
      "ds_read_b128 v[108:111], v10 offset:59392\n"
      "v_mfma_f32_32x32x16_bf16 v[64:79], v[116:119], v[228:231], v[64:79]\n"
      "s_waitcnt lgkmcnt(2)\n"
      "ds_read_b128 v[112:115], v10 offset:61440\n"
      "buffer_load_dwordx4 v2, s[4:7], s94 offen offset:2048 sc1 lds\n"
      "v_mfma_f32_32x32x16_bf16 v[64:79], v[96:99], v[232:235], v[64:79]\n"
      "ds_read_b128 v[116:119], v10 offset:63488\n"
      "s_barrier\n"
      "v_mfma_f32_32x32x16_bf16 v[64:79], v[100:103], v[236:239], v[64:79]\n"
      "s_waitcnt lgkmcnt(2)\n"
      "ds_read_b128 v[96:99], v10 offset:1024\n"
      "s_add_u32 s94, s62, 20480\n"
      "buffer_load_dwordx4 a[40:43], v2, s[4:7], s94 offen offset:0 sc1\n"
      "v_mfma_f32_32x32x16_bf16 v[64:79], v[104:107], v[240:243], v[64:79]\n"
      "ds_read_b128 v[100:103], v10 offset:3072\n"
      "v_mfma_f32_32x32x16_bf16 v[64:79], v[108:111], v[244:247], v[64:79]\n"
      "s_waitcnt lgkmcnt(2)\n"
      "ds_read_b128 v[104:107], v10 offset:5120\n"
      "buffer_load_dwordx4 a[44:47], v2, s[4:7], s94 offen offset:2048 sc1\n"
      "v_mfma_f32_32x32x16_bf16 v[64:79], v[112:115], v[248:251], v[64:79]\n"
      "s_add_u32 s94, s94, 4096\n"
      "ds_read_b128 v[108:111], v10 offset:7168\n"
      "v_mfma_f32_32x32x16_bf16 v[64:79], v[116:119], v[252:255], v[64:79]\n"
      "s_waitcnt lgkmcnt(2)\n"
      "ds_read_b128 v[112:115], v10 offset:9216\n"
      "buffer_load_dwordx4 a[48:51], v2, s[4:7], s94 offen offset:0 sc1\n"
      "v_mfma_f32_32x32x16_bf16 v[80:95], v[96:99], v[128:131], 0\n"
      "ds_read_b128 v[116:119], v10 offset:11264\n"
      "v_mfma_f32_32x32x16_bf16 v[80:95], v[100:103], v[132:135], v[80:95]\n"
      "s_waitcnt lgkmcnt(2)\n"
      "ds_read_b128 v[96:99], v10 offset:13312\n"
      "buffer_load_dword v13, v20, s[4:7], s76 offen sc1\n"
      "v_mfma_f32_32x32x16_bf16 v[80:95], v[104:107], v[136:139], v[80:95]\n"
      "ds_read_b128 v[100:103], v10 offset:15360\n"
      "buffer_load_dwordx4 a[52:55], v2, s[4:7], s94 offen offset:2048 sc1\n"
      "v_mfma_f32_32x32x16_bf16 v[80:95], v[108:111], v[140:143], v[80:95]\n"
      "s_waitcnt lgkmcnt(2)\n"
      "ds_read_b128 v[104:107], v10 offset:17408\n"
      "v_mfma_f32_32x32x16_bf16 v[80:95], v[112:115], v[144:147], v[80:95]\n"
      "s_add_u32 s94, s94, 4096\n"
      "ds_read_b128 v[108:111], v10 offset:19456\n"
      "buffer_load_dwordx4 a[56:59], v2, s[4:7], s94 offen offset:0 sc1\n"
      "v_mfma_f32_32x32x16_bf16 v[80:95], v[116:119], v[148:151], v[80:95]\n"
      "s_waitcnt lgkmcnt(2)\n"
      "ds_read_b128 v[112:115], v10 offset:21504\n"
      "v_mfma_f32_32x32x16_bf16 v[80:95], v[96:99], v[152:155], v[80:95]\n"
      "ds_read_b128 v[116:119], v10 offset:23552\n"
      "buffer_load_dwordx4 a[60:63], v2, s[4:7], s94 offen offset:2048 sc1\n"
      "v_mfma_f32_32x32x16_bf16 v[80:95], v[100:103], v[156:159], v[80:95]\n"
      "s_waitcnt lgkmcnt(2)\n"
      "ds_read_b128 v[96:99], v10 offset:25600\n"
      "v_mfma_f32_32x32x16_bf16 v[80:95], v[104:107], v[160:163], v[80:95]\n"
      "s_add_u32 s94, s94, 4096\n"
      "ds_read_b128 v[100:103], v10 offset:27648\n"
      "buffer_load_dwordx4 a[64:67], v2, s[4:7], s94 offen offset:0 sc1\n"
      "v_mfma_f32_32x32x16_bf16 v[80:95], v[108:111], v[164:167], v[80:95]\n"
      "s_waitcnt lgkmcnt(2)\n"
      "ds_read_b128 v[104:107], v10 offset:29696\n"
      "v_mfma_f32_32x32x16_bf16 v[80:95], v[112:115], v[168:171], v[80:95]\n"
      "ds_read_b128 v[108:111], v10 offset:31744\n"
      "buffer_load_dwordx4 a[68:71], v2, s[4:7], s94 offen offset:2048 sc1\n"
      "v_mfma_f32_32x32x16_bf16 v[80:95], v[116:119], v[172:175], v[80:95]\n"
      "s_waitcnt lgkmcnt(2)\n"
      "ds_read_b128 v[112:115], v10 offset:33792\n"
      "v_mfma_f32_32x32x16_bf16 v[80:95], v[96:99], v[176:179], v[80:95]\n"
      "s_add_u32 s94, s94, 4096\n"
      "ds_read_b128 v[116:119], v10 offset:35840\n"
      "buffer_load_dwordx4 a[72:75], v2, s[4:7], s94 offen offset:0 sc1\n"
      "v_mfma_f32_32x32x16_bf16 v[80:95], v[100:103], v[180:183], v[80:95]\n"
      "s_waitcnt lgkmcnt(2)\n"
      "ds_read_b128 v[96:99], v10 offset:37888\n"
      "v_mfma_f32_32x32x16_bf16 v[80:95], v[104:107], v[184:187], v[80:95]\n"
      "ds_read_b128 v[100:103], v10 offset:39936\n"
      "buffer_load_dwordx4 a[76:79], v2, s[4:7], s94 offen offset:2048 sc1\n"
      "v_mfma_f32_32x32x16_bf16 v[80:95], v[108:111], v[188:191], v[80:95]\n"
      "s_waitcnt lgkmcnt(2)\n"
      "ds_read_b128 v[104:107], v10 offset:41984\n"
      "v_mfma_f32_32x32x16_bf16 v[80:95], v[112:115], v[192:195], v[80:95]\n"
      "s_add_u32 s94, s94, 4096\n"
      "ds_read_b128 v[108:111], v10 offset:44032\n"
      "buffer_load_dwordx4 a[80:83], v2, s[4:7], s94 offen offset:0 sc1\n"
      "v_mfma_f32_32x32x16_bf16 v[80:95], v[116:119], v[196:199], v[80:95]\n"
      "s_waitcnt lgkmcnt(2)\n"
      "ds_read_b128 v[112:115], v10 offset:46080\n"
      "v_mfma_f32_32x32x16_bf16 v[80:95], v[96:99], v[200:203], v[80:95]\n"
      "ds_read_b128 v[116:119], v10 offset:48128\n"
      "buffer_load_dwordx4 a[84:87], v2, s[4:7], s94 offen offset:2048 sc1\n"
      "v_mfma_f32_32x32x16_bf16 v[80:95], v[100:103], v[204:207], v[80:95]\n"
      "s_waitcnt lgkmcnt(2)\n"
      "ds_read_b128 v[96:99], v10 offset:50176\n"
      "v_mfma_f32_32x32x16_bf16 v[80:95], v[104:107], v[208:211], v[80:95]\n"
      "s_add_u32 s94, s94, 4096\n"
      "ds_read_b128 v[100:103], v10 offset:52224\n"
      "buffer_load_dwordx4 a[88:91], v2, s[4:7], s94 offen offset:0 sc1\n"
      "v_mfma_f32_32x32x16_bf16 v[80:95], v[108:111], v[212:215], v[80:95]\n"
      "s_waitcnt lgkmcnt(2)\n"
      "ds_read_b128 v[104:107], v10 offset:54272\n"
      "v_mfma_f32_32x32x16_bf16 v[80:95], v[112:115], v[216:219], v[80:95]\n"
      "ds_read_b128 v[108:111], v10 offset:56320\n"
      "buffer_load_dwordx4 a[92:95], v2, s[4:7], s94 offen offset:2048 sc1\n"
      "v_mfma_f32_32x32x16_bf16 v[80:95], v[116:119], v[220:223], v[80:95]\n"
      "s_waitcnt lgkmcnt(2)\n"
      "ds_read_b128 v[112:115], v10 offset:58368\n"
      "v_mfma_f32_32x32x16_bf16 v[80:95], v[96:99], v[224:227], v[80:95]\n"
      "s_add_u32 s94, s94, 4096\n"
      "ds_read_b128 v[116:119], v10 offset:60416\n"
      "buffer_load_dwordx4 a[96:99], v2, s[4:7], s94 offen offset:0 sc1\n"
      "v_mfma_f32_32x32x16_bf16 v[80:95], v[100:103], v[228:231], v[80:95]\n"
      "ds_read_b128 v[96:99], v10 offset:62464\n"
      "s_waitcnt vmcnt(12)\n"
      "v_cmp_ne_u32 vcc, s86, v13\n"
      "s_cbranch_vccnz .Lretry_47\n"
      ".Lback_48:\n"
      "s_waitcnt lgkmcnt(3)\n"
      "v_mfma_f32_32x32x16_bf16 v[80:95], v[104:107], v[232:235], v[80:95]\n"
      "ds_read_b128 v[100:103], v10 offset:64512\n"
      "s_waitcnt lgkmcnt(0)\n"
      "buffer_load_dwordx4 a[100:103], v2, s[4:7], s94 offen offset:2048 sc1\n"
      "s_barrier\n"
      "v_mfma_f32_32x32x16_bf16 v[80:95], v[108:111], v[236:239], v[80:95]\n"
      "s_add_u32 s94, s94, 4096\n"
      "ds_read_b128 v[104:107], v9 offset:0\n"
      "buffer_load_dwordx4 a[104:107], v2, s[4:7], s94 offen offset:0 sc1\n"
      "v_mfma_f32_32x32x16_bf16 v[80:95], v[112:115], v[240:243], v[80:95]\n"
      "s_add_u32 s87, s58, 0\n"
      "ds_read_b128 v[108:111], v9 offset:2048\n"
      "v_mfma_f32_32x32x16_bf16 v[80:95], v[116:119], v[244:247], v[80:95]\n"
      "ds_read_b128 v[112:115], v9 offset:4096\n"
      "buffer_load_dwordx4 a[108:111], v2, s[4:7], s94 offen offset:2048 sc1\n"
      "v_mfma_f32_32x32x16_bf16 v[80:95], v[96:99], v[248:251], v[80:95]\n"
      "s_add_u32 s94, s94, 4096\n"
      "ds_read_b128 v[116:119], v9 offset:6144\n"
      "v_mfma_f32_32x32x16_bf16 v[80:95], v[100:103], v[252:255], v[80:95]\n"
      "ds_read_b128 v[96:99], v9 offset:8192\n"
      "buffer_load_dwordx4 a[112:115], v2, s[4:7], s94 offen offset:0 sc1\n"
      "s_cmp_eq_u32 s59, 0\n"
      "s_cbranch_scc0 .Lsk_46\n"
      "buffer_store_dwordx4 v[120:123], v2, s[4:7], s87 offen sc1\n"
      ".Lsk_46:\n"
      "s_waitcnt lgkmcnt(3)\n"
      "v_mfma_f32_32x32x16_bf16 v[32:47], v[104:107], v[128:131], 0\n"
      "ds_read_b128 v[100:103], v9 offset:10240\n"
      "v_mul_f32 v48, s95, v48\n"
      "v_exp_f32 v48, v48\n"
      "v_mul_f32 v49, s95, v49\n"
      "v_add_f32 v48, 1.0, v48\n"
      "v_mfma_f32_32x32x16_bf16 v[32:47], v[108:111], v[132:135], v[32:47]\n"
      "s_waitcnt lgkmcnt(2)\n"
      "ds_read_b128 v[104:107], v9 offset:12288\n"
      "v_exp_f32 v49, v49\n"
      "v_mul_f32 v50, s95, v50\n"
      "v_rcp_f32 v48, v48\n"
      "v_mfma_f32_32x32x16_bf16 v[32:47], v[112:115], v[136:139], v[32:47]\n"
      "ds_read_b128 v[108:111], v9 offset:14336\n"
      "v_add_f32 v49, 1.0, v49\n"
      "v_exp_f32 v50, v50\n"
      "v_mul_f32 v51, s95, v51\n"
      "v_mul_f32 v48, v48, v56\n"
      "v_mfma_f32_32x32x16_bf16 v[32:47], v[116:119], v[140:143], v[32:47]\n"
      "s_waitcnt lgkmcnt(2)\n"
      "ds_read_b128 v[112:115], v9 offset:16384\n"
      "v_rcp_f32 v49, v49\n"
      "v_add_f32 v50, 1.0, v50\n"
      "v_exp_f32 v51, v51\n"
      "v_mfma_f32_32x32x16_bf16 v[32:47], v[96:99], v[144:147], v[32:47]\n"
      "ds_read_b128 v[116:119], v9 offset:18432\n"
      "v_mul_f32 v52, s95, v52\n"
      "v_mul_f32 v49, v49, v57\n"
      "v_rcp_f32 v50, v50\n"
      "v_add_f32 v51, 1.0, v51\n"
      "v_mfma_f32_32x32x16_bf16 v[32:47], v[100:103], v[148:151], v[32:47]\n"
      "s_waitcnt lgkmcnt(2)\n"
      "ds_read_b128 v[96:99], v9 offset:20480\n"
      "buffer_load_dwordx4 a[116:119], v2, s[4:7], s94 offen offset:2048 sc1\n"
      "v_exp_f32 v52, v52\n"
      "v_mfma_f32_32x32x16_bf16 v[32:47], v[104:107], v[152:155], v[32:47]\n"
      "s_add_u32 s94, s94, 4096\n"
      "ds_read_b128 v[100:103], v9 offset:22528\n"
      "v_mul_f32 v53, s95, v53\n"
      "v_mul_f32 v50, v50, v58\n"
      "v_rcp_f32 v51, v51\n"
      "v_add_f32 v52, 1.0, v52\n"
      "v_mfma_f32_32x32x16_bf16 v[32:47], v[108:111], v[156:159], v[32:47]\n"
      "s_waitcnt lgkmcnt(2)\n"
      "ds_read_b128 v[104:107], v9 offset:24576\n"
      "buffer_load_dwordx4 a[120:123], v2, s[4:7], s94 offen offset:0 sc1\n"
      "v_exp_f32 v53, v53\n"
      "v_mfma_f32_32x32x16_bf16 v[32:47], v[112:115], v[160:163], v[32:47]\n"
      "ds_read_b128 v[108:111], v9 offset:26624\n"
      "v_mul_f32 v54, s95, v54\n"
      "v_mul_f32 v51, v51, v59\n"
      "v_rcp_f32 v52, v52\n"
      "v_add_f32 v53, 1.0, v53\n"
      "v_mfma_f32_32x32x16_bf16 v[32:47], v[116:119], v[164:167], v[32:47]\n"
      "s_waitcnt lgkmcnt(2)\n"
      "ds_read_b128 v[112:115], v9 offset:28672\n"
      "buffer_load_dwordx4 a[124:127], v2, s[4:7], s94 offen offset:2048 sc1\n"
      "v_exp_f32 v54, v54\n"
      "v_mfma_f32_32x32x16_bf16 v[32:47], v[96:99], v[168:171], v[32:47]\n"
      "s_add_u32 s94, s94, 4096\n"
      "ds_read_b128 v[116:119], v9 offset:30720\n"
      "v_mul_f32 v55, s95, v55\n"
      "v_mul_f32 v52, v52, v60\n"
      "v_rcp_f32 v53, v53\n"
      "v_add_f32 v54, 1.0, v54\n"
      "v_mfma_f32_32x32x16_bf16 v[32:47], v[100:103], v[172:175], v[32:47]\n"
      "s_waitcnt lgkmcnt(2)\n"
      "ds_read_b128 v[96:99], v9 offset:32768\n"
      "buffer_load_dwordx4 a[128:131], v2, s[4:7], s94 offen offset:0 sc1\n"
      "v_exp_f32 v55, v55\n"
      "v_mfma_f32_32x32x16_bf16 v[32:47], v[104:107], v[176:179], v[32:47]\n"
      "ds_read_b128 v[100:103], v9 offset:34816\n"
      "v_mul_f32 v53, v53, v61\n"
      "v_rcp_f32 v54, v54\n"
      "v_add_f32 v55, 1.0, v55\n"
      "v_mul_f32 v54, v54, v62\n"
      "v_mfma_f32_32x32x16_bf16 v[32:47], v[108:111], v[180:183], v[32:47]\n"
      "s_waitcnt lgkmcnt(2)\n"
      "ds_read_b128 v[104:107], v9 offset:36864\n"
      "buffer_load_dwordx4 a[132:135], v2, s[4:7], s94 offen offset:2048 sc1\n"
      "v_rcp_f32 v55, v55\n"
      "v_mfma_f32_32x32x16_bf16 v[32:47], v[112:115], v[184:187], v[32:47]\n"
      "s_add_u32 s94, s94, 4096\n"
      "ds_read_b128 v[108:111], v9 offset:38912\n"
      "v_mul_f32 v55, v55, v63\n"
      "v_cvt_pk_bf16_f32 v124, v48, v49\n"
      "v_cvt_pk_bf16_f32 v125, v50, v51\n"
      "v_cvt_pk_bf16_f32 v126, v52, v53\n"
      "v_cvt_pk_bf16_f32 v127, v54, v55\n"
      "v_mfma_f32_32x32x16_bf16 v[32:47], v[116:119], v[188:191], v[32:47]\n"
      "s_waitcnt lgkmcnt(2)\n"
      "ds_read_b128 v[112:115], v9 offset:40960\n"
      "buffer_load_dwordx4 a[136:139], v2, s[4:7], s94 offen offset:0 sc1\n"
      "v_mfma_f32_32x32x16_bf16 v[32:47], v[96:99], v[192:195], v[32:47]\n"
      "s_add_u32 s87, s58, 8192\n"
      "ds_read_b128 v[116:119], v9 offset:43008\n"
      "buffer_store_dwordx4 v[124:127], v2, s[4:7], s87 offen sc1\n"
      "v_mul_f32 v64, s95, v64\n"
      "v_mfma_f32_32x32x16_bf16 v[32:47], v[100:103], v[196:199], v[32:47]\n"
      "s_waitcnt lgkmcnt(2)\n"
      "ds_read_b128 v[96:99], v9 offset:45056\n"
      "v_exp_f32 v64, v64\n"
      "v_mul_f32 v65, s95, v65\n"
      "v_add_f32 v64, 1.0, v64\n"
      "v_mul_f32 v66, s95, v66\n"
      "v_mfma_f32_32x32x16_bf16 v[32:47], v[104:107], v[200:203], v[32:47]\n"
      "ds_read_b128 v[100:103], v9 offset:47104\n"
      "v_exp_f32 v65, v65\n"
      "v_rcp_f32 v64, v64\n"
      "v_add_f32 v65, 1.0, v65\n"
      "v_mfma_f32_32x32x16_bf16 v[32:47], v[108:111], v[204:207], v[32:47]\n"
      "s_waitcnt lgkmcnt(2)\n"
      "ds_read_b128 v[104:107], v9 offset:49152\n"
      "v_exp_f32 v66, v66\n"
      "v_mul_f32 v67, s95, v67\n"
      "v_mul_f32 v64, v64, v72\n"
      "v_add_f32 v66, 1.0, v66\n"
      "v_mfma_f32_32x32x16_bf16 v[32:47], v[112:115], v[208:211], v[32:47]\n"
      "ds_read_b128 v[108:111], v9 offset:51200\n"
      "v_rcp_f32 v65, v65\n"
      "v_exp_f32 v67, v67\n"
      "v_mul_f32 v68, s95, v68\n"
      "v_mfma_f32_32x32x16_bf16 v[32:47], v[116:119], v[212:215], v[32:47]\n"
      "s_waitcnt lgkmcnt(2)\n"
      "ds_read_b128 v[112:115], v9 offset:53248\n"
      "v_mul_f32 v65, v65, v73\n"
      "v_rcp_f32 v66, v66\n"
      "v_add_f32 v67, 1.0, v67\n"
      "v_mul_f32 v69, s95, v69\n"
      "v_mfma_f32_32x32x16_bf16 v[32:47], v[96:99], v[216:219], v[32:47]\n"
      "ds_read_b128 v[116:119], v9 offset:55296\n"
      "buffer_load_dwordx4 a[140:143], v2, s[4:7], s94 offen offset:2048 sc1\n"
      "v_exp_f32 v68, v68\n"
      "v_mfma_f32_32x32x16_bf16 v[32:47], v[100:103], v[220:223], v[32:47]\n"
      "s_waitcnt lgkmcnt(2)\n"
      "ds_read_b128 v[96:99], v9 offset:57344\n"
      "v_mul_f32 v66, v66, v74\n"
      "v_rcp_f32 v67, v67\n"
      "v_add_f32 v68, 1.0, v68\n"
      "v_mul_f32 v70, s95, v70\n"
      "v_mfma_f32_32x32x16_bf16 v[32:47], v[104:107], v[224:227], v[32:47]\n"
      "s_add_u32 s94, s94, 4096\n"
      "ds_read_b128 v[100:103], v9 offset:59392\n"
      "buffer_load_dwordx4 a[144:147], v2, s[4:7], s94 offen offset:0 sc1\n"
      "v_exp_f32 v69, v69\n"
      "v_mfma_f32_32x32x16_bf16 v[32:47], v[108:111], v[228:231], v[32:47]\n"
      "s_waitcnt lgkmcnt(2)\n"
      "ds_read_b128 v[104:107], v9 offset:61440\n"
      "v_mul_f32 v67, v67, v75\n"
      "v_rcp_f32 v68, v68\n"
      "v_add_f32 v69, 1.0, v69\n"
      "v_mul_f32 v71, s95, v71\n"
      "v_mfma_f32_32x32x16_bf16 v[32:47], v[112:115], v[232:235], v[32:47]\n"
      "ds_read_b128 v[108:111], v9 offset:63488\n"
      "buffer_load_dwordx4 a[148:151], v2, s[4:7], s94 offen offset:2048 sc1\n"
      "v_exp_f32 v70, v70\n"
      "s_barrier\n"
      "v_mfma_f32_32x32x16_bf16 v[32:47], v[116:119], v[236:239], v[32:47]\n"
      "s_waitcnt lgkmcnt(2)\n"
      "ds_read_b128 v[112:115], v9 offset:1024\n"
      "s_add_u32 s94, s94, 4096\n"
      "buffer_load_dwordx4 a[152:155], v2, s[4:7], s94 offen offset:0 sc1\n"
      "v_mul_f32 v68, v68, v76\n"
      "v_add_f32 v70, 1.0, v70\n"
      "v_mfma_f32_32x32x16_bf16 v[32:47], v[96:99], v[240:243], v[32:47]\n"
      "ds_read_b128 v[116:119], v9 offset:3072\n"
      "v_rcp_f32 v69, v69\n"
      "v_exp_f32 v71, v71\n"
      "v_mul_f32 v69, v69, v77\n"
      "v_mfma_f32_32x32x16_bf16 v[32:47], v[100:103], v[244:247], v[32:47]\n"
      "s_waitcnt lgkmcnt(2)\n"
      "ds_read_b128 v[96:99], v9 offset:5120\n"
      "buffer_load_dwordx4 a[156:159], v2, s[4:7], s94 offen offset:2048 sc1\n"
      "v_rcp_f32 v70, v70\n"
      "v_mfma_f32_32x32x16_bf16 v[32:47], v[104:107], v[248:251], v[32:47]\n"
      "s_add_u32 s94, s94, 4096\n"
      "ds_read_b128 v[100:103], v9 offset:7168\n"
      "v_add_f32 v71, 1.0, v71\n"
      "v_mul_f32 v70, v70, v78\n"
      "v_rcp_f32 v71, v71\n"
      "s_nop 0\n"
      "v_mul_f32 v71, v71, v79\n"
      "v_mfma_f32_32x32x16_bf16 v[32:47], v[108:111], v[252:255], v[32:47]\n"
      "s_waitcnt lgkmcnt(2)\n"
      "ds_read_b128 v[104:107], v9 offset:9216\n"
      "buffer_load_dwordx4 a[160:163], v2, s[4:7], s94 offen offset:0 sc1\n"
      "v_cvt_pk_bf16_f32 v120, v64, v65\n"
      "v_cvt_pk_bf16_f32 v121, v66, v67\n"
      "v_mfma_f32_32x32x16_bf16 v[48:63], v[112:115], v[128:131], 0\n"
      "s_add_u32 s87, s58, 24576\n"
      "ds_read_b128 v[108:111], v9 offset:11264\n"
      "v_cvt_pk_bf16_f32 v122, v68, v69\n"
      "v_cvt_pk_bf16_f32 v123, v70, v71\n"
      "v_mfma_f32_32x32x16_bf16 v[48:63], v[116:119], v[132:135], v[48:63]\n"
      "s_waitcnt lgkmcnt(2)\n"
      "ds_read_b128 v[112:115], v9 offset:13312\n"
      "buffer_load_dwordx4 a[164:167], v2, s[4:7], s94 offen offset:2048 sc1\n"
      "v_mfma_f32_32x32x16_bf16 v[48:63], v[96:99], v[136:139], v[48:63]\n"
      "ds_read_b128 v[116:119], v9 offset:15360\n"
      "buffer_store_dwordx4 v[120:123], v2, s[4:7], s87 offen sc1\n"
      "v_mul_f32 v80, s95, v80\n"
      "v_mfma_f32_32x32x16_bf16 v[48:63], v[100:103], v[140:143], v[48:63]\n"
      "s_waitcnt lgkmcnt(2)\n"
      "ds_read_b128 v[96:99], v9 offset:17408\n"
      "v_exp_f32 v80, v80\n"
      "v_mul_f32 v81, s95, v81\n"
      "v_add_f32 v80, 1.0, v80\n"
      "v_mul_f32 v82, s95, v82\n"
      "v_mfma_f32_32x32x16_bf16 v[48:63], v[104:107], v[144:147], v[48:63]\n"
      "ds_read_b128 v[100:103], v9 offset:19456\n"
      "v_exp_f32 v81, v81\n"
      "v_rcp_f32 v80, v80\n"
      "v_add_f32 v81, 1.0, v81\n"
      "v_mfma_f32_32x32x16_bf16 v[48:63], v[108:111], v[148:151], v[48:63]\n"
      "s_waitcnt lgkmcnt(2)\n"
      "ds_read_b128 v[104:107], v9 offset:21504\n"
      "v_exp_f32 v82, v82\n"
      "v_mul_f32 v83, s95, v83\n"
      "v_mul_f32 v80, v80, v88\n"
      "v_add_f32 v82, 1.0, v82\n"
      "v_mfma_f32_32x32x16_bf16 v[48:63], v[112:115], v[152:155], v[48:63]\n"
      "s_add_u32 s94, s94, 4096\n"
      "ds_read_b128 v[108:111], v9 offset:23552\n"
      "v_rcp_f32 v81, v81\n"
      "v_exp_f32 v83, v83\n"
      "v_mul_f32 v84, s95, v84\n"
      "v_mfma_f32_32x32x16_bf16 v[48:63], v[116:119], v[156:159], v[48:63]\n"
      "s_waitcnt lgkmcnt(2)\n"
      "ds_read_b128 v[112:115], v9 offset:25600\n"
      "buffer_load_dwordx4 a[168:171], v2, s[4:7], s94 offen offset:0 sc1\n"
      "v_mul_f32 v81, v81, v89\n"
      "v_add_f32 v83, 1.0, v83\n"
      "v_mfma_f32_32x32x16_bf16 v[48:63], v[96:99], v[160:163], v[48:63]\n"
      "ds_read_b128 v[116:119], v9 offset:27648\n"
      "v_rcp_f32 v82, v82\n"
      "v_exp_f32 v84, v84\n"
      "v_mul_f32 v85, s95, v85\n"
      "v_mfma_f32_32x32x16_bf16 v[48:63], v[100:103], v[164:167], v[48:63]\n"
      "s_waitcnt lgkmcnt(2)\n"
      "ds_read_b128 v[96:99], v9 offset:29696\n"
      "buffer_load_dwordx4 a[172:175], v2, s[4:7], s94 offen offset:2048 sc1\n"
      "v_mul_f32 v82, v82, v90\n"
      "v_add_f32 v84, 1.0, v84\n"
      "v_mfma_f32_32x32x16_bf16 v[48:63], v[104:107], v[168:171], v[48:63]\n"
      "s_add_u32 s20, s61, 0\n"
      "ds_read_b128 v[100:103], v9 offset:31744\n"
      "v_rcp_f32 v83, v83\n"
      "v_exp_f32 v85, v85\n"
      "v_mul_f32 v86, s95, v86\n"
      "v_mfma_f32_32x32x16_bf16 v[48:63], v[108:111], v[172:175], v[48:63]\n"
      "s_waitcnt lgkmcnt(2)\n"
      "ds_read_b128 v[104:107], v9 offset:33792\n"
      "buffer_load_dwordx4 a[176:179], v2, s[4:7], s20 offen offset:0 sc1\n"
      "v_mul_f32 v83, v83, v91\n"
      "v_add_f32 v85, 1.0, v85\n"
      "v_mfma_f32_32x32x16_bf16 v[48:63], v[112:115], v[176:179], v[48:63]\n"
      "ds_read_b128 v[108:111], v9 offset:35840\n"
      "v_rcp_f32 v84, v84\n"
      "v_exp_f32 v86, v86\n"
      "v_mul_f32 v87, s95, v87\n"
      "v_mfma_f32_32x32x16_bf16 v[48:63], v[116:119], v[180:183], v[48:63]\n"
      "s_waitcnt lgkmcnt(2)\n"
      "ds_read_b128 v[112:115], v9 offset:37888\n"
      "buffer_load_dwordx4 a[180:183], v2, s[4:7], s20 offen offset:2048 sc1\n"
      "v_mul_f32 v84, v84, v92\n"
      "v_add_f32 v86, 1.0, v86\n"
      "v_mfma_f32_32x32x16_bf16 v[48:63], v[96:99], v[184:187], v[48:63]\n"
      "s_add_u32 s20, s20, 4096\n"
      "ds_read_b128 v[116:119], v9 offset:39936\n"
      "v_rcp_f32 v85, v85\n"
      "v_exp_f32 v87, v87\n"
      "v_mul_f32 v85, v85, v93\n"
      "v_mfma_f32_32x32x16_bf16 v[48:63], v[100:103], v[188:191], v[48:63]\n"
      "s_waitcnt lgkmcnt(2)\n"
      "ds_read_b128 v[96:99], v9 offset:41984\n"
      "buffer_load_dwordx4 a[184:187], v4, s[4:7], s20 offen offset:0 sc1\n"
      "v_rcp_f32 v86, v86\n"
      "v_mfma_f32_32x32x16_bf16 v[48:63], v[104:107], v[192:195], v[48:63]\n"
      "ds_read_b128 v[100:103], v9 offset:44032\n"
      "v_add_f32 v87, 1.0, v87\n"
      "v_mul_f32 v86, v86, v94\n"
      "v_rcp_f32 v87, v87\n"
      "s_nop 0\n"
      "v_mul_f32 v87, v87, v95\n"
      "v_mfma_f32_32x32x16_bf16 v[48:63], v[108:111], v[196:199], v[48:63]\n"
      "s_waitcnt lgkmcnt(2)\n"
      "ds_read_b128 v[104:107], v9 offset:46080\n"
      "buffer_load_dwordx4 a[188:191], v4, s[4:7], s20 offen offset:2048 sc1\n"
      "v_cvt_pk_bf16_f32 v124, v80, v81\n"
      "v_cvt_pk_bf16_f32 v125, v82, v83\n"
      "v_mfma_f32_32x32x16_bf16 v[48:63], v[112:115], v[200:203], v[48:63]\n"
      "s_add_u32 s20, s20, 4096\n"
      "ds_read_b128 v[108:111], v9 offset:48128\n"
      "v_cvt_pk_bf16_f32 v126, v84, v85\n"
      "v_cvt_pk_bf16_f32 v127, v86, v87\n"
      "v_mfma_f32_32x32x16_bf16 v[48:63], v[116:119], v[204:207], v[48:63]\n"
      "s_waitcnt lgkmcnt(2)\n"
      "ds_read_b128 v[112:115], v9 offset:50176\n"
      "buffer_load_dwordx4 a[192:195], v2, s[4:7], s20 offen offset:0 sc1\n"
      "v_mfma_f32_32x32x16_bf16 v[48:63], v[96:99], v[208:211], v[48:63]\n"
      "s_add_u32 s87, s58, 40960\n"
      "ds_read_b128 v[116:119], v9 offset:52224\n"
      "buffer_store_dwordx4 v[124:127], v2, s[4:7], s87 offen sc1\n"
      "v_mul_f32 v32, s95, v32\n"
      "v_mfma_f32_32x32x16_bf16 v[48:63], v[100:103], v[212:215], v[48:63]\n"
      "s_waitcnt lgkmcnt(2)\n"
      "ds_read_b128 v[96:99], v9 offset:54272\n"
      "v_exp_f32 v32, v32\n"
      "v_mul_f32 v33, s95, v33\n"
      "v_add_f32 v32, 1.0, v32\n"
      "v_mul_f32 v34, s95, v34\n"
      "v_mfma_f32_32x32x16_bf16 v[48:63], v[104:107], v[216:219], v[48:63]\n"
      "ds_read_b128 v[100:103], v9 offset:56320\n"
      "v_exp_f32 v33, v33\n"
      "v_rcp_f32 v32, v32\n"
      "v_add_f32 v33, 1.0, v33\n"
      "v_mfma_f32_32x32x16_bf16 v[48:63], v[108:111], v[220:223], v[48:63]\n"
      "s_waitcnt lgkmcnt(2)\n"
      "ds_read_b128 v[104:107], v9 offset:58368\n"
      "v_exp_f32 v34, v34\n"
      "v_mul_f32 v35, s95, v35\n"
      "v_mul_f32 v32, v32, v40\n"
      "v_add_f32 v34, 1.0, v34\n"
      "v_mfma_f32_32x32x16_bf16 v[48:63], v[112:115], v[224:227], v[48:63]\n"
      "ds_read_b128 v[108:111], v9 offset:60416\n"
      "v_rcp_f32 v33, v33\n"
      "v_exp_f32 v35, v35\n"
      "v_mul_f32 v36, s95, v36\n"
      "v_mfma_f32_32x32x16_bf16 v[48:63], v[116:119], v[228:231], v[48:63]\n"
      "s_waitcnt lgkmcnt(2)\n"
      "ds_read_b128 v[112:115], v9 offset:62464\n"
      "v_mul_f32 v33, v33, v41\n"
      "v_rcp_f32 v34, v34\n"
      "v_add_f32 v35, 1.0, v35\n"
      "v_mul_f32 v37, s95, v37\n"
      "v_mfma_f32_32x32x16_bf16 v[48:63], v[96:99], v[232:235], v[48:63]\n"
      "ds_read_b128 v[116:119], v9 offset:64512\n"
      "buffer_load_dwordx4 a[196:199], v2, s[4:7], s20 offen offset:2048 sc1\n"
      "v_exp_f32 v36, v36\n"
      "v_mfma_f32_32x32x16_bf16 v[48:63], v[100:103], v[236:239], v[48:63]\n"
      "s_waitcnt lgkmcnt(2)\n"
      "v_mul_f32 v34, v34, v42\n"
      "v_rcp_f32 v35, v35\n"
      "v_add_f32 v36, 1.0, v36\n"
      "v_exp_f32 v37, v37\n"
      "v_mfma_f32_32x32x16_bf16 v[48:63], v[104:107], v[240:243], v[48:63]\n"
      "s_add_u32 s20, s20, 4096\n"
      "buffer_load_dwordx4 a[200:203], v2, s[4:7], s20 offen offset:0 sc1\n"
      "v_mul_f32 v38, s95, v38\n"
      "v_mul_f32 v35, v35, v43\n"
      "v_add_f32 v37, 1.0, v37\n"
      "v_mfma_f32_32x32x16_bf16 v[48:63], v[108:111], v[244:247], v[48:63]\n"
      "s_waitcnt lgkmcnt(1)\n"
      "v_rcp_f32 v36, v36\n"
      "v_exp_f32 v38, v38\n"
      "v_mul_f32 v39, s95, v39\n"
      "v_mul_f32 v36, v36, v44\n"
      "v_mfma_f32_32x32x16_bf16 v[48:63], v[112:115], v[248:251], v[48:63]\n"
      "buffer_load_dwordx4 a[204:207], v2, s[4:7], s20 offen offset:2048 sc1\n"
      "v_rcp_f32 v37, v37\n"
      "v_add_f32 v38, 1.0, v38\n"
      "s_waitcnt lgkmcnt(0)\n"
      "v_mfma_f32_32x32x16_bf16 v[48:63], v[116:119], v[252:255], v[48:63]\n"
      "s_add_u32 s87, s58, 57344\n"
      "v_exp_f32 v39, v39\n"
      "v_mul_f32 v37, v37, v45\n"
      "v_rcp_f32 v38, v38\n"
      "v_add_f32 v39, 1.0, v39\n"
      "v_mul_f32 v38, v38, v46\n"
      "v_rcp_f32 v39, v39\n"
      "s_nop 0\n"
      "v_mul_f32 v39, v39, v47\n"
      "v_cvt_pk_bf16_f32 v120, v32, v33\n"
      "v_cvt_pk_bf16_f32 v121, v34, v35\n"
      "v_cvt_pk_bf16_f32 v122, v36, v37\n"
      "v_cvt_pk_bf16_f32 v123, v38, v39\n"
      "buffer_store_dwordx4 v[120:123], v2, s[4:7], s87 offen sc1\n"
      "v_mul_f32 v48, s95, v48\n"
      "v_exp_f32 v48, v48\n"
      "v_mul_f32 v49, s95, v49\n"
      "v_add_f32 v48, 1.0, v48\n"
      "v_exp_f32 v49, v49\n"
      "v_mul_f32 v50, s95, v50\n"
      "v_rcp_f32 v48, v48\n"
      "v_add_f32 v49, 1.0, v49\n"
      "v_exp_f32 v50, v50\n"
      "v_mul_f32 v51, s95, v51\n"
      "v_mul_f32 v48, v48, v56\n"
      "v_rcp_f32 v49, v49\n"
      "v_add_f32 v50, 1.0, v50\n"
      "v_exp_f32 v51, v51\n"
      "v_mul_f32 v52, s95, v52\n"
      "v_mul_f32 v49, v49, v57\n"
      "v_rcp_f32 v50, v50\n"
      "v_add_f32 v51, 1.0, v51\n"
      "v_exp_f32 v52, v52\n"
      "v_mul_f32 v53, s95, v53\n"
      "v_mul_f32 v50, v50, v58\n"
      "v_rcp_f32 v51, v51\n"
      "v_add_f32 v52, 1.0, v52\n"
      "v_exp_f32 v53, v53\n"
      "v_mul_f32 v54, s95, v54\n"
      "v_mul_f32 v51, v51, v59\n"
      "v_rcp_f32 v52, v52\n"
      "v_add_f32 v53, 1.0, v53\n"
      "v_exp_f32 v54, v54\n"
      "v_mul_f32 v55, s95, v55\n"
      "v_mul_f32 v52, v52, v60\n"
      "v_rcp_f32 v53, v53\n"
      "v_add_f32 v54, 1.0, v54\n"
      "v_exp_f32 v55, v55\n"
      "v_mul_f32 v53, v53, v61\n"
      "v_rcp_f32 v54, v54\n"
      "v_add_f32 v55, 1.0, v55\n"
      "v_mul_f32 v54, v54, v62\n"
      "v_rcp_f32 v55, v55\n"
      "s_nop 0\n"
      "v_mul_f32 v55, v55, v63\n"
      "v_cvt_pk_bf16_f32 v124, v48, v49\n"
      "v_cvt_pk_bf16_f32 v125, v50, v51\n"
      "v_cvt_pk_bf16_f32 v126, v52, v53\n"
      "v_cvt_pk_bf16_f32 v127, v54, v55\n"
      "s_add_u32 s87, s58, 73728\n"
      "buffer_store_dwordx4 v[124:127], v2, s[4:7], s87 offen sc1\n"
      "s_waitcnt vmcnt(10)\n"
      "v_mfma_f32_32x32x16_bf16 v[32:47], a[0:3], a[176:179], 0\n"
      "v_accvgpr_read_b32 v124, a176\n"
      "v_accvgpr_read_b32 v125, a177\n"
      "v_max_u32 v15, v124, v125\n"
      "v_accvgpr_read_b32 v124, a178\n"
      "v_accvgpr_read_b32 v125, a179\n"
      "v_mfma_f32_32x32x16_bf16 v[48:63], a[4:7], a[176:179], 0\n"
      "s_add_u32 s20, s20, 4096\n"
      "buffer_load_dwordx4 a[208:211], v2, s[4:7], s20 offen offset:0 sc1\n"
      "v_max3_u32 v15, v15, v124, v125\n"
      "s_waitcnt vmcnt(10)\n"
      "v_accvgpr_read_b32 v124, a180\n"
      "v_mfma_f32_32x32x16_bf16 v[64:79], a[0:3], a[180:183], 0\n"
      "v_accvgpr_read_b32 v125, a181\n"
      "v_max3_u32 v15, v15, v124, v125\n"
      "v_accvgpr_read_b32 v124, a182\n"
      "v_accvgpr_read_b32 v125, a183\n"
      "v_max3_u32 v15, v15, v124, v125\n"
      "v_mfma_f32_32x32x16_bf16 v[80:95], a[4:7], a[180:183], 0\n"
      "buffer_load_dwordx4 a[212:215], v2, s[4:7], s20 offen offset:2048 sc1\n"
      "s_waitcnt vmcnt(10)\n"
      "v_accvgpr_read_b32 v124, a184\n"
      "v_accvgpr_read_b32 v125, a185\n"
      "v_mfma_f32_32x32x16_bf16 v[32:47], a[8:11], a[184:187], v[32:47]\n"
      "v_max3_u32 v15, v15, v124, v125\n"
      "v_accvgpr_read_b32 v124, a186\n"
      "v_accvgpr_read_b32 v125, a187\n"
      "v_max3_u32 v15, v15, v124, v125\n"
      "s_waitcnt vmcnt(9)\n"
      "v_accvgpr_read_b32 v124, a188\n"
      "v_mfma_f32_32x32x16_bf16 v[48:63], a[12:15], a[184:187], v[48:63]\n"
      "s_add_u32 s20, s20, 4096\n"
      "buffer_load_dwordx4 a[216:219], v2, s[4:7], s20 offen offset:0 sc1\n"
      "v_accvgpr_read_b32 v125, a189\n"
      "v_max3_u32 v15, v15, v124, v125\n"
      "v_mfma_f32_32x32x16_bf16 v[64:79], a[8:11], a[188:191], v[64:79]\n"
      "v_accvgpr_read_b32 v124, a190\n"
      "v_accvgpr_read_b32 v125, a191\n"
      "v_max3_u32 v15, v15, v124, v125\n"
      "s_waitcnt vmcnt(9)\n"
      "v_accvgpr_read_b32 v124, a192\n"
      "v_accvgpr_read_b32 v125, a193\n"
      "v_mfma_f32_32x32x16_bf16 v[80:95], a[12:15], a[188:191], v[80:95]\n"
      "buffer_load_dwordx4 a[220:223], v2, s[4:7], s20 offen offset:2048 sc1\n"
      "v_max3_u32 v15, v15, v124, v125\n"
      "v_accvgpr_read_b32 v124, a194\n"
      "v_accvgpr_read_b32 v125, a195\n"
      "v_max3_u32 v15, v15, v124, v125\n"
      "s_waitcnt vmcnt(8)\n"
      "v_accvgpr_read_b32 v124, a196\n"
      "v_accvgpr_read_b32 v125, a197\n"
      "v_max3_u32 v15, v15, v124, v125\n"
      "v_accvgpr_read_b32 v124, a198\n"
      "v_accvgpr_read_b32 v125, a199\n"
      "v_max3_u32 v15, v15, v124, v125\n"
      "s_waitcnt vmcnt(7)\n"
      "v_accvgpr_read_b32 v124, a200\n"
      "v_accvgpr_read_b32 v125, a201\n"
      "v_max3_u32 v15, v15, v124, v125\n"
      "v_accvgpr_read_b32 v124, a202\n"
      "v_accvgpr_read_b32 v125, a203\n"
      "v_max3_u32 v15, v15, v124, v125\n"
      "s_waitcnt vmcnt(6)\n"
      "v_accvgpr_read_b32 v124, a204\n"
      "v_accvgpr_read_b32 v125, a205\n"
      "v_max3_u32 v15, v15, v124, v125\n"
      "v_accvgpr_read_b32 v124, a206\n"
      "v_accvgpr_read_b32 v125, a207\n"
      "v_max3_u32 v15, v15, v124, v125\n"
      "v_cmp_eq_u32 vcc, -1, v15\n"
      "s_cbranch_vccnz .Lretry_49\n"
      ".Lback_50:\n"
      "v_mfma_f32_32x32x16_bf16 v[32:47], a[16:19], a[192:195], v[32:47]\n"
      "s_add_u32 s20, s61, 24576\n"
      "buffer_load_dwordx4 v[128:131], v2, s[4:7], s20 offen offset:0 sc1\n"
      "v_mfma_f32_32x32x16_bf16 v[48:63], a[20:23], a[192:195], v[48:63]\n"
      "v_mfma_f32_32x32x16_bf16 v[64:79], a[16:19], a[196:199], v[64:79]\n"
      "buffer_load_dwordx4 v[132:135], v2, s[4:7], s20 offen offset:2048 sc1\n"
      "v_mfma_f32_32x32x16_bf16 v[80:95], a[20:23], a[196:199], v[80:95]\n"
      "v_mfma_f32_32x32x16_bf16 v[32:47], a[24:27], a[200:203], v[32:47]\n"
      "s_add_u32 s20, s20, 4096\n"
      "buffer_load_dwordx4 v[136:139], v2, s[4:7], s20 offen offset:0 sc1\n"
      "v_mfma_f32_32x32x16_bf16 v[48:63], a[28:31], a[200:203], v[48:63]\n"
      "v_mfma_f32_32x32x16_bf16 v[64:79], a[24:27], a[204:207], v[64:79]\n"
      "buffer_load_dwordx4 v[140:143], v2, s[4:7], s20 offen offset:2048 sc1\n"
      "v_mfma_f32_32x32x16_bf16 v[80:95], a[28:31], a[204:207], v[80:95]\n"
      "s_add_u32 s20, s20, 4096\n"
      "buffer_load_dwordx4 v[144:147], v2, s[4:7], s20 offen offset:0 sc1\n"
      "buffer_load_dwordx4 v[148:151], v2, s[4:7], s20 offen offset:2048 sc1\n"
      "s_add_u32 s20, s20, 4096\n"
      "buffer_load_dwordx4 v[152:155], v2, s[4:7], s20 offen offset:0 sc1\n"
      "s_waitcnt vmcnt(10)\n"
      "v_accvgpr_read_b32 v124, a208\n"
      "v_accvgpr_read_b32 v125, a209\n"
      "v_max_u32 v15, v124, v125\n"
      "v_accvgpr_read_b32 v124, a210\n"
      "v_accvgpr_read_b32 v125, a211\n"
      "v_max3_u32 v15, v15, v124, v125\n"
      "buffer_load_dwordx4 v[156:159], v2, s[4:7], s20 offen offset:2048 sc1\n"
      "s_waitcnt vmcnt(10)\n"
      "v_accvgpr_read_b32 v124, a212\n"
      "v_accvgpr_read_b32 v125, a213\n"
      "v_max3_u32 v15, v15, v124, v125\n"
      "v_accvgpr_read_b32 v124, a214\n"
      "v_accvgpr_read_b32 v125, a215\n"
      "v_max3_u32 v15, v15, v124, v125\n"
      "s_add_u32 s20, s20, 4096\n"
      "buffer_load_dwordx4 v[160:163], v2, s[4:7], s20 offen offset:0 sc1\n"
      "s_waitcnt vmcnt(10)\n"
      "v_accvgpr_read_b32 v124, a216\n"
      "v_accvgpr_read_b32 v125, a217\n"
      "v_max3_u32 v15, v15, v124, v125\n"
      "v_accvgpr_read_b32 v124, a218\n"
      "v_accvgpr_read_b32 v125, a219\n"
      "v_max3_u32 v15, v15, v124, v125\n"
      "buffer_load_dwordx4 v[164:167], v2, s[4:7], s20 offen offset:2048 sc1\n"
      "s_waitcnt vmcnt(10)\n"
      "v_accvgpr_read_b32 v124, a220\n"
      "v_accvgpr_read_b32 v125, a221\n"
      "v_max3_u32 v15, v15, v124, v125\n"
      "v_accvgpr_read_b32 v124, a222\n"
      "v_accvgpr_read_b32 v125, a223\n"
      "v_max3_u32 v15, v15, v124, v125\n"
      "v_cmp_eq_u32 vcc, -1, v15\n"
      "s_cbranch_vccnz .Lretry_51\n"
      ".Lback_52:\n"
      "v_mfma_f32_32x32x16_bf16 v[32:47], a[32:35], a[208:211], v[32:47]\n"
      "v_mfma_f32_32x32x16_bf16 v[48:63], a[36:39], a[208:211], v[48:63]\n"
      "v_mfma_f32_32x32x16_bf16 v[64:79], a[32:35], a[212:215], v[64:79]\n"
      "s_add_u32 s20, s20, 4096\n"
      "buffer_load_dwordx4 v[168:171], v2, s[4:7], s20 offen offset:0 sc1\n"
      "s_waitcnt vmcnt(10)\n"
      "v_max_u32 v15, v128, v129\n"
      "v_max3_u32 v15, v15, v130, v131\n"
      "v_mfma_f32_32x32x16_bf16 v[80:95], a[36:39], a[212:215], v[80:95]\n"
      "v_mfma_f32_32x32x16_bf16 v[32:47], a[40:43], a[216:219], v[32:47]\n"
      "buffer_load_dwordx4 v[172:175], v2, s[4:7], s20 offen offset:2048 sc1\n"
      "s_waitcnt vmcnt(10)\n"
      "v_max3_u32 v15, v15, v132, v133\n"
      "v_max3_u32 v15, v15, v134, v135\n"
      "v_mfma_f32_32x32x16_bf16 v[48:63], a[44:47], a[216:219], v[48:63]\n"
      "v_mfma_f32_32x32x16_bf16 v[64:79], a[40:43], a[220:223], v[64:79]\n"
      "s_add_u32 s20, s20, 4096\n"
      "buffer_load_dwordx4 v[176:179], v2, s[4:7], s20 offen offset:0 sc1\n"
      "s_waitcnt vmcnt(10)\n"
      "v_max3_u32 v15, v15, v136, v137\n"
      "v_max3_u32 v15, v15, v138, v139\n"
      "v_mfma_f32_32x32x16_bf16 v[80:95], a[44:47], a[220:223], v[80:95]\n"
      "buffer_load_dwordx4 v[180:183], v2, s[4:7], s20 offen offset:2048 sc1\n"
      "s_waitcnt vmcnt(10)\n"
      "v_max3_u32 v15, v15, v140, v141\n"
      "v_max3_u32 v15, v15, v142, v143\n"
      "v_cmp_eq_u32 vcc, -1, v15\n"
      "s_cbranch_vccnz .Lretry_53\n"
      ".Lback_54:\n"
      "v_mfma_f32_32x32x16_bf16 v[32:47], a[48:51], v[128:131], v[32:47]\n"
      "v_xor_b32 v96, 0, v7\n"
      "v_xor_b32 v97, 32, v7\n"
      "v_xor_b32 v98, 64, v7\n"
      "v_xor_b32 v99, 96, v7\n"
      "v_xor_b32 v100, 128, v7\n"
      "v_mfma_f32_32x32x16_bf16 v[48:63], a[52:55], v[128:131], v[48:63]\n"
      "s_add_u32 s20, s20, 4096\n"
      "buffer_load_dwordx4 v[184:187], v2, s[4:7], s20 offen offset:0 sc1\n"
      "s_waitcnt vmcnt(10)\n"
      "v_max_u32 v15, v144, v145\n"
      "v_max3_u32 v15, v15, v146, v147\n"
      "v_mfma_f32_32x32x16_bf16 v[64:79], a[48:51], v[132:135], v[64:79]\n"
      "v_xor_b32 v101, 160, v7\n"
      "v_xor_b32 v102, 192, v7\n"
      "v_xor_b32 v103, 224, v7\n"
      "v_xor_b32 v104, 16, v8\n"
      "v_xor_b32 v105, 2176, v8\n"
      "v_mfma_f32_32x32x16_bf16 v[80:95], a[52:55], v[132:135], v[80:95]\n"
      "s_add_u32 s90, s66, 8192\n"
      "buffer_load_dwordx4 v[188:191], v2, s[4:7], s20 offen offset:2048 sc1\n"
      "s_waitcnt vmcnt(10)\n"
      "v_max3_u32 v15, v15, v148, v149\n"
      "v_max3_u32 v15, v15, v150, v151\n"
      "v_mfma_f32_32x32x16_bf16 v[32:47], a[56:59], v[136:139], v[32:47]\n"
      "v_xor_b32 v106, 2192, v8\n"
      "ds_read_b128 v[108:111], v11 offset:384\n"
      "ds_read_b128 v[112:115], v11 offset:1408\n"
      "v_mfma_f32_32x32x16_bf16 v[48:63], a[60:63], v[136:139], v[48:63]\n"
      "s_add_u32 s20, s61, 57344\n"
      "buffer_load_dwordx4 v[192:195], v2, s[4:7], s20 offen offset:0 sc1\n"
      "s_waitcnt vmcnt(10)\n"
      "v_max3_u32 v15, v15, v152, v153\n"
      "v_max3_u32 v15, v15, v154, v155\n"
      "v_mfma_f32_32x32x16_bf16 v[64:79], a[56:59], v[140:143], v[64:79]\n"
      "v_mfma_f32_32x32x16_bf16 v[80:95], a[60:63], v[140:143], v[80:95]\n"
      "buffer_load_dwordx4 v[196:199], v2, s[4:7], s20 offen offset:2048 sc1\n"
      "s_waitcnt vmcnt(10)\n"
      "v_max3_u32 v15, v15, v156, v157\n"
      "v_max3_u32 v15, v15, v158, v159\n"
      "v_cmp_eq_u32 vcc, -1, v15\n"
      "s_cbranch_vccnz .Lretry_55\n"
      ".Lback_56:\n"
      "v_mfma_f32_32x32x16_bf16 v[32:47], a[64:67], v[144:147], v[32:47]\n"
      "v_mfma_f32_32x32x16_bf16 v[48:63], a[68:71], v[144:147], v[48:63]\n"
      "s_add_u32 s20, s20, 4096\n"
      "buffer_load_dwordx4 v[200:203], v2, s[4:7], s20 offen offset:0 sc1\n"
      "s_waitcnt vmcnt(10)\n"
      "v_max_u32 v15, v160, v161\n"
      "v_max3_u32 v15, v15, v162, v163\n"
      "v_mfma_f32_32x32x16_bf16 v[64:79], a[64:67], v[148:151], v[64:79]\n"
      "v_mfma_f32_32x32x16_bf16 v[80:95], a[68:71], v[148:151], v[80:95]\n"
      "buffer_load_dwordx4 v[204:207], v2, s[4:7], s20 offen offset:2048 sc1\n"
      "s_waitcnt vmcnt(10)\n"
      "v_max3_u32 v15, v15, v164, v165\n"
      "v_max3_u32 v15, v15, v166, v167\n"
      "v_mfma_f32_32x32x16_bf16 v[32:47], a[72:75], v[152:155], v[32:47]\n"
      "v_mfma_f32_32x32x16_bf16 v[48:63], a[76:79], v[152:155], v[48:63]\n"
      "s_add_u32 s20, s20, 4096\n"
      "buffer_load_dwordx4 v[208:211], v2, s[4:7], s20 offen offset:0 sc1\n"
      "s_waitcnt vmcnt(10)\n"
      "v_max3_u32 v15, v15, v168, v169\n"
      "v_max3_u32 v15, v15, v170, v171\n"
      "v_mfma_f32_32x32x16_bf16 v[64:79], a[72:75], v[156:159], v[64:79]\n"
      "v_mfma_f32_32x32x16_bf16 v[80:95], a[76:79], v[156:159], v[80:95]\n"
      "buffer_load_dwordx4 v[212:215], v2, s[4:7], s20 offen offset:2048 sc1\n"
      "s_waitcnt vmcnt(10)\n"
      "v_max3_u32 v15, v15, v172, v173\n"
      "v_max3_u32 v15, v15, v174, v175\n"
      "v_cmp_eq_u32 vcc, -1, v15\n"
      "s_cbranch_vccnz .Lretry_57\n"
      ".Lback_58:\n"
      "v_mfma_f32_32x32x16_bf16 v[32:47], a[80:83], v[160:163], v[32:47]\n"
      "v_lshlrev_b32 v128, 16, v24\n"
      "v_and_b32 v129, s83, v24\n"
      "v_lshlrev_b32 v130, 16, v25\n"
      "v_and_b32 v131, s83, v25\n"
      "v_lshlrev_b32 v132, 16, v26\n"
      "v_mfma_f32_32x32x16_bf16 v[48:63], a[84:87], v[160:163], v[48:63]\n"
      "s_add_u32 s20, s20, 4096\n"
      "buffer_load_dwordx4 v[216:219], v2, s[4:7], s20 offen offset:0 sc1\n"
      "s_waitcnt vmcnt(10)\n"
      "v_max_u32 v15, v176, v177\n"
      "v_max3_u32 v15, v15, v178, v179\n"
      "v_mfma_f32_32x32x16_bf16 v[64:79], a[80:83], v[164:167], v[64:79]\n"
      "v_and_b32 v133, s83, v26\n"
      "v_lshlrev_b32 v134, 16, v27\n"
      "v_and_b32 v135, s83, v27\n"
      "v_lshlrev_b32 v136, 16, v28\n"
      "v_and_b32 v137, s83, v28\n"
      "v_mfma_f32_32x32x16_bf16 v[80:95], a[84:87], v[164:167], v[80:95]\n"
      "buffer_load_dwordx4 v[220:223], v2, s[4:7], s20 offen offset:2048 sc1\n"
      "s_waitcnt vmcnt(10)\n"
      "v_max3_u32 v15, v15, v180, v181\n"
      "v_max3_u32 v15, v15, v182, v183\n"
      "v_mfma_f32_32x32x16_bf16 v[32:47], a[88:91], v[168:171], v[32:47]\n"
      "v_lshlrev_b32 v138, 16, v29\n"
      "v_and_b32 v139, s83, v29\n"
      "v_lshlrev_b32 v140, 16, v30\n"
      "v_and_b32 v141, s83, v30\n"
      "v_lshlrev_b32 v142, 16, v31\n"
      "v_mfma_f32_32x32x16_bf16 v[48:63], a[92:95], v[168:171], v[48:63]\n"
      "s_add_u32 s20, s20, 4096\n"
      "buffer_load_dwordx4 v[224:227], v2, s[4:7], s20 offen offset:0 sc1\n"
      "s_waitcnt vmcnt(10)\n"
      "v_max3_u32 v15, v15, v184, v185\n"
      "v_max3_u32 v15, v15, v186, v187\n"
      "v_mfma_f32_32x32x16_bf16 v[64:79], a[88:91], v[172:175], v[64:79]\n"
      "v_and_b32 v143, s83, v31\n"
      "v_mfma_f32_32x32x16_bf16 v[80:95], a[92:95], v[172:175], v[80:95]\n"
      "buffer_load_dwordx4 v[228:231], v2, s[4:7], s20 offen offset:2048 sc1\n"
      "s_waitcnt vmcnt(10)\n"
      "v_max3_u32 v15, v15, v188, v189\n"
      "v_max3_u32 v15, v15, v190, v191\n"
      "v_cmp_eq_u32 vcc, -1, v15\n"
      "s_cbranch_vccnz .Lretry_59\n"
      ".Lback_60:\n"
      "v_mfma_f32_32x32x16_bf16 v[32:47], a[96:99], v[176:179], v[32:47]\n"
      "v_mfma_f32_32x32x16_bf16 v[48:63], a[100:103], v[176:179], v[48:63]\n"
      "s_add_u32 s20, s20, 4096\n"
      "buffer_load_dwordx4 v[232:235], v2, s[4:7], s20 offen offset:0 sc1\n"
      "s_waitcnt vmcnt(10)\n"
      "v_max_u32 v15, v192, v193\n"
      "v_max3_u32 v15, v15, v194, v195\n"
      "v_mfma_f32_32x32x16_bf16 v[64:79], a[96:99], v[180:183], v[64:79]\n"
      "v_mfma_f32_32x32x16_bf16 v[80:95], a[100:103], v[180:183], v[80:95]\n"
      "buffer_load_dwordx4 v[236:239], v2, s[4:7], s20 offen offset:2048 sc1\n"
      "s_waitcnt vmcnt(10)\n"
      "v_max3_u32 v15, v15, v196, v197\n"
      "v_max3_u32 v15, v15, v198, v199\n"
      "v_mfma_f32_32x32x16_bf16 v[32:47], a[104:107], v[184:187], v[32:47]\n"
      "v_mfma_f32_32x32x16_bf16 v[48:63], a[108:111], v[184:187], v[48:63]\n"
      "s_add_u32 s20, s20, 4096\n"
      "buffer_load_dwordx4 v[240:243], v2, s[4:7], s20 offen offset:0 sc1\n"
      "s_waitcnt vmcnt(10)\n"
      "v_max3_u32 v15, v15, v200, v201\n"
      "v_max3_u32 v15, v15, v202, v203\n"
      "v_mfma_f32_32x32x16_bf16 v[64:79], a[104:107], v[188:191], v[64:79]\n"
      "v_mfma_f32_32x32x16_bf16 v[80:95], a[108:111], v[188:191], v[80:95]\n"
      "buffer_load_dwordx4 v[244:247], v2, s[4:7], s20 offen offset:2048 sc1\n"
      "s_waitcnt vmcnt(10)\n"
      "v_max3_u32 v15, v15, v204, v205\n"
      "v_max3_u32 v15, v15, v206, v207\n"
      "v_cmp_eq_u32 vcc, -1, v15\n"
      "s_cbranch_vccnz .Lretry_61\n"
      ".Lback_62:\n"
      "v_mfma_f32_32x32x16_bf16 v[32:47], a[112:115], v[192:195], v[32:47]\n"
      "v_mfma_f32_32x32x16_bf16 v[48:63], a[116:119], v[192:195], v[48:63]\n"
      "s_add_u32 s20, s20, 4096\n"
      "buffer_load_dwordx4 v[248:251], v2, s[4:7], s20 offen offset:0 sc1\n"
      "s_waitcnt vmcnt(10)\n"
      "v_max_u32 v15, v208, v209\n"
      "v_max3_u32 v15, v15, v210, v211\n"
      "v_mfma_f32_32x32x16_bf16 v[64:79], a[112:115], v[196:199], v[64:79]\n"
      "v_mfma_f32_32x32x16_bf16 v[80:95], a[116:119], v[196:199], v[80:95]\n"
      "buffer_load_dwordx4 v[252:255], v2, s[4:7], s20 offen offset:2048 sc1\n"
      "s_waitcnt vmcnt(10)\n"
      "v_max3_u32 v15, v15, v212, v213\n"
      "v_max3_u32 v15, v15, v214, v215\n"
      "v_mfma_f32_32x32x16_bf16 v[32:47], a[120:123], v[200:203], v[32:47]\n"
      "v_mfma_f32_32x32x16_bf16 v[48:63], a[124:127], v[200:203], v[48:63]\n"
      "s_waitcnt vmcnt(9)\n"
      "v_max3_u32 v15, v15, v216, v217\n"
      "v_max3_u32 v15, v15, v218, v219\n"
      "v_mfma_f32_32x32x16_bf16 v[64:79], a[120:123], v[204:207], v[64:79]\n"
      "v_mfma_f32_32x32x16_bf16 v[80:95], a[124:127], v[204:207], v[80:95]\n"
      "s_waitcnt vmcnt(8)\n"
      "v_max3_u32 v15, v15, v220, v221\n"
      "v_max3_u32 v15, v15, v222, v223\n"
      "v_cmp_eq_u32 vcc, -1, v15\n"
      "s_cbranch_vccnz .Lretry_63\n"
      ".Lback_64:\n"
      "v_mfma_f32_32x32x16_bf16 v[32:47], a[128:131], v[208:211], v[32:47]\n"
      "v_mfma_f32_32x32x16_bf16 v[48:63], a[132:135], v[208:211], v[48:63]\n"
      "s_waitcnt vmcnt(7)\n"
      "v_max_u32 v15, v224, v225\n"
      "v_max3_u32 v15, v15, v226, v227\n"
      "v_mfma_f32_32x32x16_bf16 v[64:79], a[128:131], v[212:215], v[64:79]\n"
      "v_mfma_f32_32x32x16_bf16 v[80:95], a[132:135], v[212:215], v[80:95]\n"
      "s_waitcnt vmcnt(6)\n"
      "v_max3_u32 v15, v15, v228, v229\n"
      "v_max3_u32 v15, v15, v230, v231\n"
      "v_mfma_f32_32x32x16_bf16 v[32:47], a[136:139], v[216:219], v[32:47]\n"
      "v_mfma_f32_32x32x16_bf16 v[48:63], a[140:143], v[216:219], v[48:63]\n"
      "s_waitcnt vmcnt(5)\n"
      "v_max3_u32 v15, v15, v232, v233\n"
      "v_max3_u32 v15, v15, v234, v235\n"
      "v_mfma_f32_32x32x16_bf16 v[64:79], a[136:139], v[220:223], v[64:79]\n"
      "v_mfma_f32_32x32x16_bf16 v[80:95], a[140:143], v[220:223], v[80:95]\n"
      "s_waitcnt vmcnt(4)\n"
      "v_max3_u32 v15, v15, v236, v237\n"
      "v_max3_u32 v15, v15, v238, v239\n"
      "s_waitcnt vmcnt(3)\n"
      "v_max3_u32 v15, v15, v240, v241\n"
      "v_max3_u32 v15, v15, v242, v243\n"
      "s_waitcnt vmcnt(2)\n"
      "v_max3_u32 v15, v15, v244, v245\n"
      "v_max3_u32 v15, v15, v246, v247\n"
      "s_waitcnt vmcnt(1)\n"
      "v_max3_u32 v15, v15, v248, v249\n"
      "v_max3_u32 v15, v15, v250, v251\n"
      "s_waitcnt vmcnt(0)\n"
      "v_max3_u32 v15, v15, v252, v253\n"
      "v_max3_u32 v15, v15, v254, v255\n"
      "v_cmp_eq_u32 vcc, -1, v15\n"
      "s_cbranch_vccnz .Lretry_65\n"
      ".Lback_66:\n"
      "v_mfma_f32_32x32x16_bf16 v[32:47], a[144:147], v[224:227], v[32:47]\n"
      "v_mfma_f32_32x32x16_bf16 v[32:47], a[152:155], v[232:235], v[32:47]\n"
      "v_mfma_f32_32x32x16_bf16 v[32:47], a[160:163], v[240:243], v[32:47]\n"
      "v_mfma_f32_32x32x16_bf16 v[32:47], a[168:171], v[248:251], v[32:47]\n"
      "v_mfma_f32_32x32x16_bf16 v[48:63], a[148:151], v[224:227], v[48:63]\n"
      "v_mfma_f32_32x32x16_bf16 v[48:63], a[156:159], v[232:235], v[48:63]\n"
      "v_mfma_f32_32x32x16_bf16 v[48:63], a[164:167], v[240:243], v[48:63]\n"
      "ds_write_b128 v96, v[32:35] offset:0\n"
      "v_mfma_f32_32x32x16_bf16 v[48:63], a[172:175], v[248:251], v[48:63]\n"
      "ds_write_b128 v97, v[36:39] offset:0\n"
      "ds_write_b128 v98, v[40:43] offset:0\n"
      "ds_write_b128 v99, v[44:47] offset:0\n"
      "v_mfma_f32_32x32x16_bf16 v[64:79], a[144:147], v[228:231], v[64:79]\n"
      "v_mfma_f32_32x32x16_bf16 v[64:79], a[152:155], v[236:239], v[64:79]\n"
      "v_mfma_f32_32x32x16_bf16 v[64:79], a[160:163], v[244:247], v[64:79]\n"
      "ds_write_b128 v100, v[48:51] offset:0\n"
      "v_mfma_f32_32x32x16_bf16 v[64:79], a[168:171], v[252:255], v[64:79]\n"
      "ds_write_b128 v101, v[52:55] offset:0\n"
      "ds_write_b128 v102, v[56:59] offset:0\n"
      "ds_write_b128 v103, v[60:63] offset:0\n"
      "v_mfma_f32_32x32x16_bf16 v[80:95], a[148:151], v[228:231], v[80:95]\n"
      "v_mfma_f32_32x32x16_bf16 v[80:95], a[156:159], v[236:239], v[80:95]\n"
      "v_mfma_f32_32x32x16_bf16 v[80:95], a[164:167], v[244:247], v[80:95]\n"
      "ds_write_b128 v96, v[64:67] offset:8192\n"
      "v_mfma_f32_32x32x16_bf16 v[80:95], a[172:175], v[252:255], v[80:95]\n"
      "ds_write_b128 v97, v[68:71] offset:8192\n"
      "ds_write_b128 v98, v[72:75] offset:8192\n"
      "ds_write_b128 v99, v[76:79] offset:8192\n"
      "s_nop 8\n"
      "ds_write_b128 v100, v[80:83] offset:8192\n"
      "ds_write_b128 v101, v[84:87] offset:8192\n"
      "ds_write_b128 v102, v[88:91] offset:8192\n"
      "ds_write_b128 v103, v[92:95] offset:8192\n"
      "s_waitcnt lgkmcnt(0)\n"
      "s_barrier\n"
      "ds_read_b128 v[32:35], v8 offset:0\n"
      "ds_read_b128 v[36:39], v104 offset:0\n"
      "ds_read_b128 v[40:43], v8 offset:16384\n"
      "ds_read_b128 v[44:47], v104 offset:16384\n"
      "ds_read_b128 v[48:51], v8 offset:32768\n"
      "ds_read_b128 v[52:55], v104 offset:32768\n"
      "ds_read_b128 v[56:59], v8 offset:49152\n"
      "ds_read_b128 v[60:63], v104 offset:49152\n"
      "ds_read_b128 v[64:67], v105 offset:0\n"
      "ds_read_b128 v[68:71], v106 offset:0\n"
      "ds_read_b128 v[72:75], v105 offset:16384\n"
      "ds_read_b128 v[76:79], v106 offset:16384\n"
      "ds_read_b128 v[80:83], v105 offset:32768\n"
      "ds_read_b128 v[84:87], v106 offset:32768\n"
      "ds_read_b128 v[88:91], v105 offset:49152\n"
      "ds_read_b128 v[92:95], v106 offset:49152\n"
      "s_waitcnt lgkmcnt(13)\n"
      "v_pk_add_f32 v[32:33], v[32:33], v[40:41]\n"
      "v_pk_add_f32 v[34:35], v[34:35], v[42:43]\n"
      "s_waitcnt lgkmcnt(12)\n"
      "v_pk_add_f32 v[36:37], v[36:37], v[44:45]\n"
      "v_pk_add_f32 v[38:39], v[38:39], v[46:47]\n"
      "s_waitcnt lgkmcnt(9)\n"
      "v_pk_add_f32 v[48:49], v[48:49], v[56:57]\n"
      "v_pk_add_f32 v[50:51], v[50:51], v[58:59]\n"
      "s_waitcnt lgkmcnt(8)\n"
      "v_pk_add_f32 v[52:53], v[52:53], v[60:61]\n"
      "v_pk_add_f32 v[54:55], v[54:55], v[62:63]\n"
      "v_pk_add_f32 v[32:33], v[32:33], v[48:49]\n"
      "v_pk_add_f32 v[34:35], v[34:35], v[50:51]\n"
      "v_pk_add_f32 v[36:37], v[36:37], v[52:53]\n"
      "v_pk_add_f32 v[38:39], v[38:39], v[54:55]\n"
      "v_pk_fma_f32 v[32:33], v[32:33], v[108:109], v[128:129]\n"
      "v_pk_fma_f32 v[34:35], v[34:35], v[110:111], v[130:131]\n"
      "v_pk_fma_f32 v[36:37], v[36:37], v[112:113], v[132:133]\n"
      "v_pk_fma_f32 v[38:39], v[38:39], v[114:115], v[134:135]\n"
      "v_cvt_pk_bf16_f32 v144, v32, v33\n"
      "v_cvt_pk_bf16_f32 v145, v34, v35\n"
      "v_cvt_pk_bf16_f32 v146, v36, v37\n"
      "v_cvt_pk_bf16_f32 v147, v38, v39\n"
      "buffer_store_dwordx4 v[144:147], v5, s[40:43], s66 offen\n"
      "s_waitcnt lgkmcnt(5)\n"
      "v_pk_add_f32 v[64:65], v[64:65], v[72:73]\n"
      "v_pk_add_f32 v[66:67], v[66:67], v[74:75]\n"
      "s_waitcnt lgkmcnt(4)\n"
      "v_pk_add_f32 v[68:69], v[68:69], v[76:77]\n"
      "v_pk_add_f32 v[70:71], v[70:71], v[78:79]\n"
      "s_waitcnt lgkmcnt(1)\n"
      "v_pk_add_f32 v[80:81], v[80:81], v[88:89]\n"
      "v_pk_add_f32 v[82:83], v[82:83], v[90:91]\n"
      "s_waitcnt lgkmcnt(0)\n"
      "v_pk_add_f32 v[84:85], v[84:85], v[92:93]\n"
      "v_pk_add_f32 v[86:87], v[86:87], v[94:95]\n"
      "v_pk_add_f32 v[64:65], v[64:65], v[80:81]\n"
      "v_pk_add_f32 v[66:67], v[66:67], v[82:83]\n"
      "v_pk_add_f32 v[68:69], v[68:69], v[84:85]\n"
      "v_pk_add_f32 v[70:71], v[70:71], v[86:87]\n"
      "v_pk_fma_f32 v[64:65], v[64:65], v[108:109], v[136:137]\n"
      "v_pk_fma_f32 v[66:67], v[66:67], v[110:111], v[138:139]\n"
      "v_pk_fma_f32 v[68:69], v[68:69], v[112:113], v[140:141]\n"
      "v_pk_fma_f32 v[70:71], v[70:71], v[114:115], v[142:143]\n"
      "v_cvt_pk_bf16_f32 v148, v64, v65\n"
      "v_cvt_pk_bf16_f32 v149, v66, v67\n"
      "v_cvt_pk_bf16_f32 v150, v68, v69\n"
      "v_cvt_pk_bf16_f32 v151, v70, v71\n"
      "buffer_store_dwordx4 v[148:151], v5, s[40:43], s90 offen\n"
      "s_endpgm\n"
      ".Lretry_9:\n"
      "s_mov_b32 s91, 0\n"
      ".Lretry_9_l:\n"
      "s_nop 7\n"
      "s_nop 7\n"
      "s_add_u32 s91, s91, 1\n"
      "s_cmp_gt_u32 s91, 65536\n"
      "s_cbranch_scc1 .Lback_10\n"
      "s_mov_b32 s92, 65536\n"
      "buffer_load_dwordx4 v[48:51], v13, s[4:7], s92 offen sc1\n"
      "s_mov_b32 s92, 77824\n"
      "buffer_load_dwordx4 v[52:55], v13, s[4:7], s92 offen sc1\n"
      "s_mov_b32 s92, 90112\n"
      "buffer_load_dwordx4 v[56:59], v13, s[4:7], s92 offen sc1\n"
      "s_mov_b32 s92, 102400\n"
      "buffer_load_dwordx4 v[60:63], v13, s[4:7], s92 offen sc1\n"
      "s_waitcnt vmcnt(0)\n"
      "v_xor_b32 v64, s86, v49\n"
      "v_bitop3_b32 v64, v64, v51, s86 bitop3:0xf6\n"
      "v_bitop3_b32 v64, v64, v53, s86 bitop3:0xf6\n"
      "v_bitop3_b32 v64, v64, v55, s86 bitop3:0xf6\n"
      "v_bitop3_b32 v64, v64, v57, s86 bitop3:0xf6\n"
      "v_bitop3_b32 v64, v64, v59, s86 bitop3:0xf6\n"
      "v_bitop3_b32 v64, v64, v61, s86 bitop3:0xf6\n"
      "v_bitop3_b32 v64, v64, v63, s86 bitop3:0xf6\n"
      "v_cmp_ne_u32 vcc, 0, v64\n"
      "s_cbranch_vccnz .Lretry_9_l\n"
      "s_nop 4\n"
      "s_branch .Lback_10\n"
      ".Lretry_14:\n"
      "s_mov_b32 s91, 0\n"
      ".Lretry_14_l:\n"
      "s_nop 7\n"
      "s_nop 7\n"
      "s_add_u32 s91, s91, 1\n"
      "s_cmp_gt_u32 s91, 65536\n"
      "s_cbranch_scc1 .Lback_15\n"
      "buffer_load_dword v97, v20, s[4:7], s73 offen sc1\n"
      "buffer_load_dword v96, v11, s[4:7], s71 offen sc1\n"
      "s_waitcnt vmcnt(0)\n"
      "v_xor_b32 v98, s86, v96\n"
      "v_bitop3_b32 v98, v98, v97, s86 bitop3:0xf6\n"
      "v_cmp_ne_u32 vcc, 0, v98\n"
      "s_cbranch_vccnz .Lretry_14_l\n"
      "s_nop 4\n"
      "s_branch .Lback_15\n"
      ".Lretry_16:\n"
      "s_mov_b32 s91, 0\n"
      ".Lretry_16_l:\n"
      "s_nop 7\n"
      "s_nop 7\n"
      "s_add_u32 s91, s91, 1\n"
      "s_cmp_gt_u32 s91, 65536\n"
      "s_cbranch_scc1 .Lback_17\n"
      "buffer_load_dwordx4 v[100:103], v127, s[4:7], s67 offen sc1\n"
      "s_waitcnt vmcnt(0)\n"
      "v_xor_b32 v105, s86, v101\n"
      "v_bitop3_b32 v105, v105, v103, s86 bitop3:0xf6\n"
      "v_cmp_ne_u32 vcc, 0, v105\n"
      "s_cbranch_vccnz .Lretry_16_l\n"
      "s_nop 4\n"
      "s_branch .Lback_17\n"
      ".Lretry_18:\n"
      "s_mov_b32 s91, 0\n"
      ".Lretry_18_l:\n"
      "s_waitcnt vmcnt(0)\n"
      "s_mov_b32 s92, 0\n"
      "v_max_u32 v107, v128, v129\n"
      "v_max3_u32 v107, v107, v130, v131\n"
      "v_max3_u32 v107, v107, v132, v133\n"
      "v_max3_u32 v107, v107, v134, v135\n"
      "v_max3_u32 v107, v107, v136, v137\n"
      "v_max3_u32 v107, v107, v138, v139\n"
      "v_max3_u32 v107, v107, v140, v141\n"
      "v_max3_u32 v107, v107, v142, v143\n"
      "v_cmp_eq_u32 vcc, -1, v107\n"
      "s_cbranch_vccz .Lretry_18_0\n"
      "s_add_u32 s88, s56, 0\n"
      "s_and_b32 s88, s88, 15\n"
      "s_lshl_b32 s88, s88, 11\n"
      "s_add_u32 s88, s88, s55\n"
      "buffer_load_dwordx4 v[128:131], v2, s[4:7], s88 offen offset:0 sc1\n"
      "buffer_load_dwordx4 v[132:135], v2, s[4:7], s88 offen offset:2048 sc1\n"
      "s_add_u32 s88, s88, 4096\n"
      "buffer_load_dwordx4 v[136:139], v2, s[4:7], s88 offen offset:0 sc1\n"
      "buffer_load_dwordx4 v[140:143], v2, s[4:7], s88 offen offset:2048 sc1\n"
      "s_mov_b32 s92, 1\n"
      ".Lretry_18_0:\n"
      "v_max_u32 v107, v144, v145\n"
      "v_max3_u32 v107, v107, v146, v147\n"
      "v_max3_u32 v107, v107, v148, v149\n"
      "v_max3_u32 v107, v107, v150, v151\n"
      "v_max3_u32 v107, v107, v152, v153\n"
      "v_max3_u32 v107, v107, v154, v155\n"
      "v_max3_u32 v107, v107, v156, v157\n"
      "v_max3_u32 v107, v107, v158, v159\n"
      "v_cmp_eq_u32 vcc, -1, v107\n"
      "s_cbranch_vccz .Lretry_18_1\n"
      "s_add_u32 s88, s56, 4\n"
      "s_and_b32 s88, s88, 15\n"
      "s_lshl_b32 s88, s88, 11\n"
      "s_add_u32 s88, s88, s55\n"
      "buffer_load_dwordx4 v[144:147], v2, s[4:7], s88 offen offset:0 sc1\n"
      "buffer_load_dwordx4 v[148:151], v2, s[4:7], s88 offen offset:2048 sc1\n"
      "s_add_u32 s88, s88, 4096\n"
      "buffer_load_dwordx4 v[152:155], v2, s[4:7], s88 offen offset:0 sc1\n"
      "buffer_load_dwordx4 v[156:159], v2, s[4:7], s88 offen offset:2048 sc1\n"
      "s_mov_b32 s92, 1\n"
      ".Lretry_18_1:\n"
      "s_add_u32 s91, s91, 1\n"
      "s_cmp_gt_u32 s91, 65536\n"
      "s_cselect_b32 s92, 0, s92\n"
      "s_cmp_lg_u32 s92, 0\n"
      "s_cbranch_scc1 .Lretry_18_l\n"
      "s_waitcnt vmcnt(0)\n"
      "s_nop 7\n"
      "s_nop 7\n"
      "s_branch .Lback_19\n"
      ".Lretry_20:\n"
      "s_mov_b32 s91, 0\n"
      ".Lretry_20_l:\n"
      "s_waitcnt vmcnt(0)\n"
      "s_mov_b32 s92, 0\n"
      "v_max_u32 v107, v144, v145\n"
      "v_max3_u32 v107, v107, v146, v147\n"
      "v_max3_u32 v107, v107, v148, v149\n"
      "v_max3_u32 v107, v107, v150, v151\n"
      "v_max3_u32 v107, v107, v152, v153\n"
      "v_max3_u32 v107, v107, v154, v155\n"
      "v_max3_u32 v107, v107, v156, v157\n"
      "v_max3_u32 v107, v107, v158, v159\n"
      "v_cmp_eq_u32 vcc, -1, v107\n"
      "s_cbranch_vccz .Lretry_20_0\n"
      "s_add_u32 s88, s56, 4\n"
      "s_and_b32 s88, s88, 15\n"
      "s_lshl_b32 s88, s88, 11\n"
      "s_add_u32 s88, s88, s55\n"
      "buffer_load_dwordx4 v[144:147], v2, s[4:7], s88 offen offset:0 sc1\n"
      "buffer_load_dwordx4 v[148:151], v2, s[4:7], s88 offen offset:2048 sc1\n"
      "s_add_u32 s88, s88, 4096\n"
      "buffer_load_dwordx4 v[152:155], v2, s[4:7], s88 offen offset:0 sc1\n"
      "buffer_load_dwordx4 v[156:159], v2, s[4:7], s88 offen offset:2048 sc1\n"
      "s_mov_b32 s92, 1\n"
      ".Lretry_20_0:\n"
      "v_max_u32 v107, v160, v161\n"
      "v_max3_u32 v107, v107, v162, v163\n"
      "v_max3_u32 v107, v107, v164, v165\n"
      "v_max3_u32 v107, v107, v166, v167\n"
      "v_max3_u32 v107, v107, v168, v169\n"
      "v_max3_u32 v107, v107, v170, v171\n"
      "v_max3_u32 v107, v107, v172, v173\n"
      "v_max3_u32 v107, v107, v174, v175\n"
      "v_cmp_eq_u32 vcc, -1, v107\n"
      "s_cbranch_vccz .Lretry_20_1\n"
      "s_add_u32 s88, s56, 8\n"
      "s_and_b32 s88, s88, 15\n"
      "s_lshl_b32 s88, s88, 11\n"
      "s_add_u32 s88, s88, s55\n"
      "buffer_load_dwordx4 v[160:163], v2, s[4:7], s88 offen offset:0 sc1\n"
      "buffer_load_dwordx4 v[164:167], v2, s[4:7], s88 offen offset:2048 sc1\n"
      "s_add_u32 s88, s88, 4096\n"
      "buffer_load_dwordx4 v[168:171], v2, s[4:7], s88 offen offset:0 sc1\n"
      "buffer_load_dwordx4 v[172:175], v2, s[4:7], s88 offen offset:2048 sc1\n"
      "s_mov_b32 s92, 1\n"
      ".Lretry_20_1:\n"
      "v_max_u32 v107, v176, v177\n"
      "v_max3_u32 v107, v107, v178, v179\n"
      "v_max3_u32 v107, v107, v180, v181\n"
      "v_max3_u32 v107, v107, v182, v183\n"
      "v_max3_u32 v107, v107, v184, v185\n"
      "v_max3_u32 v107, v107, v186, v187\n"
      "v_max3_u32 v107, v107, v188, v189\n"
      "v_max3_u32 v107, v107, v190, v191\n"
      "v_cmp_eq_u32 vcc, -1, v107\n"
      "s_cbranch_vccz .Lretry_20_2\n"
      "s_add_u32 s88, s56, 12\n"
      "s_and_b32 s88, s88, 15\n"
      "s_lshl_b32 s88, s88, 11\n"
      "s_add_u32 s88, s88, s55\n"
      "buffer_load_dwordx4 v[176:179], v2, s[4:7], s88 offen offset:0 sc1\n"
      "buffer_load_dwordx4 v[180:183], v2, s[4:7], s88 offen offset:2048 sc1\n"
      "s_add_u32 s88, s88, 4096\n"
      "buffer_load_dwordx4 v[184:187], v2, s[4:7], s88 offen offset:0 sc1\n"
      "buffer_load_dwordx4 v[188:191], v2, s[4:7], s88 offen offset:2048 sc1\n"
      "s_mov_b32 s92, 1\n"
      ".Lretry_20_2:\n"
      "s_add_u32 s91, s91, 1\n"
      "s_cmp_gt_u32 s91, 65536\n"
      "s_cselect_b32 s92, 0, s92\n"
      "s_cmp_lg_u32 s92, 0\n"
      "s_cbranch_scc1 .Lretry_20_l\n"
      "s_waitcnt vmcnt(0)\n"
      "s_nop 7\n"
      "s_nop 7\n"
      "s_branch .Lback_21\n"
      ".Lretry_22:\n"
      "s_mov_b32 s91, 0\n"
      ".Lretry_22_l:\n"
      "s_waitcnt vmcnt(0)\n"
      "s_mov_b32 s92, 0\n"
      "v_max_u32 v107, v160, v161\n"
      "v_max3_u32 v107, v107, v162, v163\n"
      "v_max3_u32 v107, v107, v164, v165\n"
      "v_max3_u32 v107, v107, v166, v167\n"
      "v_max3_u32 v107, v107, v168, v169\n"
      "v_max3_u32 v107, v107, v170, v171\n"
      "v_max3_u32 v107, v107, v172, v173\n"
      "v_max3_u32 v107, v107, v174, v175\n"
      "v_cmp_eq_u32 vcc, -1, v107\n"
      "s_cbranch_vccz .Lretry_22_0\n"
      "s_add_u32 s88, s56, 8\n"
      "s_and_b32 s88, s88, 15\n"
      "s_lshl_b32 s88, s88, 11\n"
      "s_add_u32 s88, s88, s55\n"
      "buffer_load_dwordx4 v[160:163], v2, s[4:7], s88 offen offset:0 sc1\n"
      "buffer_load_dwordx4 v[164:167], v2, s[4:7], s88 offen offset:2048 sc1\n"
      "s_add_u32 s88, s88, 4096\n"
      "buffer_load_dwordx4 v[168:171], v2, s[4:7], s88 offen offset:0 sc1\n"
      "buffer_load_dwordx4 v[172:175], v2, s[4:7], s88 offen offset:2048 sc1\n"
      "s_mov_b32 s92, 1\n"
      ".Lretry_22_0:\n"
      "v_max_u32 v107, v176, v177\n"
      "v_max3_u32 v107, v107, v178, v179\n"
      "v_max3_u32 v107, v107, v180, v181\n"
      "v_max3_u32 v107, v107, v182, v183\n"
      "v_max3_u32 v107, v107, v184, v185\n"
      "v_max3_u32 v107, v107, v186, v187\n"
      "v_max3_u32 v107, v107, v188, v189\n"
      "v_max3_u32 v107, v107, v190, v191\n"
      "v_cmp_eq_u32 vcc, -1, v107\n"
      "s_cbranch_vccz .Lretry_22_1\n"
      "s_add_u32 s88, s56, 12\n"
      "s_and_b32 s88, s88, 15\n"
      "s_lshl_b32 s88, s88, 11\n"
      "s_add_u32 s88, s88, s55\n"
      "buffer_load_dwordx4 v[176:179], v2, s[4:7], s88 offen offset:0 sc1\n"
      "buffer_load_dwordx4 v[180:183], v2, s[4:7], s88 offen offset:2048 sc1\n"
      "s_add_u32 s88, s88, 4096\n"
      "buffer_load_dwordx4 v[184:187], v2, s[4:7], s88 offen offset:0 sc1\n"
      "buffer_load_dwordx4 v[188:191], v2, s[4:7], s88 offen offset:2048 sc1\n"
      "s_mov_b32 s92, 1\n"
      ".Lretry_22_1:\n"
      "s_add_u32 s91, s91, 1\n"
      "s_cmp_gt_u32 s91, 65536\n"
      "s_cselect_b32 s92, 0, s92\n"
      "s_cmp_lg_u32 s92, 0\n"
      "s_cbranch_scc1 .Lretry_22_l\n"
      "s_waitcnt vmcnt(0)\n"
      "s_nop 7\n"
      "s_nop 7\n"
      "s_branch .Lback_23\n"
      ".Lretry_24:\n"
      "s_mov_b32 s91, 0\n"
      ".Lretry_24_l:\n"
      "s_waitcnt vmcnt(0)\n"
      "s_mov_b32 s92, 0\n"
      "v_max_u32 v107, v176, v177\n"
      "v_max3_u32 v107, v107, v178, v179\n"
      "v_max3_u32 v107, v107, v180, v181\n"
      "v_max3_u32 v107, v107, v182, v183\n"
      "v_max3_u32 v107, v107, v184, v185\n"
      "v_max3_u32 v107, v107, v186, v187\n"
      "v_max3_u32 v107, v107, v188, v189\n"
      "v_max3_u32 v107, v107, v190, v191\n"
      "v_cmp_eq_u32 vcc, -1, v107\n"
      "s_cbranch_vccz .Lretry_24_0\n"
      "s_add_u32 s88, s56, 12\n"
      "s_and_b32 s88, s88, 15\n"
      "s_lshl_b32 s88, s88, 11\n"
      "s_add_u32 s88, s88, s55\n"
      "buffer_load_dwordx4 v[176:179], v2, s[4:7], s88 offen offset:0 sc1\n"
      "buffer_load_dwordx4 v[180:183], v2, s[4:7], s88 offen offset:2048 sc1\n"
      "s_add_u32 s88, s88, 4096\n"
      "buffer_load_dwordx4 v[184:187], v2, s[4:7], s88 offen offset:0 sc1\n"
      "buffer_load_dwordx4 v[188:191], v2, s[4:7], s88 offen offset:2048 sc1\n"
      "s_mov_b32 s92, 1\n"
      ".Lretry_24_0:\n"
      "s_add_u32 s91, s91, 1\n"
      "s_cmp_gt_u32 s91, 65536\n"
      "s_cselect_b32 s92, 0, s92\n"
      "s_cmp_lg_u32 s92, 0\n"
      "s_cbranch_scc1 .Lretry_24_l\n"
      "s_waitcnt vmcnt(0)\n"
      "s_nop 7\n"
      "s_nop 7\n"
      "s_branch .Lback_25\n"
      ".Lretry_26:\n"
      "s_mov_b32 s91, 0\n"
      ".Lretry_26_l:\n"
      "s_nop 7\n"
      "s_nop 7\n"
      "s_add_u32 s91, s91, 1\n"
      "s_cmp_gt_u32 s91, 65536\n"
      "s_cbranch_scc1 .Lback_27\n"
      "buffer_load_dwordx2 v[64:65], v125, s[4:7], s68 offen offset:0 sc1\n"
      "buffer_load_dwordx2 v[66:67], v125, s[4:7], s68 offen offset:256 sc1\n"
      "buffer_load_dwordx2 v[68:69], v125, s[4:7], s68 offen offset:512 sc1\n"
      "buffer_load_dwordx2 v[70:71], v125, s[4:7], s68 offen offset:768 sc1\n"
      "s_waitcnt vmcnt(0)\n"
      "v_xor_b32 v72, s86, v65\n"
      "v_bitop3_b32 v72, v72, v67, s86 bitop3:0xf6\n"
      "v_bitop3_b32 v72, v72, v69, s86 bitop3:0xf6\n"
      "v_bitop3_b32 v72, v72, v71, s86 bitop3:0xf6\n"
      "v_cmp_ne_u32 vcc, 0, v72\n"
      "s_cbranch_vccnz .Lretry_26_l\n"
      "s_nop 4\n"
      "s_branch .Lback_27\n"
      ".Lretry_28:\n"
      "s_mov_b32 s91, 0\n"
      ".Lretry_28_l:\n"
      "s_nop 7\n"
      "s_nop 7\n"
      "s_add_u32 s91, s91, 1\n"
      "s_cmp_gt_u32 s91, 65536\n"
      "s_cbranch_scc1 .Lback_29\n"
      "buffer_load_dword v126, v11, s[4:7], s74 offen sc1\n"
      "s_waitcnt vmcnt(0)\n"
      "v_cmp_ne_u32 vcc, s86, v126\n"
      "s_cbranch_vccnz .Lretry_28_l\n"
      "s_nop 4\n"
      "s_branch .Lback_29\n"
      ".Lretry_30:\n"
      "s_mov_b32 s91, 0\n"
      ".Lretry_30_l:\n"
      "s_waitcnt vmcnt(0)\n"
      "s_mov_b32 s92, 0\n"
      "v_max_u32 v15, v128, v129\n"
      "v_max3_u32 v15, v15, v130, v131\n"
      "v_max3_u32 v15, v15, v132, v133\n"
      "v_max3_u32 v15, v15, v134, v135\n"
      "v_max3_u32 v15, v15, v136, v137\n"
      "v_max3_u32 v15, v15, v138, v139\n"
      "v_max3_u32 v15, v15, v140, v141\n"
      "v_max3_u32 v15, v15, v142, v143\n"
      "v_cmp_eq_u32 vcc, -1, v15\n"
      "s_cbranch_vccz .Lretry_30_0\n"
      "s_add_u32 s88, s65, 0\n"
      "buffer_load_dwordx4 v[128:131], v2, s[4:7], s88 offen offset:0 sc1\n"
      "buffer_load_dwordx4 v[132:135], v2, s[4:7], s88 offen offset:2048 sc1\n"
      "s_add_u32 s88, s88, 4096\n"
      "buffer_load_dwordx4 v[136:139], v2, s[4:7], s88 offen offset:0 sc1\n"
      "buffer_load_dwordx4 v[140:143], v2, s[4:7], s88 offen offset:2048 sc1\n"
      "s_mov_b32 s92, 1\n"
      ".Lretry_30_0:\n"
      "v_max_u32 v15, v144, v145\n"
      "v_max3_u32 v15, v15, v146, v147\n"
      "v_max3_u32 v15, v15, v148, v149\n"
      "v_max3_u32 v15, v15, v150, v151\n"
      "v_max3_u32 v15, v15, v152, v153\n"
      "v_max3_u32 v15, v15, v154, v155\n"
      "v_max3_u32 v15, v15, v156, v157\n"
      "v_max3_u32 v15, v15, v158, v159\n"
      "v_cmp_eq_u32 vcc, -1, v15\n"
      "s_cbranch_vccz .Lretry_30_1\n"
      "s_add_u32 s88, s65, 8192\n"
      "buffer_load_dwordx4 v[144:147], v2, s[4:7], s88 offen offset:0 sc1\n"
      "buffer_load_dwordx4 v[148:151], v2, s[4:7], s88 offen offset:2048 sc1\n"
      "s_add_u32 s88, s88, 4096\n"
      "buffer_load_dwordx4 v[152:155], v2, s[4:7], s88 offen offset:0 sc1\n"
      "buffer_load_dwordx4 v[156:159], v2, s[4:7], s88 offen offset:2048 sc1\n"
      "s_mov_b32 s92, 1\n"
      ".Lretry_30_1:\n"
      "v_max_u32 v15, v160, v161\n"
      "v_max3_u32 v15, v15, v162, v163\n"
      "v_max3_u32 v15, v15, v164, v165\n"
      "v_max3_u32 v15, v15, v166, v167\n"
      "v_max3_u32 v15, v15, v168, v169\n"
      "v_max3_u32 v15, v15, v170, v171\n"
      "v_max3_u32 v15, v15, v172, v173\n"
      "v_max3_u32 v15, v15, v174, v175\n"
      "v_cmp_eq_u32 vcc, -1, v15\n"
      "s_cbranch_vccz .Lretry_30_2\n"
      "s_add_u32 s88, s65, 16384\n"
      "buffer_load_dwordx4 v[160:163], v2, s[4:7], s88 offen offset:0 sc1\n"
      "buffer_load_dwordx4 v[164:167], v2, s[4:7], s88 offen offset:2048 sc1\n"
      "s_add_u32 s88, s88, 4096\n"
      "buffer_load_dwordx4 v[168:171], v2, s[4:7], s88 offen offset:0 sc1\n"
      "buffer_load_dwordx4 v[172:175], v2, s[4:7], s88 offen offset:2048 sc1\n"
      "s_mov_b32 s92, 1\n"
      ".Lretry_30_2:\n"
      "s_add_u32 s91, s91, 1\n"
      "s_cmp_gt_u32 s91, 65536\n"
      "s_cselect_b32 s92, 0, s92\n"
      "s_cmp_lg_u32 s92, 0\n"
      "s_cbranch_scc1 .Lretry_30_l\n"
      "s_waitcnt vmcnt(0)\n"
      "s_nop 7\n"
      "s_nop 7\n"
      "s_branch .Lback_31\n"
      ".Lretry_32:\n"
      "s_mov_b32 s91, 0\n"
      ".Lretry_32_l:\n"
      "s_waitcnt vmcnt(0)\n"
      "s_mov_b32 s92, 0\n"
      "v_max_u32 v15, v144, v145\n"
      "v_max3_u32 v15, v15, v146, v147\n"
      "v_max3_u32 v15, v15, v148, v149\n"
      "v_max3_u32 v15, v15, v150, v151\n"
      "v_max3_u32 v15, v15, v152, v153\n"
      "v_max3_u32 v15, v15, v154, v155\n"
      "v_max3_u32 v15, v15, v156, v157\n"
      "v_max3_u32 v15, v15, v158, v159\n"
      "v_cmp_eq_u32 vcc, -1, v15\n"
      "s_cbranch_vccz .Lretry_32_0\n"
      "s_add_u32 s88, s65, 8192\n"
      "buffer_load_dwordx4 v[144:147], v2, s[4:7], s88 offen offset:0 sc1\n"
      "buffer_load_dwordx4 v[148:151], v2, s[4:7], s88 offen offset:2048 sc1\n"
      "s_add_u32 s88, s88, 4096\n"
      "buffer_load_dwordx4 v[152:155], v2, s[4:7], s88 offen offset:0 sc1\n"
      "buffer_load_dwordx4 v[156:159], v2, s[4:7], s88 offen offset:2048 sc1\n"
      "s_mov_b32 s92, 1\n"
      ".Lretry_32_0:\n"
      "v_max_u32 v15, v160, v161\n"
      "v_max3_u32 v15, v15, v162, v163\n"
      "v_max3_u32 v15, v15, v164, v165\n"
      "v_max3_u32 v15, v15, v166, v167\n"
      "v_max3_u32 v15, v15, v168, v169\n"
      "v_max3_u32 v15, v15, v170, v171\n"
      "v_max3_u32 v15, v15, v172, v173\n"
      "v_max3_u32 v15, v15, v174, v175\n"
      "v_cmp_eq_u32 vcc, -1, v15\n"
      "s_cbranch_vccz .Lretry_32_1\n"
      "s_add_u32 s88, s65, 16384\n"
      "buffer_load_dwordx4 v[160:163], v2, s[4:7], s88 offen offset:0 sc1\n"
      "buffer_load_dwordx4 v[164:167], v2, s[4:7], s88 offen offset:2048 sc1\n"
      "s_add_u32 s88, s88, 4096\n"
      "buffer_load_dwordx4 v[168:171], v2, s[4:7], s88 offen offset:0 sc1\n"
      "buffer_load_dwordx4 v[172:175], v2, s[4:7], s88 offen offset:2048 sc1\n"
      "s_mov_b32 s92, 1\n"
      ".Lretry_32_1:\n"
      "v_max_u32 v15, v176, v177\n"
      "v_max3_u32 v15, v15, v178, v179\n"
      "v_max3_u32 v15, v15, v180, v181\n"
      "v_max3_u32 v15, v15, v182, v183\n"
      "v_max3_u32 v15, v15, v184, v185\n"
      "v_max3_u32 v15, v15, v186, v187\n"
      "v_max3_u32 v15, v15, v188, v189\n"
      "v_max3_u32 v15, v15, v190, v191\n"
      "v_cmp_eq_u32 vcc, -1, v15\n"
      "s_cbranch_vccz .Lretry_32_2\n"
      "s_add_u32 s88, s65, 24576\n"
      "buffer_load_dwordx4 v[176:179], v2, s[4:7], s88 offen offset:0 sc1\n"
      "buffer_load_dwordx4 v[180:183], v2, s[4:7], s88 offen offset:2048 sc1\n"
      "s_add_u32 s88, s88, 4096\n"
      "buffer_load_dwordx4 v[184:187], v2, s[4:7], s88 offen offset:0 sc1\n"
      "buffer_load_dwordx4 v[188:191], v2, s[4:7], s88 offen offset:2048 sc1\n"
      "s_mov_b32 s92, 1\n"
      ".Lretry_32_2:\n"
      "s_add_u32 s91, s91, 1\n"
      "s_cmp_gt_u32 s91, 65536\n"
      "s_cselect_b32 s92, 0, s92\n"
      "s_cmp_lg_u32 s92, 0\n"
      "s_cbranch_scc1 .Lretry_32_l\n"
      "s_waitcnt vmcnt(0)\n"
      "s_nop 7\n"
      "s_nop 7\n"
      "s_branch .Lback_33\n"
      ".Lretry_34:\n"
      "s_mov_b32 s91, 0\n"
      ".Lretry_34_l:\n"
      "s_waitcnt vmcnt(0)\n"
      "s_mov_b32 s92, 0\n"
      "v_max_u32 v15, v160, v161\n"
      "v_max3_u32 v15, v15, v162, v163\n"
      "v_max3_u32 v15, v15, v164, v165\n"
      "v_max3_u32 v15, v15, v166, v167\n"
      "v_max3_u32 v15, v15, v168, v169\n"
      "v_max3_u32 v15, v15, v170, v171\n"
      "v_max3_u32 v15, v15, v172, v173\n"
      "v_max3_u32 v15, v15, v174, v175\n"
      "v_cmp_eq_u32 vcc, -1, v15\n"
      "s_cbranch_vccz .Lretry_34_0\n"
      "s_add_u32 s88, s65, 16384\n"
      "buffer_load_dwordx4 v[160:163], v2, s[4:7], s88 offen offset:0 sc1\n"
      "buffer_load_dwordx4 v[164:167], v2, s[4:7], s88 offen offset:2048 sc1\n"
      "s_add_u32 s88, s88, 4096\n"
      "buffer_load_dwordx4 v[168:171], v2, s[4:7], s88 offen offset:0 sc1\n"
      "buffer_load_dwordx4 v[172:175], v2, s[4:7], s88 offen offset:2048 sc1\n"
      "s_mov_b32 s92, 1\n"
      ".Lretry_34_0:\n"
      "v_max_u32 v15, v176, v177\n"
      "v_max3_u32 v15, v15, v178, v179\n"
      "v_max3_u32 v15, v15, v180, v181\n"
      "v_max3_u32 v15, v15, v182, v183\n"
      "v_max3_u32 v15, v15, v184, v185\n"
      "v_max3_u32 v15, v15, v186, v187\n"
      "v_max3_u32 v15, v15, v188, v189\n"
      "v_max3_u32 v15, v15, v190, v191\n"
      "v_cmp_eq_u32 vcc, -1, v15\n"
      "s_cbranch_vccz .Lretry_34_1\n"
      "s_add_u32 s88, s65, 24576\n"
      "buffer_load_dwordx4 v[176:179], v2, s[4:7], s88 offen offset:0 sc1\n"
      "buffer_load_dwordx4 v[180:183], v2, s[4:7], s88 offen offset:2048 sc1\n"
      "s_add_u32 s88, s88, 4096\n"
      "buffer_load_dwordx4 v[184:187], v2, s[4:7], s88 offen offset:0 sc1\n"
      "buffer_load_dwordx4 v[188:191], v2, s[4:7], s88 offen offset:2048 sc1\n"
      "s_mov_b32 s92, 1\n"
      ".Lretry_34_1:\n"
      "v_max_u32 v15, v192, v193\n"
      "v_max3_u32 v15, v15, v194, v195\n"
      "v_max3_u32 v15, v15, v196, v197\n"
      "v_max3_u32 v15, v15, v198, v199\n"
      "v_max3_u32 v15, v15, v200, v201\n"
      "v_max3_u32 v15, v15, v202, v203\n"
      "v_max3_u32 v15, v15, v204, v205\n"
      "v_max3_u32 v15, v15, v206, v207\n"
      "v_cmp_eq_u32 vcc, -1, v15\n"
      "s_cbranch_vccz .Lretry_34_2\n"
      "s_add_u32 s88, s65, 32768\n"
      "buffer_load_dwordx4 v[192:195], v2, s[4:7], s88 offen offset:0 sc1\n"
      "buffer_load_dwordx4 v[196:199], v2, s[4:7], s88 offen offset:2048 sc1\n"
      "s_add_u32 s88, s88, 4096\n"
      "buffer_load_dwordx4 v[200:203], v2, s[4:7], s88 offen offset:0 sc1\n"
      "buffer_load_dwordx4 v[204:207], v2, s[4:7], s88 offen offset:2048 sc1\n"
      "s_mov_b32 s92, 1\n"
      ".Lretry_34_2:\n"
      "s_add_u32 s91, s91, 1\n"
      "s_cmp_gt_u32 s91, 65536\n"
      "s_cselect_b32 s92, 0, s92\n"
      "s_cmp_lg_u32 s92, 0\n"
      "s_cbranch_scc1 .Lretry_34_l\n"
      "s_waitcnt vmcnt(0)\n"
      "s_nop 7\n"
      "s_nop 7\n"
      "s_branch .Lback_35\n"
      ".Lretry_36:\n"
      "s_mov_b32 s91, 0\n"
      ".Lretry_36_l:\n"
      "s_waitcnt vmcnt(0)\n"
      "s_mov_b32 s92, 0\n"
      "v_max_u32 v15, v176, v177\n"
      "v_max3_u32 v15, v15, v178, v179\n"
      "v_max3_u32 v15, v15, v180, v181\n"
      "v_max3_u32 v15, v15, v182, v183\n"
      "v_max3_u32 v15, v15, v184, v185\n"
      "v_max3_u32 v15, v15, v186, v187\n"
      "v_max3_u32 v15, v15, v188, v189\n"
      "v_max3_u32 v15, v15, v190, v191\n"
      "v_cmp_eq_u32 vcc, -1, v15\n"
      "s_cbranch_vccz .Lretry_36_0\n"
      "s_add_u32 s88, s65, 24576\n"
      "buffer_load_dwordx4 v[176:179], v2, s[4:7], s88 offen offset:0 sc1\n"
      "buffer_load_dwordx4 v[180:183], v2, s[4:7], s88 offen offset:2048 sc1\n"
      "s_add_u32 s88, s88, 4096\n"
      "buffer_load_dwordx4 v[184:187], v2, s[4:7], s88 offen offset:0 sc1\n"
      "buffer_load_dwordx4 v[188:191], v2, s[4:7], s88 offen offset:2048 sc1\n"
      "s_mov_b32 s92, 1\n"
      ".Lretry_36_0:\n"
      "v_max_u32 v15, v192, v193\n"
      "v_max3_u32 v15, v15, v194, v195\n"
      "v_max3_u32 v15, v15, v196, v197\n"
      "v_max3_u32 v15, v15, v198, v199\n"
      "v_max3_u32 v15, v15, v200, v201\n"
      "v_max3_u32 v15, v15, v202, v203\n"
      "v_max3_u32 v15, v15, v204, v205\n"
      "v_max3_u32 v15, v15, v206, v207\n"
      "v_cmp_eq_u32 vcc, -1, v15\n"
      "s_cbranch_vccz .Lretry_36_1\n"
      "s_add_u32 s88, s65, 32768\n"
      "buffer_load_dwordx4 v[192:195], v2, s[4:7], s88 offen offset:0 sc1\n"
      "buffer_load_dwordx4 v[196:199], v2, s[4:7], s88 offen offset:2048 sc1\n"
      "s_add_u32 s88, s88, 4096\n"
      "buffer_load_dwordx4 v[200:203], v2, s[4:7], s88 offen offset:0 sc1\n"
      "buffer_load_dwordx4 v[204:207], v2, s[4:7], s88 offen offset:2048 sc1\n"
      "s_mov_b32 s92, 1\n"
      ".Lretry_36_1:\n"
      "v_max_u32 v15, v208, v209\n"
      "v_max3_u32 v15, v15, v210, v211\n"
      "v_max3_u32 v15, v15, v212, v213\n"
      "v_max3_u32 v15, v15, v214, v215\n"
      "v_max3_u32 v15, v15, v216, v217\n"
      "v_max3_u32 v15, v15, v218, v219\n"
      "v_max3_u32 v15, v15, v220, v221\n"
      "v_max3_u32 v15, v15, v222, v223\n"
      "v_cmp_eq_u32 vcc, -1, v15\n"
      "s_cbranch_vccz .Lretry_36_2\n"
      "s_add_u32 s88, s65, 40960\n"
      "buffer_load_dwordx4 v[208:211], v2, s[4:7], s88 offen offset:0 sc1\n"
      "buffer_load_dwordx4 v[212:215], v2, s[4:7], s88 offen offset:2048 sc1\n"
      "s_add_u32 s88, s88, 4096\n"
      "buffer_load_dwordx4 v[216:219], v2, s[4:7], s88 offen offset:0 sc1\n"
      "buffer_load_dwordx4 v[220:223], v2, s[4:7], s88 offen offset:2048 sc1\n"
      "s_mov_b32 s92, 1\n"
      ".Lretry_36_2:\n"
      "s_add_u32 s91, s91, 1\n"
      "s_cmp_gt_u32 s91, 65536\n"
      "s_cselect_b32 s92, 0, s92\n"
      "s_cmp_lg_u32 s92, 0\n"
      "s_cbranch_scc1 .Lretry_36_l\n"
      "s_waitcnt vmcnt(0)\n"
      "s_nop 7\n"
      "s_nop 7\n"
      "s_branch .Lback_37\n"
      ".Lretry_38:\n"
      "s_mov_b32 s91, 0\n"
      ".Lretry_38_l:\n"
      "s_waitcnt vmcnt(0)\n"
      "s_mov_b32 s92, 0\n"
      "v_max_u32 v15, v192, v193\n"
      "v_max3_u32 v15, v15, v194, v195\n"
      "v_max3_u32 v15, v15, v196, v197\n"
      "v_max3_u32 v15, v15, v198, v199\n"
      "v_max3_u32 v15, v15, v200, v201\n"
      "v_max3_u32 v15, v15, v202, v203\n"
      "v_max3_u32 v15, v15, v204, v205\n"
      "v_max3_u32 v15, v15, v206, v207\n"
      "v_cmp_eq_u32 vcc, -1, v15\n"
      "s_cbranch_vccz .Lretry_38_0\n"
      "s_add_u32 s88, s65, 32768\n"
      "buffer_load_dwordx4 v[192:195], v2, s[4:7], s88 offen offset:0 sc1\n"
      "buffer_load_dwordx4 v[196:199], v2, s[4:7], s88 offen offset:2048 sc1\n"
      "s_add_u32 s88, s88, 4096\n"
      "buffer_load_dwordx4 v[200:203], v2, s[4:7], s88 offen offset:0 sc1\n"
      "buffer_load_dwordx4 v[204:207], v2, s[4:7], s88 offen offset:2048 sc1\n"
      "s_mov_b32 s92, 1\n"
      ".Lretry_38_0:\n"
      "v_max_u32 v15, v208, v209\n"
      "v_max3_u32 v15, v15, v210, v211\n"
      "v_max3_u32 v15, v15, v212, v213\n"
      "v_max3_u32 v15, v15, v214, v215\n"
      "v_max3_u32 v15, v15, v216, v217\n"
      "v_max3_u32 v15, v15, v218, v219\n"
      "v_max3_u32 v15, v15, v220, v221\n"
      "v_max3_u32 v15, v15, v222, v223\n"
      "v_cmp_eq_u32 vcc, -1, v15\n"
      "s_cbranch_vccz .Lretry_38_1\n"
      "s_add_u32 s88, s65, 40960\n"
      "buffer_load_dwordx4 v[208:211], v2, s[4:7], s88 offen offset:0 sc1\n"
      "buffer_load_dwordx4 v[212:215], v2, s[4:7], s88 offen offset:2048 sc1\n"
      "s_add_u32 s88, s88, 4096\n"
      "buffer_load_dwordx4 v[216:219], v2, s[4:7], s88 offen offset:0 sc1\n"
      "buffer_load_dwordx4 v[220:223], v2, s[4:7], s88 offen offset:2048 sc1\n"
      "s_mov_b32 s92, 1\n"
      ".Lretry_38_1:\n"
      "v_max_u32 v15, v224, v225\n"
      "v_max3_u32 v15, v15, v226, v227\n"
      "v_max3_u32 v15, v15, v228, v229\n"
      "v_max3_u32 v15, v15, v230, v231\n"
      "v_max3_u32 v15, v15, v232, v233\n"
      "v_max3_u32 v15, v15, v234, v235\n"
      "v_max3_u32 v15, v15, v236, v237\n"
      "v_max3_u32 v15, v15, v238, v239\n"
      "v_cmp_eq_u32 vcc, -1, v15\n"
      "s_cbranch_vccz .Lretry_38_2\n"
      "s_add_u32 s88, s65, 49152\n"
      "buffer_load_dwordx4 v[224:227], v2, s[4:7], s88 offen offset:0 sc1\n"
      "buffer_load_dwordx4 v[228:231], v2, s[4:7], s88 offen offset:2048 sc1\n"
      "s_add_u32 s88, s88, 4096\n"
      "buffer_load_dwordx4 v[232:235], v2, s[4:7], s88 offen offset:0 sc1\n"
      "buffer_load_dwordx4 v[236:239], v2, s[4:7], s88 offen offset:2048 sc1\n"
      "s_mov_b32 s92, 1\n"
      ".Lretry_38_2:\n"
      "s_add_u32 s91, s91, 1\n"
      "s_cmp_gt_u32 s91, 65536\n"
      "s_cselect_b32 s92, 0, s92\n"
      "s_cmp_lg_u32 s92, 0\n"
      "s_cbranch_scc1 .Lretry_38_l\n"
      "s_waitcnt vmcnt(0)\n"
      "s_nop 7\n"
      "s_nop 7\n"
      "s_branch .Lback_39\n"
      ".Lretry_40:\n"
      "s_mov_b32 s91, 0\n"
      ".Lretry_40_l:\n"
      "s_waitcnt vmcnt(0)\n"
      "s_mov_b32 s92, 0\n"
      "v_max_u32 v15, v208, v209\n"
      "v_max3_u32 v15, v15, v210, v211\n"
      "v_max3_u32 v15, v15, v212, v213\n"
      "v_max3_u32 v15, v15, v214, v215\n"
      "v_max3_u32 v15, v15, v216, v217\n"
      "v_max3_u32 v15, v15, v218, v219\n"
      "v_max3_u32 v15, v15, v220, v221\n"
      "v_max3_u32 v15, v15, v222, v223\n"
      "v_cmp_eq_u32 vcc, -1, v15\n"
      "s_cbranch_vccz .Lretry_40_0\n"
      "s_add_u32 s88, s65, 40960\n"
      "buffer_load_dwordx4 v[208:211], v2, s[4:7], s88 offen offset:0 sc1\n"
      "buffer_load_dwordx4 v[212:215], v2, s[4:7], s88 offen offset:2048 sc1\n"
      "s_add_u32 s88, s88, 4096\n"
      "buffer_load_dwordx4 v[216:219], v2, s[4:7], s88 offen offset:0 sc1\n"
      "buffer_load_dwordx4 v[220:223], v2, s[4:7], s88 offen offset:2048 sc1\n"
      "s_mov_b32 s92, 1\n"
      ".Lretry_40_0:\n"
      "v_max_u32 v15, v224, v225\n"
      "v_max3_u32 v15, v15, v226, v227\n"
      "v_max3_u32 v15, v15, v228, v229\n"
      "v_max3_u32 v15, v15, v230, v231\n"
      "v_max3_u32 v15, v15, v232, v233\n"
      "v_max3_u32 v15, v15, v234, v235\n"
      "v_max3_u32 v15, v15, v236, v237\n"
      "v_max3_u32 v15, v15, v238, v239\n"
      "v_cmp_eq_u32 vcc, -1, v15\n"
      "s_cbranch_vccz .Lretry_40_1\n"
      "s_add_u32 s88, s65, 49152\n"
      "buffer_load_dwordx4 v[224:227], v2, s[4:7], s88 offen offset:0 sc1\n"
      "buffer_load_dwordx4 v[228:231], v2, s[4:7], s88 offen offset:2048 sc1\n"
      "s_add_u32 s88, s88, 4096\n"
      "buffer_load_dwordx4 v[232:235], v2, s[4:7], s88 offen offset:0 sc1\n"
      "buffer_load_dwordx4 v[236:239], v2, s[4:7], s88 offen offset:2048 sc1\n"
      "s_mov_b32 s92, 1\n"
      ".Lretry_40_1:\n"
      "v_max_u32 v15, v240, v241\n"
      "v_max3_u32 v15, v15, v242, v243\n"
      "v_max3_u32 v15, v15, v244, v245\n"
      "v_max3_u32 v15, v15, v246, v247\n"
      "v_max3_u32 v15, v15, v248, v249\n"
      "v_max3_u32 v15, v15, v250, v251\n"
      "v_max3_u32 v15, v15, v252, v253\n"
      "v_max3_u32 v15, v15, v254, v255\n"
      "v_cmp_eq_u32 vcc, -1, v15\n"
      "s_cbranch_vccz .Lretry_40_2\n"
      "s_add_u32 s88, s65, 57344\n"
      "buffer_load_dwordx4 v[240:243], v2, s[4:7], s88 offen offset:0 sc1\n"
      "buffer_load_dwordx4 v[244:247], v2, s[4:7], s88 offen offset:2048 sc1\n"
      "s_add_u32 s88, s88, 4096\n"
      "buffer_load_dwordx4 v[248:251], v2, s[4:7], s88 offen offset:0 sc1\n"
      "buffer_load_dwordx4 v[252:255], v2, s[4:7], s88 offen offset:2048 sc1\n"
      "s_mov_b32 s92, 1\n"
      ".Lretry_40_2:\n"
      "s_add_u32 s91, s91, 1\n"
      "s_cmp_gt_u32 s91, 65536\n"
      "s_cselect_b32 s92, 0, s92\n"
      "s_cmp_lg_u32 s92, 0\n"
      "s_cbranch_scc1 .Lretry_40_l\n"
      "s_waitcnt vmcnt(0)\n"
      "s_nop 7\n"
      "s_nop 7\n"
      "s_branch .Lback_41\n"
      ".Lretry_42:\n"
      "s_mov_b32 s91, 0\n"
      ".Lretry_42_l:\n"
      "s_waitcnt vmcnt(0)\n"
      "s_mov_b32 s92, 0\n"
      "v_max_u32 v15, v224, v225\n"
      "v_max3_u32 v15, v15, v226, v227\n"
      "v_max3_u32 v15, v15, v228, v229\n"
      "v_max3_u32 v15, v15, v230, v231\n"
      "v_max3_u32 v15, v15, v232, v233\n"
      "v_max3_u32 v15, v15, v234, v235\n"
      "v_max3_u32 v15, v15, v236, v237\n"
      "v_max3_u32 v15, v15, v238, v239\n"
      "v_cmp_eq_u32 vcc, -1, v15\n"
      "s_cbranch_vccz .Lretry_42_0\n"
      "s_add_u32 s88, s65, 49152\n"
      "buffer_load_dwordx4 v[224:227], v2, s[4:7], s88 offen offset:0 sc1\n"
      "buffer_load_dwordx4 v[228:231], v2, s[4:7], s88 offen offset:2048 sc1\n"
      "s_add_u32 s88, s88, 4096\n"
      "buffer_load_dwordx4 v[232:235], v2, s[4:7], s88 offen offset:0 sc1\n"
      "buffer_load_dwordx4 v[236:239], v2, s[4:7], s88 offen offset:2048 sc1\n"
      "s_mov_b32 s92, 1\n"
      ".Lretry_42_0:\n"
      "v_max_u32 v15, v240, v241\n"
      "v_max3_u32 v15, v15, v242, v243\n"
      "v_max3_u32 v15, v15, v244, v245\n"
      "v_max3_u32 v15, v15, v246, v247\n"
      "v_max3_u32 v15, v15, v248, v249\n"
      "v_max3_u32 v15, v15, v250, v251\n"
      "v_max3_u32 v15, v15, v252, v253\n"
      "v_max3_u32 v15, v15, v254, v255\n"
      "v_cmp_eq_u32 vcc, -1, v15\n"
      "s_cbranch_vccz .Lretry_42_1\n"
      "s_add_u32 s88, s65, 57344\n"
      "buffer_load_dwordx4 v[240:243], v2, s[4:7], s88 offen offset:0 sc1\n"
      "buffer_load_dwordx4 v[244:247], v2, s[4:7], s88 offen offset:2048 sc1\n"
      "s_add_u32 s88, s88, 4096\n"
      "buffer_load_dwordx4 v[248:251], v2, s[4:7], s88 offen offset:0 sc1\n"
      "buffer_load_dwordx4 v[252:255], v2, s[4:7], s88 offen offset:2048 sc1\n"
      "s_mov_b32 s92, 1\n"
      ".Lretry_42_1:\n"
      "s_add_u32 s91, s91, 1\n"
      "s_cmp_gt_u32 s91, 65536\n"
      "s_cselect_b32 s92, 0, s92\n"
      "s_cmp_lg_u32 s92, 0\n"
      "s_cbranch_scc1 .Lretry_42_l\n"
      "s_waitcnt vmcnt(0)\n"
      "s_nop 7\n"
      "s_nop 7\n"
      "s_branch .Lback_43\n"
      ".Lretry_44:\n"
      "s_mov_b32 s91, 0\n"
      ".Lretry_44_l:\n"
      "s_waitcnt vmcnt(0)\n"
      "s_mov_b32 s92, 0\n"
      "v_max_u32 v15, v240, v241\n"
      "v_max3_u32 v15, v15, v242, v243\n"
      "v_max3_u32 v15, v15, v244, v245\n"
      "v_max3_u32 v15, v15, v246, v247\n"
      "v_max3_u32 v15, v15, v248, v249\n"
      "v_max3_u32 v15, v15, v250, v251\n"
      "v_max3_u32 v15, v15, v252, v253\n"
      "v_max3_u32 v15, v15, v254, v255\n"
      "v_cmp_eq_u32 vcc, -1, v15\n"
      "s_cbranch_vccz .Lretry_44_0\n"
      "s_add_u32 s88, s65, 57344\n"
      "buffer_load_dwordx4 v[240:243], v2, s[4:7], s88 offen offset:0 sc1\n"
      "buffer_load_dwordx4 v[244:247], v2, s[4:7], s88 offen offset:2048 sc1\n"
      "s_add_u32 s88, s88, 4096\n"
      "buffer_load_dwordx4 v[248:251], v2, s[4:7], s88 offen offset:0 sc1\n"
      "buffer_load_dwordx4 v[252:255], v2, s[4:7], s88 offen offset:2048 sc1\n"
      "s_mov_b32 s92, 1\n"
      ".Lretry_44_0:\n"
      "s_add_u32 s91, s91, 1\n"
      "s_cmp_gt_u32 s91, 65536\n"
      "s_cselect_b32 s92, 0, s92\n"
      "s_cmp_lg_u32 s92, 0\n"
      "s_cbranch_scc1 .Lretry_44_l\n"
      "s_waitcnt vmcnt(0)\n"
      "s_nop 7\n"
      "s_nop 7\n"
      "s_branch .Lback_45\n"
      ".Lretry_47:\n"
      "s_mov_b32 s91, 0\n"
      ".Lretry_47_l:\n"
      "s_nop 7\n"
      "s_nop 7\n"
      "s_add_u32 s91, s91, 1\n"
      "s_cmp_gt_u32 s91, 65536\n"
      "s_cbranch_scc1 .Lback_48\n"
      "buffer_load_dword v13, v20, s[4:7], s76 offen sc1\n"
      "s_waitcnt vmcnt(0)\n"
      "v_cmp_ne_u32 vcc, s86, v13\n"
      "s_cbranch_vccnz .Lretry_47_l\n"
      "s_nop 4\n"
      "s_branch .Lback_48\n"
      ".Lretry_49:\n"
      "s_mov_b32 s91, 0\n"
      ".Lretry_49_l:\n"
      "s_waitcnt vmcnt(0)\n"
      "s_mov_b32 s92, 0\n"
      "v_accvgpr_read_b32 v124, a176\n"
      "v_accvgpr_read_b32 v125, a177\n"
      "v_max_u32 v15, v124, v125\n"
      "v_accvgpr_read_b32 v124, a178\n"
      "v_accvgpr_read_b32 v125, a179\n"
      "v_max3_u32 v15, v15, v124, v125\n"
      "v_accvgpr_read_b32 v124, a180\n"
      "v_accvgpr_read_b32 v125, a181\n"
      "v_max3_u32 v15, v15, v124, v125\n"
      "v_accvgpr_read_b32 v124, a182\n"
      "v_accvgpr_read_b32 v125, a183\n"
      "v_max3_u32 v15, v15, v124, v125\n"
      "v_accvgpr_read_b32 v124, a184\n"
      "v_accvgpr_read_b32 v125, a185\n"
      "v_max3_u32 v15, v15, v124, v125\n"
      "v_accvgpr_read_b32 v124, a186\n"
      "v_accvgpr_read_b32 v125, a187\n"
      "v_max3_u32 v15, v15, v124, v125\n"
      "v_accvgpr_read_b32 v124, a188\n"
      "v_accvgpr_read_b32 v125, a189\n"
      "v_max3_u32 v15, v15, v124, v125\n"
      "v_accvgpr_read_b32 v124, a190\n"
      "v_accvgpr_read_b32 v125, a191\n"
      "v_max3_u32 v15, v15, v124, v125\n"
      "v_cmp_eq_u32 vcc, -1, v15\n"
      "s_cbranch_vccz .Lretry_49_0\n"
      "s_add_u32 s89, s61, 0\n"
      "buffer_load_dwordx4 a[176:179], v2, s[4:7], s89 offen offset:0 sc1\n"
      "buffer_load_dwordx4 a[180:183], v2, s[4:7], s89 offen offset:2048 sc1\n"
      "s_add_u32 s89, s89, 4096\n"
      "buffer_load_dwordx4 a[184:187], v4, s[4:7], s89 offen offset:0 sc1\n"
      "buffer_load_dwordx4 a[188:191], v4, s[4:7], s89 offen offset:2048 sc1\n"
      "s_mov_b32 s92, 1\n"
      ".Lretry_49_0:\n"
      "v_accvgpr_read_b32 v124, a192\n"
      "v_accvgpr_read_b32 v125, a193\n"
      "v_max_u32 v15, v124, v125\n"
      "v_accvgpr_read_b32 v124, a194\n"
      "v_accvgpr_read_b32 v125, a195\n"
      "v_max3_u32 v15, v15, v124, v125\n"
      "v_accvgpr_read_b32 v124, a196\n"
      "v_accvgpr_read_b32 v125, a197\n"
      "v_max3_u32 v15, v15, v124, v125\n"
      "v_accvgpr_read_b32 v124, a198\n"
      "v_accvgpr_read_b32 v125, a199\n"
      "v_max3_u32 v15, v15, v124, v125\n"
      "v_accvgpr_read_b32 v124, a200\n"
      "v_accvgpr_read_b32 v125, a201\n"
      "v_max3_u32 v15, v15, v124, v125\n"
      "v_accvgpr_read_b32 v124, a202\n"
      "v_accvgpr_read_b32 v125, a203\n"
      "v_max3_u32 v15, v15, v124, v125\n"
      "v_accvgpr_read_b32 v124, a204\n"
      "v_accvgpr_read_b32 v125, a205\n"
      "v_max3_u32 v15, v15, v124, v125\n"
      "v_accvgpr_read_b32 v124, a206\n"
      "v_accvgpr_read_b32 v125, a207\n"
      "v_max3_u32 v15, v15, v124, v125\n"
      "v_cmp_eq_u32 vcc, -1, v15\n"
      "s_cbranch_vccz .Lretry_49_1\n"
      "s_add_u32 s89, s61, 8192\n"
      "buffer_load_dwordx4 a[192:195], v2, s[4:7], s89 offen offset:0 sc1\n"
      "buffer_load_dwordx4 a[196:199], v2, s[4:7], s89 offen offset:2048 sc1\n"
      "s_add_u32 s89, s89, 4096\n"
      "buffer_load_dwordx4 a[200:203], v2, s[4:7], s89 offen offset:0 sc1\n"
      "buffer_load_dwordx4 a[204:207], v2, s[4:7], s89 offen offset:2048 sc1\n"
      "s_mov_b32 s92, 1\n"
      ".Lretry_49_1:\n"
      "v_accvgpr_read_b32 v124, a208\n"
      "v_accvgpr_read_b32 v125, a209\n"
      "v_max_u32 v15, v124, v125\n"
      "v_accvgpr_read_b32 v124, a210\n"
      "v_accvgpr_read_b32 v125, a211\n"
      "v_max3_u32 v15, v15, v124, v125\n"
      "v_accvgpr_read_b32 v124, a212\n"
      "v_accvgpr_read_b32 v125, a213\n"
      "v_max3_u32 v15, v15, v124, v125\n"
      "v_accvgpr_read_b32 v124, a214\n"
      "v_accvgpr_read_b32 v125, a215\n"
      "v_max3_u32 v15, v15, v124, v125\n"
      "v_accvgpr_read_b32 v124, a216\n"
      "v_accvgpr_read_b32 v125, a217\n"
      "v_max3_u32 v15, v15, v124, v125\n"
      "v_accvgpr_read_b32 v124, a218\n"
      "v_accvgpr_read_b32 v125, a219\n"
      "v_max3_u32 v15, v15, v124, v125\n"
      "v_accvgpr_read_b32 v124, a220\n"
      "v_accvgpr_read_b32 v125, a221\n"
      "v_max3_u32 v15, v15, v124, v125\n"
      "v_accvgpr_read_b32 v124, a222\n"
      "v_accvgpr_read_b32 v125, a223\n"
      "v_max3_u32 v15, v15, v124, v125\n"
      "v_cmp_eq_u32 vcc, -1, v15\n"
      "s_cbranch_vccz .Lretry_49_2\n"
      "s_add_u32 s89, s61, 16384\n"
      "buffer_load_dwordx4 a[208:211], v2, s[4:7], s89 offen offset:0 sc1\n"
      "buffer_load_dwordx4 a[212:215], v2, s[4:7], s89 offen offset:2048 sc1\n"
      "s_add_u32 s89, s89, 4096\n"
      "buffer_load_dwordx4 a[216:219], v2, s[4:7], s89 offen offset:0 sc1\n"
      "buffer_load_dwordx4 a[220:223], v2, s[4:7], s89 offen offset:2048 sc1\n"
      "s_mov_b32 s92, 1\n"
      ".Lretry_49_2:\n"
      "s_add_u32 s91, s91, 1\n"
      "s_cmp_gt_u32 s91, 65536\n"
      "s_cselect_b32 s92, 0, s92\n"
      "s_cmp_lg_u32 s92, 0\n"
      "s_cbranch_scc1 .Lretry_49_l\n"
      "s_waitcnt vmcnt(0)\n"
      "s_nop 7\n"
      "s_nop 7\n"
      "v_mfma_f32_32x32x16_bf16 v[32:47], a[0:3], a[176:179], 0\n"
      "v_mfma_f32_32x32x16_bf16 v[48:63], a[4:7], a[176:179], 0\n"
      "v_mfma_f32_32x32x16_bf16 v[64:79], a[0:3], a[180:183], 0\n"
      "v_mfma_f32_32x32x16_bf16 v[80:95], a[4:7], a[180:183], 0\n"
      "v_mfma_f32_32x32x16_bf16 v[32:47], a[8:11], a[184:187], v[32:47]\n"
      "v_mfma_f32_32x32x16_bf16 v[48:63], a[12:15], a[184:187], v[48:63]\n"
      "v_mfma_f32_32x32x16_bf16 v[64:79], a[8:11], a[188:191], v[64:79]\n"
      "v_mfma_f32_32x32x16_bf16 v[80:95], a[12:15], a[188:191], v[80:95]\n"
      "s_branch .Lback_50\n"
      ".Lretry_51:\n"
      "s_mov_b32 s91, 0\n"
      ".Lretry_51_l:\n"
      "s_waitcnt vmcnt(0)\n"
      "s_mov_b32 s92, 0\n"
      "v_accvgpr_read_b32 v124, a208\n"
      "v_accvgpr_read_b32 v125, a209\n"
      "v_max_u32 v15, v124, v125\n"
      "v_accvgpr_read_b32 v124, a210\n"
      "v_accvgpr_read_b32 v125, a211\n"
      "v_max3_u32 v15, v15, v124, v125\n"
      "v_accvgpr_read_b32 v124, a212\n"
      "v_accvgpr_read_b32 v125, a213\n"
      "v_max3_u32 v15, v15, v124, v125\n"
      "v_accvgpr_read_b32 v124, a214\n"
      "v_accvgpr_read_b32 v125, a215\n"
      "v_max3_u32 v15, v15, v124, v125\n"
      "v_accvgpr_read_b32 v124, a216\n"
      "v_accvgpr_read_b32 v125, a217\n"
      "v_max3_u32 v15, v15, v124, v125\n"
      "v_accvgpr_read_b32 v124, a218\n"
      "v_accvgpr_read_b32 v125, a219\n"
      "v_max3_u32 v15, v15, v124, v125\n"
      "v_accvgpr_read_b32 v124, a220\n"
      "v_accvgpr_read_b32 v125, a221\n"
      "v_max3_u32 v15, v15, v124, v125\n"
      "v_accvgpr_read_b32 v124, a222\n"
      "v_accvgpr_read_b32 v125, a223\n"
      "v_max3_u32 v15, v15, v124, v125\n"
      "v_cmp_eq_u32 vcc, -1, v15\n"
      "s_cbranch_vccz .Lretry_51_0\n"
      "s_add_u32 s89, s61, 16384\n"
      "buffer_load_dwordx4 a[208:211], v2, s[4:7], s89 offen offset:0 sc1\n"
      "buffer_load_dwordx4 a[212:215], v2, s[4:7], s89 offen offset:2048 sc1\n"
      "s_add_u32 s89, s89, 4096\n"
      "buffer_load_dwordx4 a[216:219], v2, s[4:7], s89 offen offset:0 sc1\n"
      "buffer_load_dwordx4 a[220:223], v2, s[4:7], s89 offen offset:2048 sc1\n"
      "s_mov_b32 s92, 1\n"
      ".Lretry_51_0:\n"
      "v_max_u32 v15, v128, v129\n"
      "v_max3_u32 v15, v15, v130, v131\n"
      "v_max3_u32 v15, v15, v132, v133\n"
      "v_max3_u32 v15, v15, v134, v135\n"
      "v_max3_u32 v15, v15, v136, v137\n"
      "v_max3_u32 v15, v15, v138, v139\n"
      "v_max3_u32 v15, v15, v140, v141\n"
      "v_max3_u32 v15, v15, v142, v143\n"
      "v_cmp_eq_u32 vcc, -1, v15\n"
      "s_cbranch_vccz .Lretry_51_1\n"
      "s_add_u32 s89, s61, 24576\n"
      "buffer_load_dwordx4 v[128:131], v2, s[4:7], s89 offen offset:0 sc1\n"
      "buffer_load_dwordx4 v[132:135], v2, s[4:7], s89 offen offset:2048 sc1\n"
      "s_add_u32 s89, s89, 4096\n"
      "buffer_load_dwordx4 v[136:139], v2, s[4:7], s89 offen offset:0 sc1\n"
      "buffer_load_dwordx4 v[140:143], v2, s[4:7], s89 offen offset:2048 sc1\n"
      "s_mov_b32 s92, 1\n"
      ".Lretry_51_1:\n"
      "s_add_u32 s91, s91, 1\n"
      "s_cmp_gt_u32 s91, 65536\n"
      "s_cselect_b32 s92, 0, s92\n"
      "s_cmp_lg_u32 s92, 0\n"
      "s_cbranch_scc1 .Lretry_51_l\n"
      "s_waitcnt vmcnt(0)\n"
      "s_nop 7\n"
      "s_nop 7\n"
      "s_branch .Lback_52\n"
      ".Lretry_53:\n"
      "s_mov_b32 s91, 0\n"
      ".Lretry_53_l:\n"
      "s_waitcnt vmcnt(0)\n"
      "s_mov_b32 s92, 0\n"
      "v_max_u32 v15, v128, v129\n"
      "v_max3_u32 v15, v15, v130, v131\n"
      "v_max3_u32 v15, v15, v132, v133\n"
      "v_max3_u32 v15, v15, v134, v135\n"
      "v_max3_u32 v15, v15, v136, v137\n"
      "v_max3_u32 v15, v15, v138, v139\n"
      "v_max3_u32 v15, v15, v140, v141\n"
      "v_max3_u32 v15, v15, v142, v143\n"
      "v_cmp_eq_u32 vcc, -1, v15\n"
      "s_cbranch_vccz .Lretry_53_0\n"
      "s_add_u32 s89, s61, 24576\n"
      "buffer_load_dwordx4 v[128:131], v2, s[4:7], s89 offen offset:0 sc1\n"
      "buffer_load_dwordx4 v[132:135], v2, s[4:7], s89 offen offset:2048 sc1\n"
      "s_add_u32 s89, s89, 4096\n"
      "buffer_load_dwordx4 v[136:139], v2, s[4:7], s89 offen offset:0 sc1\n"
      "buffer_load_dwordx4 v[140:143], v2, s[4:7], s89 offen offset:2048 sc1\n"
      "s_mov_b32 s92, 1\n"
      ".Lretry_53_0:\n"
      "v_max_u32 v15, v144, v145\n"
      "v_max3_u32 v15, v15, v146, v147\n"
      "v_max3_u32 v15, v15, v148, v149\n"
      "v_max3_u32 v15, v15, v150, v151\n"
      "v_max3_u32 v15, v15, v152, v153\n"
      "v_max3_u32 v15, v15, v154, v155\n"
      "v_max3_u32 v15, v15, v156, v157\n"
      "v_max3_u32 v15, v15, v158, v159\n"
      "v_cmp_eq_u32 vcc, -1, v15\n"
      "s_cbranch_vccz .Lretry_53_1\n"
      "s_add_u32 s89, s61, 32768\n"
      "buffer_load_dwordx4 v[144:147], v2, s[4:7], s89 offen offset:0 sc1\n"
      "buffer_load_dwordx4 v[148:151], v2, s[4:7], s89 offen offset:2048 sc1\n"
      "s_add_u32 s89, s89, 4096\n"
      "buffer_load_dwordx4 v[152:155], v2, s[4:7], s89 offen offset:0 sc1\n"
      "buffer_load_dwordx4 v[156:159], v2, s[4:7], s89 offen offset:2048 sc1\n"
      "s_mov_b32 s92, 1\n"
      ".Lretry_53_1:\n"
      "s_add_u32 s91, s91, 1\n"
      "s_cmp_gt_u32 s91, 65536\n"
      "s_cselect_b32 s92, 0, s92\n"
      "s_cmp_lg_u32 s92, 0\n"
      "s_cbranch_scc1 .Lretry_53_l\n"
      "s_waitcnt vmcnt(0)\n"
      "s_nop 7\n"
      "s_nop 7\n"
      "s_branch .Lback_54\n"
      ".Lretry_55:\n"
      "s_mov_b32 s91, 0\n"
      ".Lretry_55_l:\n"
      "s_waitcnt vmcnt(0)\n"
      "s_mov_b32 s92, 0\n"
      "v_max_u32 v15, v144, v145\n"
      "v_max3_u32 v15, v15, v146, v147\n"
      "v_max3_u32 v15, v15, v148, v149\n"
      "v_max3_u32 v15, v15, v150, v151\n"
      "v_max3_u32 v15, v15, v152, v153\n"
      "v_max3_u32 v15, v15, v154, v155\n"
      "v_max3_u32 v15, v15, v156, v157\n"
      "v_max3_u32 v15, v15, v158, v159\n"
      "v_cmp_eq_u32 vcc, -1, v15\n"
      "s_cbranch_vccz .Lretry_55_0\n"
      "s_add_u32 s89, s61, 32768\n"
      "buffer_load_dwordx4 v[144:147], v2, s[4:7], s89 offen offset:0 sc1\n"
      "buffer_load_dwordx4 v[148:151], v2, s[4:7], s89 offen offset:2048 sc1\n"
      "s_add_u32 s89, s89, 4096\n"
      "buffer_load_dwordx4 v[152:155], v2, s[4:7], s89 offen offset:0 sc1\n"
      "buffer_load_dwordx4 v[156:159], v2, s[4:7], s89 offen offset:2048 sc1\n"
      "s_mov_b32 s92, 1\n"
      ".Lretry_55_0:\n"
      "v_max_u32 v15, v160, v161\n"
      "v_max3_u32 v15, v15, v162, v163\n"
      "v_max3_u32 v15, v15, v164, v165\n"
      "v_max3_u32 v15, v15, v166, v167\n"
      "v_max3_u32 v15, v15, v168, v169\n"
      "v_max3_u32 v15, v15, v170, v171\n"
      "v_max3_u32 v15, v15, v172, v173\n"
      "v_max3_u32 v15, v15, v174, v175\n"
      "v_cmp_eq_u32 vcc, -1, v15\n"
      "s_cbranch_vccz .Lretry_55_1\n"
      "s_add_u32 s89, s61, 40960\n"
      "buffer_load_dwordx4 v[160:163], v2, s[4:7], s89 offen offset:0 sc1\n"
      "buffer_load_dwordx4 v[164:167], v2, s[4:7], s89 offen offset:2048 sc1\n"
      "s_add_u32 s89, s89, 4096\n"
      "buffer_load_dwordx4 v[168:171], v2, s[4:7], s89 offen offset:0 sc1\n"
      "buffer_load_dwordx4 v[172:175], v2, s[4:7], s89 offen offset:2048 sc1\n"
      "s_mov_b32 s92, 1\n"
      ".Lretry_55_1:\n"
      "s_add_u32 s91, s91, 1\n"
      "s_cmp_gt_u32 s91, 65536\n"
      "s_cselect_b32 s92, 0, s92\n"
      "s_cmp_lg_u32 s92, 0\n"
      "s_cbranch_scc1 .Lretry_55_l\n"
      "s_waitcnt vmcnt(0)\n"
      "s_nop 7\n"
      "s_nop 7\n"
      "s_branch .Lback_56\n"
      ".Lretry_57:\n"
      "s_mov_b32 s91, 0\n"
      ".Lretry_57_l:\n"
      "s_waitcnt vmcnt(0)\n"
      "s_mov_b32 s92, 0\n"
      "v_max_u32 v15, v160, v161\n"
      "v_max3_u32 v15, v15, v162, v163\n"
      "v_max3_u32 v15, v15, v164, v165\n"
      "v_max3_u32 v15, v15, v166, v167\n"
      "v_max3_u32 v15, v15, v168, v169\n"
      "v_max3_u32 v15, v15, v170, v171\n"
      "v_max3_u32 v15, v15, v172, v173\n"
      "v_max3_u32 v15, v15, v174, v175\n"
      "v_cmp_eq_u32 vcc, -1, v15\n"
      "s_cbranch_vccz .Lretry_57_0\n"
      "s_add_u32 s89, s61, 40960\n"
      "buffer_load_dwordx4 v[160:163], v2, s[4:7], s89 offen offset:0 sc1\n"
      "buffer_load_dwordx4 v[164:167], v2, s[4:7], s89 offen offset:2048 sc1\n"
      "s_add_u32 s89, s89, 4096\n"
      "buffer_load_dwordx4 v[168:171], v2, s[4:7], s89 offen offset:0 sc1\n"
      "buffer_load_dwordx4 v[172:175], v2, s[4:7], s89 offen offset:2048 sc1\n"
      "s_mov_b32 s92, 1\n"
      ".Lretry_57_0:\n"
      "v_max_u32 v15, v176, v177\n"
      "v_max3_u32 v15, v15, v178, v179\n"
      "v_max3_u32 v15, v15, v180, v181\n"
      "v_max3_u32 v15, v15, v182, v183\n"
      "v_max3_u32 v15, v15, v184, v185\n"
      "v_max3_u32 v15, v15, v186, v187\n"
      "v_max3_u32 v15, v15, v188, v189\n"
      "v_max3_u32 v15, v15, v190, v191\n"
      "v_cmp_eq_u32 vcc, -1, v15\n"
      "s_cbranch_vccz .Lretry_57_1\n"
      "s_add_u32 s89, s61, 49152\n"
      "buffer_load_dwordx4 v[176:179], v2, s[4:7], s89 offen offset:0 sc1\n"
      "buffer_load_dwordx4 v[180:183], v2, s[4:7], s89 offen offset:2048 sc1\n"
      "s_add_u32 s89, s89, 4096\n"
      "buffer_load_dwordx4 v[184:187], v2, s[4:7], s89 offen offset:0 sc1\n"
      "buffer_load_dwordx4 v[188:191], v2, s[4:7], s89 offen offset:2048 sc1\n"
      "s_mov_b32 s92, 1\n"
      ".Lretry_57_1:\n"
      "s_add_u32 s91, s91, 1\n"
      "s_cmp_gt_u32 s91, 65536\n"
      "s_cselect_b32 s92, 0, s92\n"
      "s_cmp_lg_u32 s92, 0\n"
      "s_cbranch_scc1 .Lretry_57_l\n"
      "s_waitcnt vmcnt(0)\n"
      "s_nop 7\n"
      "s_nop 7\n"
      "s_branch .Lback_58\n"
      ".Lretry_59:\n"
      "s_mov_b32 s91, 0\n"
      ".Lretry_59_l:\n"
      "s_waitcnt vmcnt(0)\n"
      "s_mov_b32 s92, 0\n"
      "v_max_u32 v15, v176, v177\n"
      "v_max3_u32 v15, v15, v178, v179\n"
      "v_max3_u32 v15, v15, v180, v181\n"
      "v_max3_u32 v15, v15, v182, v183\n"
      "v_max3_u32 v15, v15, v184, v185\n"
      "v_max3_u32 v15, v15, v186, v187\n"
      "v_max3_u32 v15, v15, v188, v189\n"
      "v_max3_u32 v15, v15, v190, v191\n"
      "v_cmp_eq_u32 vcc, -1, v15\n"
      "s_cbranch_vccz .Lretry_59_0\n"
      "s_add_u32 s89, s61, 49152\n"
      "buffer_load_dwordx4 v[176:179], v2, s[4:7], s89 offen offset:0 sc1\n"
      "buffer_load_dwordx4 v[180:183], v2, s[4:7], s89 offen offset:2048 sc1\n"
      "s_add_u32 s89, s89, 4096\n"
      "buffer_load_dwordx4 v[184:187], v2, s[4:7], s89 offen offset:0 sc1\n"
      "buffer_load_dwordx4 v[188:191], v2, s[4:7], s89 offen offset:2048 sc1\n"
      "s_mov_b32 s92, 1\n"
      ".Lretry_59_0:\n"
      "v_max_u32 v15, v192, v193\n"
      "v_max3_u32 v15, v15, v194, v195\n"
      "v_max3_u32 v15, v15, v196, v197\n"
      "v_max3_u32 v15, v15, v198, v199\n"
      "v_max3_u32 v15, v15, v200, v201\n"
      "v_max3_u32 v15, v15, v202, v203\n"
      "v_max3_u32 v15, v15, v204, v205\n"
      "v_max3_u32 v15, v15, v206, v207\n"
      "v_cmp_eq_u32 vcc, -1, v15\n"
      "s_cbranch_vccz .Lretry_59_1\n"
      "s_add_u32 s89, s61, 57344\n"
      "buffer_load_dwordx4 v[192:195], v2, s[4:7], s89 offen offset:0 sc1\n"
      "buffer_load_dwordx4 v[196:199], v2, s[4:7], s89 offen offset:2048 sc1\n"
      "s_add_u32 s89, s89, 4096\n"
      "buffer_load_dwordx4 v[200:203], v2, s[4:7], s89 offen offset:0 sc1\n"
      "buffer_load_dwordx4 v[204:207], v2, s[4:7], s89 offen offset:2048 sc1\n"
      "s_mov_b32 s92, 1\n"
      ".Lretry_59_1:\n"
      "s_add_u32 s91, s91, 1\n"
      "s_cmp_gt_u32 s91, 65536\n"
      "s_cselect_b32 s92, 0, s92\n"
      "s_cmp_lg_u32 s92, 0\n"
      "s_cbranch_scc1 .Lretry_59_l\n"
      "s_waitcnt vmcnt(0)\n"
      "s_nop 7\n"
      "s_nop 7\n"
      "s_branch .Lback_60\n"
      ".Lretry_61:\n"
      "s_mov_b32 s91, 0\n"
      ".Lretry_61_l:\n"
      "s_waitcnt vmcnt(0)\n"
      "s_mov_b32 s92, 0\n"
      "v_max_u32 v15, v192, v193\n"
      "v_max3_u32 v15, v15, v194, v195\n"
      "v_max3_u32 v15, v15, v196, v197\n"
      "v_max3_u32 v15, v15, v198, v199\n"
      "v_max3_u32 v15, v15, v200, v201\n"
      "v_max3_u32 v15, v15, v202, v203\n"
      "v_max3_u32 v15, v15, v204, v205\n"
      "v_max3_u32 v15, v15, v206, v207\n"
      "v_cmp_eq_u32 vcc, -1, v15\n"
      "s_cbranch_vccz .Lretry_61_0\n"
      "s_add_u32 s89, s61, 57344\n"
      "buffer_load_dwordx4 v[192:195], v2, s[4:7], s89 offen offset:0 sc1\n"
      "buffer_load_dwordx4 v[196:199], v2, s[4:7], s89 offen offset:2048 sc1\n"
      "s_add_u32 s89, s89, 4096\n"
      "buffer_load_dwordx4 v[200:203], v2, s[4:7], s89 offen offset:0 sc1\n"
      "buffer_load_dwordx4 v[204:207], v2, s[4:7], s89 offen offset:2048 sc1\n"
      "s_mov_b32 s92, 1\n"
      ".Lretry_61_0:\n"
      "v_max_u32 v15, v208, v209\n"
      "v_max3_u32 v15, v15, v210, v211\n"
      "v_max3_u32 v15, v15, v212, v213\n"
      "v_max3_u32 v15, v15, v214, v215\n"
      "v_max3_u32 v15, v15, v216, v217\n"
      "v_max3_u32 v15, v15, v218, v219\n"
      "v_max3_u32 v15, v15, v220, v221\n"
      "v_max3_u32 v15, v15, v222, v223\n"
      "v_cmp_eq_u32 vcc, -1, v15\n"
      "s_cbranch_vccz .Lretry_61_1\n"
      "s_add_u32 s89, s61, 65536\n"
      "buffer_load_dwordx4 v[208:211], v2, s[4:7], s89 offen offset:0 sc1\n"
      "buffer_load_dwordx4 v[212:215], v2, s[4:7], s89 offen offset:2048 sc1\n"
      "s_add_u32 s89, s89, 4096\n"
      "buffer_load_dwordx4 v[216:219], v2, s[4:7], s89 offen offset:0 sc1\n"
      "buffer_load_dwordx4 v[220:223], v2, s[4:7], s89 offen offset:2048 sc1\n"
      "s_mov_b32 s92, 1\n"
      ".Lretry_61_1:\n"
      "s_add_u32 s91, s91, 1\n"
      "s_cmp_gt_u32 s91, 65536\n"
      "s_cselect_b32 s92, 0, s92\n"
      "s_cmp_lg_u32 s92, 0\n"
      "s_cbranch_scc1 .Lretry_61_l\n"
      "s_waitcnt vmcnt(0)\n"
      "s_nop 7\n"
      "s_nop 7\n"
      "s_branch .Lback_62\n"
      ".Lretry_63:\n"
      "s_mov_b32 s91, 0\n"
      ".Lretry_63_l:\n"
      "s_waitcnt vmcnt(0)\n"
      "s_mov_b32 s92, 0\n"
      "v_max_u32 v15, v208, v209\n"
      "v_max3_u32 v15, v15, v210, v211\n"
      "v_max3_u32 v15, v15, v212, v213\n"
      "v_max3_u32 v15, v15, v214, v215\n"
      "v_max3_u32 v15, v15, v216, v217\n"
      "v_max3_u32 v15, v15, v218, v219\n"
      "v_max3_u32 v15, v15, v220, v221\n"
      "v_max3_u32 v15, v15, v222, v223\n"
      "v_cmp_eq_u32 vcc, -1, v15\n"
      "s_cbranch_vccz .Lretry_63_0\n"
      "s_add_u32 s89, s61, 65536\n"
      "buffer_load_dwordx4 v[208:211], v2, s[4:7], s89 offen offset:0 sc1\n"
      "buffer_load_dwordx4 v[212:215], v2, s[4:7], s89 offen offset:2048 sc1\n"
      "s_add_u32 s89, s89, 4096\n"
      "buffer_load_dwordx4 v[216:219], v2, s[4:7], s89 offen offset:0 sc1\n"
      "buffer_load_dwordx4 v[220:223], v2, s[4:7], s89 offen offset:2048 sc1\n"
      "s_mov_b32 s92, 1\n"
      ".Lretry_63_0:\n"
      "v_max_u32 v15, v224, v225\n"
      "v_max3_u32 v15, v15, v226, v227\n"
      "v_max3_u32 v15, v15, v228, v229\n"
      "v_max3_u32 v15, v15, v230, v231\n"
      "v_max3_u32 v15, v15, v232, v233\n"
      "v_max3_u32 v15, v15, v234, v235\n"
      "v_max3_u32 v15, v15, v236, v237\n"
      "v_max3_u32 v15, v15, v238, v239\n"
      "v_cmp_eq_u32 vcc, -1, v15\n"
      "s_cbranch_vccz .Lretry_63_1\n"
      "s_add_u32 s89, s61, 73728\n"
      "buffer_load_dwordx4 v[224:227], v2, s[4:7], s89 offen offset:0 sc1\n"
      "buffer_load_dwordx4 v[228:231], v2, s[4:7], s89 offen offset:2048 sc1\n"
      "s_add_u32 s89, s89, 4096\n"
      "buffer_load_dwordx4 v[232:235], v2, s[4:7], s89 offen offset:0 sc1\n"
      "buffer_load_dwordx4 v[236:239], v2, s[4:7], s89 offen offset:2048 sc1\n"
      "s_mov_b32 s92, 1\n"
      ".Lretry_63_1:\n"
      "s_add_u32 s91, s91, 1\n"
      "s_cmp_gt_u32 s91, 65536\n"
      "s_cselect_b32 s92, 0, s92\n"
      "s_cmp_lg_u32 s92, 0\n"
      "s_cbranch_scc1 .Lretry_63_l\n"
      "s_waitcnt vmcnt(0)\n"
      "s_nop 7\n"
      "s_nop 7\n"
      "s_branch .Lback_64\n"
      ".Lretry_65:\n"
      "s_mov_b32 s91, 0\n"
      ".Lretry_65_l:\n"
      "s_waitcnt vmcnt(0)\n"
      "s_mov_b32 s92, 0\n"
      "v_max_u32 v15, v224, v225\n"
      "v_max3_u32 v15, v15, v226, v227\n"
      "v_max3_u32 v15, v15, v228, v229\n"
      "v_max3_u32 v15, v15, v230, v231\n"
      "v_max3_u32 v15, v15, v232, v233\n"
      "v_max3_u32 v15, v15, v234, v235\n"
      "v_max3_u32 v15, v15, v236, v237\n"
      "v_max3_u32 v15, v15, v238, v239\n"
      "v_cmp_eq_u32 vcc, -1, v15\n"
      "s_cbranch_vccz .Lretry_65_0\n"
      "s_add_u32 s89, s61, 73728\n"
      "buffer_load_dwordx4 v[224:227], v2, s[4:7], s89 offen offset:0 sc1\n"
      "buffer_load_dwordx4 v[228:231], v2, s[4:7], s89 offen offset:2048 sc1\n"
      "s_add_u32 s89, s89, 4096\n"
      "buffer_load_dwordx4 v[232:235], v2, s[4:7], s89 offen offset:0 sc1\n"
      "buffer_load_dwordx4 v[236:239], v2, s[4:7], s89 offen offset:2048 sc1\n"
      "s_mov_b32 s92, 1\n"
      ".Lretry_65_0:\n"
      "v_max_u32 v15, v240, v241\n"
      "v_max3_u32 v15, v15, v242, v243\n"
      "v_max3_u32 v15, v15, v244, v245\n"
      "v_max3_u32 v15, v15, v246, v247\n"
      "v_max3_u32 v15, v15, v248, v249\n"
      "v_max3_u32 v15, v15, v250, v251\n"
      "v_max3_u32 v15, v15, v252, v253\n"
      "v_max3_u32 v15, v15, v254, v255\n"
      "v_cmp_eq_u32 vcc, -1, v15\n"
      "s_cbranch_vccz .Lretry_65_1\n"
      "s_add_u32 s89, s61, 81920\n"
      "buffer_load_dwordx4 v[240:243], v2, s[4:7], s89 offen offset:0 sc1\n"
      "buffer_load_dwordx4 v[244:247], v2, s[4:7], s89 offen offset:2048 sc1\n"
      "s_add_u32 s89, s89, 4096\n"
      "buffer_load_dwordx4 v[248:251], v2, s[4:7], s89 offen offset:0 sc1\n"
      "buffer_load_dwordx4 v[252:255], v2, s[4:7], s89 offen offset:2048 sc1\n"
      "s_mov_b32 s92, 1\n"
      ".Lretry_65_1:\n"
      "s_add_u32 s91, s91, 1\n"
      "s_cmp_gt_u32 s91, 65536\n"
      "s_cselect_b32 s92, 0, s92\n"
      "s_cmp_lg_u32 s92, 0\n"
      "s_cbranch_scc1 .Lretry_65_l\n"
      "s_waitcnt vmcnt(0)\n"
      "s_nop 7\n"
      "s_nop 7\n"
      "s_branch .Lback_66\n"
      ".Lend:\n"
      :
      : "{s[0:1]}"(__builtin_amdgcn_kernarg_segment_ptr()), "{s2}"(blockIdx.x), "{v0}"(threadIdx.x), "{s3}"((unsigned)(size_t)lds)
      : "v1","v2","v3","v4","v5","v6","v7","v8","v9","v10","v11","v12","v13","v14","v15","v16","v17","v18","v19","v20","v21","v22","v23","v24","v25","v26","v27","v28","v29","v30","v31","v32","v33","v34","v35","v36","v37","v38","v39","v40","v41","v42","v43","v44","v45","v46","v47","v48","v49","v50","v51","v52","v53","v54","v55","v56","v57","v58","v59","v60","v61","v62","v63","v64","v65","v66","v67","v68","v69","v70","v71","v72","v73","v74","v75","v76","v77","v78","v79","v80","v81","v82","v83","v84","v85","v86","v87","v88","v89","v90","v91","v92","v93","v94","v95","v96","v97","v98","v99","v100","v101","v102","v103","v104","v105","v106","v107","v108","v109","v110","v111","v112","v113","v114","v115","v116","v117","v118","v119","v120","v121","v122","v123","v124","v125","v126","v127","v128","v129","v130","v131","v132","v133","v134","v135","v136","v137","v138","v139","v140","v141","v142","v143","v144","v145","v146","v147","v148","v149","v150","v151","v152","v153","v154","v155","v156","v157","v158","v159","v160","v161","v162","v163","v164","v165","v166","v167","v168","v169","v170","v171","v172","v173","v174","v175","v176","v177","v178","v179","v180","v181","v182","v183","v184","v185","v186","v187","v188","v189","v190","v191","v192","v193","v194","v195","v196","v197","v198","v199","v200","v201","v202","v203","v204","v205","v206","v207","v208","v209","v210","v211","v212","v213","v214","v215","v216","v217","v218","v219","v220","v221","v222","v223","v224","v225","v226","v227","v228","v229","v230","v231","v232","v233","v234","v235","v236","v237","v238","v239","v240","v241","v242","v243","v244","v245","v246","v247","v248","v249","v250","v251","v252","v253","v254","v255","a0","a1","a2","a3","a4","a5","a6","a7","a8","a9","a10","a11","a12","a13","a14","a15","a16","a17","a18","a19","a20","a21","a22","a23","a24","a25","a26","a27","a28","a29","a30","a31","a32","a33","a34","a35","a36","a37","a38","a39","a40","a41","a42","a43","a44","a45","a46","a47","a48","a49","a50","a51","a52","a53","a54","a55","a56","a57","a58","a59","a60","a61","a62","a63","a64","a65","a66","a67","a68","a69","a70","a71","a72","a73","a74","a75","a76","a77","a78","a79","a80","a81","a82","a83","a84","a85","a86","a87","a88","a89","a90","a91","a92","a93","a94","a95","a96","a97","a98","a99","a100","a101","a102","a103","a104","a105","a106","a107","a108","a109","a110","a111","a112","a113","a114","a115","a116","a117","a118","a119","a120","a121","a122","a123","a124","a125","a126","a127","a128","a129","a130","a131","a132","a133","a134","a135","a136","a137","a138","a139","a140","a141","a142","a143","a144","a145","a146","a147","a148","a149","a150","a151","a152","a153","a154","a155","a156","a157","a158","a159","a160","a161","a162","a163","a164","a165","a166","a167","a168","a169","a170","a171","a172","a173","a174","a175","a176","a177","a178","a179","a180","a181","a182","a183","a184","a185","a186","a187","a188","a189","a190","a191","a192","a193","a194","a195","a196","a197","a198","a199","a200","a201","a202","a203","a204","a205","a206","a207","a208","a209","a210","a211","a212","a213","a214","a215","a216","a217","a218","a219","a220","a221","a222","a223","a224","a225","a226","a227","a228","a229","a230","a231","a232","a233","a234","a235","a236","a237","a238","a239","a240","a241","a242","a243","a244","a245","a246","a247","a248","a249","a250","a251","a252","a253","a254","a255","s4","s5","s6","s7","s8","s9","s10","s11","s12","s13","s14","s15","s16","s17","s18","s19","s20","s21","s22","s23","s24","s25","s26","s27","s28","s29","s30","s31","s34","s35","s36","s37","s38","s39","s40","s41","s42","s43","s44","s45","s46","s47","s48","s49","s50","s51","s52","s53","s54","s55","s56","s57","s58","s59","s60","s61","s62","s63","s64","s65","s66","s67","s68","s69","s70","s71","s72","s73","s74","s75","s76","s77","s78","s79","s80","s81","s82","s83","s84","s85","s86","s87","s88","s89","s90","s91","s92","s93","s94","s95","s96","s97","s98","s99","vcc","scc","memory");
#endif
}

extern "C" void kernel_launch(void* const* d_in, const int* in_sizes, int n_in, void* d_out, int out_size, void* d_ws, size_t ws_size,
                              hipStream_t stream) {
  const unsigned F = 0x20000u;
  fused_block_kernel<<<256, 256, 0, stream>>>(d_ws, 31457280u, F, d_in[7], 1024u, F, d_in[8], 3145728u, F, d_in[0], 2097152u, F,
                                              d_in[1], 1572864u, F, d_in[4], 1409024u, F, d_in[5], 1409024u, F, d_in[2], 524288u, F,
                                              d_in[6], 1409024u, F, d_out, 2097152u, F);
}
```
